```python
import torch

def _softmax(x, axis=-1):
    return torch.softmax(x, dim=axis)
def _gelu(x):
    return torch.nn.functional.gelu(x, approximate="tanh")
def _silu(x):
    return torch.nn.functional.silu(x)
def _sigmoid(x):
    return torch.sigmoid(x)
def _layernorm(x, axis=-1, eps=1e-5):
    m = x.mean(dim=axis, keepdim=True)
    v = x.var(dim=axis, keepdim=True, unbiased=False)
    return (x - m) / torch.sqrt(v + eps)
def _scatter_add(tgt, idx, src, axis=0):
    return tgt.clone().index_add_(axis, idx, src)
def _segment_sum(src, seg, n):
    out = torch.zeros((n,) + src.shape[1:], dtype=src.dtype)
    return out.index_add_(0, seg, src)
def _pad(x, pw):
    flat = tuple(v for pair in reversed(pw) for v in pair)
    return torch.nn.functional.pad(x, flat)

kernel_name = 'concat_heads__mha_block_cached__gqa_block_cached__near_ridge'

def setup_inputs():

    _w = (lambda a: (lambda t: t.to(torch.bfloat16) if t.is_floating_point() else t)(torch.as_tensor(a)))
    def _A_setup():
        B, Sq, D = 8, 240, 512
        H, Sk, Dh = 32, 512, 128
        return {
            "x": _w((lambda *s: torch.randn(*s, dtype=torch.bfloat16) * 0.8)(B, Sq, D)),
            "k_cache": _w((lambda *s: torch.randn(*s, dtype=torch.bfloat16) * 0.8)(B, H, Sk, Dh)),
            "v_cache": _w((lambda *s: torch.randn(*s, dtype=torch.bfloat16) * 0.05)(B, H, Sk, Dh)),
            "wq": _w((lambda *s: torch.randn(*s, dtype=torch.bfloat16) * 0.8)(D, H * Dh) * (D ** -0.5)),
            "wo": _w((lambda *s: torch.randn(*s, dtype=torch.bfloat16) * 0.05)(H * Dh, D) * ((H * Dh) ** -0.5)),
        }
    _A_in = _A_setup()

    def _B_setup():
        B, Sq, D = 8, 240, 512
        Hq, Hkv, Sk, Dh = 32, 32, 512, 128
        assert Hq % Hkv == 0, (Hq, Hkv)
        return {
            "x": _w((lambda *s: torch.randn(*s, dtype=torch.bfloat16) * 0.8)(B, Sq, D)),
            "k_cache": _w((lambda *s: torch.randn(*s, dtype=torch.bfloat16) * 0.8)(B, Hkv, Sk, Dh)),
            "v_cache": _w((lambda *s: torch.randn(*s, dtype=torch.bfloat16) * 0.05)(B, Hkv, Sk, Dh)),
            "wq": _w((lambda *s: torch.randn(*s, dtype=torch.bfloat16) * 0.8)(D, Hq * Dh) * (D ** -0.5)),
            "wo": _w((lambda *s: torch.randn(*s, dtype=torch.bfloat16) * 0.05)(Hq * Dh, D) * ((Hq * Dh) ** -0.5)),
        }
    _B_in = _B_setup()

    return {
        "A_x": _A_in["x"],
        "A_k_cache": _A_in["k_cache"],
        "A_v_cache": _A_in["v_cache"],
        "A_wq": _A_in["wq"],
        "A_wo": _A_in["wo"],
        "B_x": _B_in["x"],
        "B_k_cache": _B_in["k_cache"],
        "B_v_cache": _B_in["v_cache"],
        "B_wq": _B_in["wq"],
        "B_wo": _B_in["wo"],
    }

def reference(A_x, A_k_cache, A_v_cache, A_wq, A_wo, B_x, B_k_cache, B_v_cache, B_wq, B_wo):
    def _A_ref(x, k_cache, v_cache, wq, wo):
        B, Sq, D = x.shape
        H, Dh = 32, 128
        q = (lambda x, *d: x.permute(*d))((x @ wq).reshape(B, Sq, H, Dh), 0, 2, 1, 3)
        scores = torch.einsum("bhqd,bhkd->bhqk", q, k_cache) * (Dh ** -0.5)
        p = _softmax(scores, axis=-1)
        o = torch.einsum("bhqk,bhkd->bhqd", p, v_cache)
        return (lambda x, *d: x.permute(*d))(o, 0, 2, 1, 3).reshape(B, Sq, H * Dh) @ wo

    def _B_ref(x, k_cache, v_cache, wq, wo):
        B, Sq, D = x.shape
        Hq, Dh = 32, 128
        Hkv = k_cache.shape[1]
        rep = Hq // Hkv
        q = (lambda x, *d: x.permute(*d))((x @ wq).reshape(B, Sq, Hq, Dh), 0, 2, 1, 3)
        k = (lambda x, n, axis: torch.repeat_interleave(x, n, dim=axis))(k_cache, rep, axis=1)
        v = (lambda x, n, axis: torch.repeat_interleave(x, n, dim=axis))(v_cache, rep, axis=1)
        scores = torch.einsum("bhqd,bhkd->bhqk", q, k) * (Dh ** -0.5)
        p = _softmax(scores, axis=-1)
        o = torch.einsum("bhqk,bhkd->bhqd", p, v)
        return (lambda x, *d: x.permute(*d))(o, 0, 2, 1, 3).reshape(B, Sq, Hq * Dh) @ wo

    a = _A_ref(A_x, A_k_cache, A_v_cache, A_wq, A_wo)
    b = _B_ref(B_x, B_k_cache, B_v_cache, B_wq, B_wo)
    return torch.concatenate([a, b], axis=-1)
```

```cpp
#include <hip/hip_runtime.h>
#include <hip/hip_bf16.h>
#include <stdint.h>

#define STAMP 0
typedef float    f32x2  __attribute__((ext_vector_type(2)));
typedef __bf16   bf2    __attribute__((ext_vector_type(2)));
typedef unsigned u32x4  __attribute__((ext_vector_type(4)));
#define DI __device__ __forceinline__
DI unsigned cvtpk(float lo, float hi) { f32x2 v = {lo, hi}; bf2 b = __builtin_convertvector(v, bf2); return __builtin_bit_cast(unsigned, b); }

struct Ptrs { const char* x[2]; const char* k[2]; const char* v[2]; const char* wq[2]; const char* wo[2]; };

__global__ __launch_bounds__(256, 1) void fused_attn(Ptrs P, char* part) {
  const int w = __builtin_amdgcn_readfirstlane(threadIdx.x >> 6);
  const int bid = blockIdx.x, xcd = bid & 7, idx = bid >> 3;
  const int br = xcd >> 2, hp = (xcd & 3) * 4 + (idx & 3), b = idx >> 2;
  const char* X = P.x[br] + (size_t)b * 240 * 1024;
  const char* WQ = P.wq[br] + hp * 512;
  const size_t hoff = ((size_t)b * 32 + 2 * hp) * 131072;
  const char* K = P.k[br] + hoff;
  const char* V = P.v[br] + hoff - 896;
  const char* WO = P.wo[br] + (size_t)(hp & ~1) * 262144;
  const int wg = (br * 8 + b) * 16 + hp;
  char* PW = part + (size_t)((((br * 8 + b) * 2 + (hp & 1)) * 8) + (hp >> 1)) * 131072;
  char* XB = part + (size_t)64 * 1024 * 1024;
  char* FL = part + (size_t)96 * 1024 * 1024;
  char* ST = part + (size_t)128 * 1024 * 1024 + (size_t)(bid * 4 + w) * 128;
  asm volatile(R"ASM(
s_nop 4
s_mov_b64 s[36:37], %0
s_and_b32 s37, s37, 0xffff
s_mov_b32 s38, 245760
s_mov_b32 s39, 0x00020000
s_mov_b64 s[40:41], %1
s_and_b32 s41, s41, 0xffff
s_mov_b32 s42, 4186624
s_mov_b32 s43, 0x00020000
s_mov_b32 s60, %6
s_lshl_b32 s63, s60, 13
s_lshl_b32 s62, s60, 16
s_lshl_b32 s74, s60, 17
v_mbcnt_lo_u32_b32 v160, -1, 0
v_mbcnt_hi_u32_b32 v160, -1, v160
v_and_b32 v161, 31, v160
v_lshrrev_b32 v162, 5, v160
v_lshrrev_b32 v163, 4, v160
v_and_b32 v164, 7, v160
v_xor_b32 v163, v163, v164
v_lshlrev_b32 v163, 4, v163
v_lshrrev_b32 v164, 3, v160
v_lshl_add_u32 v117, v164, 10, v163
v_lshrrev_b32 v163, 4, v160
v_or_b32 v163, 4, v163
v_and_b32 v164, 7, v160
v_xor_b32 v163, v163, v164
v_lshlrev_b32 v163, 4, v163
v_lshrrev_b32 v164, 3, v160
v_lshl_add_u32 v118, v164, 10, v163
v_and_b32 v163, 3, v160
v_lshlrev_b32 v163, 4, v163
v_lshl_add_u32 v165, v162, 6, v163
v_bfe_u32 v166, v160, 2, 3
v_lshl_add_u32 v119, v166, 13, v165
s_add_u32 m0, s63, 65536
s_add_u32 s64, s62, 0
buffer_load_dwordx4 v117, s[36:39], s64 offen offset:0 lds
s_add_u32 m0, s63, 66560
s_add_u32 s64, s62, 8192
buffer_load_dwordx4 v118, s[36:39], s64 offen offset:0 lds
s_add_u32 m0, s63, 67584
s_add_u32 s64, s62, 16384
buffer_load_dwordx4 v117, s[36:39], s64 offen offset:0 lds
s_add_u32 m0, s63, 68608
s_add_u32 s64, s62, 24576
buffer_load_dwordx4 v118, s[36:39], s64 offen offset:0 lds
s_add_u32 m0, s63, 69632
s_add_u32 s64, s62, 32768
buffer_load_dwordx4 v117, s[36:39], s64 offen offset:0 lds
s_add_u32 m0, s63, 70656
s_add_u32 s64, s62, 40960
buffer_load_dwordx4 v118, s[36:39], s64 offen offset:0 lds
s_add_u32 m0, s63, 71680
s_add_u32 s64, s62, 49152
buffer_load_dwordx4 v117, s[36:39], s64 offen offset:0 lds
s_add_u32 m0, s63, 72704
s_add_u32 s64, s62, 57344
buffer_load_dwordx4 v118, s[36:39], s64 offen offset:0 lds
s_add_u32 m0, s63, 32768
s_add_u32 s65, s74, 0
buffer_load_dwordx4 v119, s[40:43], s65 offen offset:0 lds
s_add_u32 m0, s63, 33792
s_add_u32 s65, s74, 128
buffer_load_dwordx4 v119, s[40:43], s65 offen offset:0 lds
s_add_u32 m0, s63, 34816
s_add_u32 s65, s74, 256
buffer_load_dwordx4 v119, s[40:43], s65 offen offset:0 lds
s_add_u32 m0, s63, 35840
s_add_u32 s65, s74, 384
buffer_load_dwordx4 v119, s[40:43], s65 offen offset:0 lds
s_add_u32 m0, s63, 36864
s_add_u32 s65, s74, 65536
buffer_load_dwordx4 v119, s[40:43], s65 offen offset:0 lds
s_add_u32 m0, s63, 37888
s_add_u32 s65, s74, 65664
buffer_load_dwordx4 v119, s[40:43], s65 offen offset:0 lds
s_add_u32 m0, s63, 38912
s_add_u32 s65, s74, 65792
buffer_load_dwordx4 v119, s[40:43], s65 offen offset:0 lds
s_add_u32 m0, s63, 39936
s_add_u32 s65, s74, 65920
buffer_load_dwordx4 v119, s[40:43], s65 offen offset:0 lds
v_lshlrev_b32 v167, 7, v160
s_getpc_b64 s[84:85]
L_pf:
s_and_b32 s85, s85, 0xffff
s_mov_b32 s86, L_cend-L_pf
s_mov_b32 s87, 0x00020000
s_mul_i32 s64, s60, 24576
buffer_load_dword a127, v167, s[84:87], s64 offen
s_add_u32 s64, s64, 8192
buffer_load_dword a127, v167, s[84:87], s64 offen
s_add_u32 s64, s64, 8192
buffer_load_dword a127, v167, s[84:87], s64 offen
s_add_u32 m0, s63, 131072
s_add_u32 s64, s62, 128
buffer_load_dwordx4 v117, s[36:39], s64 offen offset:0 lds
s_add_u32 m0, s63, 132096
s_add_u32 s64, s62, 8320
buffer_load_dwordx4 v118, s[36:39], s64 offen offset:0 lds
s_add_u32 m0, s63, 133120
s_add_u32 s64, s62, 16512
buffer_load_dwordx4 v117, s[36:39], s64 offen offset:0 lds
s_add_u32 m0, s63, 134144
s_add_u32 s64, s62, 24704
buffer_load_dwordx4 v118, s[36:39], s64 offen offset:0 lds
s_add_u32 m0, s63, 135168
s_add_u32 s64, s62, 32896
buffer_load_dwordx4 v117, s[36:39], s64 offen offset:0 lds
s_add_u32 m0, s63, 136192
s_add_u32 s64, s62, 41088
buffer_load_dwordx4 v118, s[36:39], s64 offen offset:0 lds
s_add_u32 m0, s63, 137216
s_add_u32 s64, s62, 49280
buffer_load_dwordx4 v117, s[36:39], s64 offen offset:0 lds
s_add_u32 m0, s63, 138240
s_add_u32 s64, s62, 57472
buffer_load_dwordx4 v118, s[36:39], s64 offen offset:0 lds
s_add_u32 m0, s63, 98304
s_add_u32 s65, s74, 524288
buffer_load_dwordx4 v119, s[40:43], s65 offen offset:0 lds
s_add_u32 m0, s63, 99328
s_add_u32 s65, s74, 524416
buffer_load_dwordx4 v119, s[40:43], s65 offen offset:0 lds
s_add_u32 m0, s63, 100352
s_add_u32 s65, s74, 524544
buffer_load_dwordx4 v119, s[40:43], s65 offen offset:0 lds
s_add_u32 m0, s63, 101376
s_add_u32 s65, s74, 524672
buffer_load_dwordx4 v119, s[40:43], s65 offen offset:0 lds
s_add_u32 m0, s63, 102400
s_add_u32 s65, s74, 589824
buffer_load_dwordx4 v119, s[40:43], s65 offen offset:0 lds
s_add_u32 m0, s63, 103424
s_add_u32 s65, s74, 589952
buffer_load_dwordx4 v119, s[40:43], s65 offen offset:0 lds
s_add_u32 m0, s63, 104448
s_add_u32 s65, s74, 590080
buffer_load_dwordx4 v119, s[40:43], s65 offen offset:0 lds
s_add_u32 m0, s63, 105472
s_add_u32 s65, s74, 590208
buffer_load_dwordx4 v119, s[40:43], s65 offen offset:0 lds
s_mov_b64 s[76:77], %8
s_and_b32 s77, s77, 0xffff
s_mov_b32 s78, 524288
s_mov_b32 s79, 0x00020000
s_and_b32 s64, %10, 1
s_and_b32 s65, s60, 1
s_lshr_b32 s66, s60, 1
s_xor_b32 s90, s66, s64
s_xor_b32 s90, s90, 1
s_lshl_b32 s93, %10, 10
s_lshl_b32 s67, s65, 7
s_add_u32 s93, s93, s67
s_xor_b32 s94, s93, 1024
buffer_load_dwordx4 v[108:111], off, s[76:79], s94 offset:0 sc1
s_lshl_b32 s91, %10, 16
s_lshl_b32 s67, s65, 15
s_add_u32 s91, s91, s67
s_xor_b32 s92, s91, 65536
s_lshl_b32 s88, s64, 18
s_lshl_b32 s67, s65, 16
s_add_u32 s88, s88, s67
s_lshl_b32 s67, s66, 9
s_add_u32 s88, s88, s67
s_xor_b32 s89, s88, 262144
s_lshl_b32 s97, s60, 15
s_lshl_b32 s66, s66, 14
s_mov_b64 s[84:85], %9
s_and_b32 s85, s85, 0xffff
s_mov_b32 s86, 16777216
s_mov_b32 s87, 0x00020000
s_mov_b64 s[44:45], %2
s_and_b32 s45, s45, 0xffff
s_mov_b32 s46, 262144
s_mov_b32 s47, 0x00020000
s_mov_b64 s[48:49], %3
s_and_b32 s49, s49, 0xffff
s_mov_b32 s50, 263040
s_mov_b32 s51, 0x00020000
s_mov_b64 s[52:53], %4
s_and_b32 s53, s53, 0xffff
s_mov_b32 s54, 524288
s_mov_b32 s55, 0x00020000
s_mov_b64 s[56:57], %5
s_and_b32 s57, s57, 0xffff
s_mov_b32 s58, 131072
s_mov_b32 s59, 0x00020000
s_lshl_b32 s61, s60, 12
s_mov_b32 s68, 0x3e0293ee
v_lshl_add_u32 v126, v166, 10, v165
v_lshl_add_u32 v125, v166, 8, v165
v_add_u32 v124, 896, v125
v_lshrrev_b32 v163, 4, v160
v_and_b32 v164, 15, v160
v_xor_b32 v163, v163, v164
v_lshlrev_b32 v163, 4, v163
v_lshrrev_b32 v164, 4, v160
v_lshl_add_u32 v120, v164, 8, v163
v_lshrrev_b32 v163, 4, v160
v_or_b32 v163, 4, v163
v_and_b32 v164, 15, v160
v_xor_b32 v163, v163, v164
v_lshlrev_b32 v163, 4, v163
v_lshrrev_b32 v164, 4, v160
v_lshl_add_u32 v121, v164, 8, v163
v_lshrrev_b32 v163, 4, v160
v_or_b32 v163, 8, v163
v_and_b32 v164, 15, v160
v_xor_b32 v163, v163, v164
v_lshlrev_b32 v163, 4, v163
v_lshrrev_b32 v164, 4, v160
v_lshl_add_u32 v122, v164, 8, v163
v_lshrrev_b32 v163, 4, v160
v_or_b32 v163, 12, v163
v_and_b32 v164, 15, v160
v_xor_b32 v163, v163, v164
v_lshlrev_b32 v163, 4, v163
v_lshrrev_b32 v164, 4, v160
v_lshl_add_u32 v123, v164, 8, v163
v_lshlrev_b32 v127, 4, v160
v_bfe_u32 v163, v161, 1, 3
v_or_b32 v164, 0, v162
v_xor_b32 v163, v163, v164
v_lshlrev_b32 v163, 4, v163
v_lshl_add_u32 v163, v161, 7, v163
v_add_u32 v163, s63, v163
v_add_u32 v112, 65536, v163
v_bfe_u32 v163, v161, 1, 3
v_or_b32 v164, 2, v162
v_xor_b32 v163, v163, v164
v_lshlrev_b32 v163, 4, v163
v_lshl_add_u32 v163, v161, 7, v163
v_add_u32 v163, s63, v163
v_add_u32 v113, 65536, v163
v_bfe_u32 v163, v161, 1, 3
v_or_b32 v164, 4, v162
v_xor_b32 v163, v163, v164
v_lshlrev_b32 v163, 4, v163
v_lshl_add_u32 v163, v161, 7, v163
v_add_u32 v163, s63, v163
v_add_u32 v114, 65536, v163
v_bfe_u32 v163, v161, 1, 3
v_or_b32 v164, 6, v162
v_xor_b32 v163, v163, v164
v_lshlrev_b32 v163, 4, v163
v_lshl_add_u32 v163, v161, 7, v163
v_add_u32 v163, s63, v163
v_add_u32 v115, 65536, v163
v_and_b32 v163, 1, v160
v_lshlrev_b32 v163, 4, v163
v_bfe_u32 v164, v160, 1, 1
v_lshl_add_u32 v163, v164, 3, v163
v_bfe_u32 v164, v160, 2, 2
v_lshl_add_u32 v163, v164, 6, v163
v_bfe_u32 v164, v160, 4, 1
v_lshl_add_u32 v163, v164, 5, v163
v_lshl_add_u32 v164, v162, 12, v163
v_add_u32 v116, 32768, v164
v_lshl_add_u32 v163, v162, 8, v163
v_add_u32 v163, s66, v163
v_add_u32 v105, 32768, v163
v_and_b32 v163, 15, v161
v_or_b32 v164, 0, v162
v_xor_b32 v163, v163, v164
v_lshlrev_b32 v163, 4, v163
v_lshl_add_u32 v96, v161, 8, v163
v_and_b32 v163, 15, v161
v_or_b32 v164, 2, v162
v_xor_b32 v163, v163, v164
v_lshlrev_b32 v163, 4, v163
v_lshl_add_u32 v97, v161, 8, v163
v_and_b32 v163, 15, v161
v_or_b32 v164, 4, v162
v_xor_b32 v163, v163, v164
v_lshlrev_b32 v163, 4, v163
v_lshl_add_u32 v98, v161, 8, v163
v_and_b32 v163, 15, v161
v_or_b32 v164, 6, v162
v_xor_b32 v163, v163, v164
v_lshlrev_b32 v163, 4, v163
v_lshl_add_u32 v99, v161, 8, v163
v_and_b32 v163, 15, v161
v_or_b32 v164, 8, v162
v_xor_b32 v163, v163, v164
v_lshlrev_b32 v163, 4, v163
v_lshl_add_u32 v100, v161, 8, v163
v_and_b32 v163, 15, v161
v_or_b32 v164, 10, v162
v_xor_b32 v163, v163, v164
v_lshlrev_b32 v163, 4, v163
v_lshl_add_u32 v101, v161, 8, v163
v_and_b32 v163, 15, v161
v_or_b32 v164, 12, v162
v_xor_b32 v163, v163, v164
v_lshlrev_b32 v163, 4, v163
v_lshl_add_u32 v102, v161, 8, v163
v_and_b32 v163, 15, v161
v_or_b32 v164, 14, v162
v_xor_b32 v163, v163, v164
v_lshlrev_b32 v163, 4, v163
v_lshl_add_u32 v103, v161, 8, v163
v_and_b32 v163, 3, v160
v_lshlrev_b32 v163, 3, v163
v_bfe_u32 v164, v160, 2, 2
v_lshl_add_u32 v163, v164, 6, v163
v_bfe_u32 v164, v160, 4, 1
v_lshl_add_u32 v163, v164, 5, v163
v_lshl_add_u32 v163, v162, 8, v163
v_add_u32 v104, 16384, v163
v_mov_b32 v106, 0
v_mov_b32 v107, 0
s_waitcnt vmcnt(20)
s_barrier
ds_read_b128 a[0:3], v112 offset:0
ds_read_b128 a[4:7], v112 offset:4096
ds_read_b64_tr_b16 v[64:65], v116 offset:0
ds_read_b64_tr_b16 v[66:67], v116 offset:256
ds_read_b64_tr_b16 v[68:69], v116 offset:512
ds_read_b64_tr_b16 v[70:71], v116 offset:768
s_waitcnt lgkmcnt(2)
v_mfma_f32_32x32x16_bf16 v[0:15], v[64:67], a[0:3], 0
v_xor_b32 v112, 0x30000, v112
ds_read_b64_tr_b16 v[72:73], v116 offset:1024
ds_read_b64_tr_b16 v[74:75], v116 offset:1280
ds_read_b64_tr_b16 v[76:77], v116 offset:1536
ds_read_b64_tr_b16 v[78:79], v116 offset:1792
v_mfma_f32_32x32x16_bf16 v[16:31], v[64:67], a[4:7], 0
ds_read_b64_tr_b16 v[80:81], v116 offset:2048
ds_read_b64_tr_b16 v[82:83], v116 offset:2304
s_waitcnt lgkmcnt(6)
v_mfma_f32_32x32x16_bf16 v[32:47], v[68:71], a[0:3], 0
s_add_u32 m0, s63, 0
s_add_u32 s65, s74, 1048576
buffer_load_dwordx4 v119, s[40:43], s65 offen offset:0 lds
ds_read_b64_tr_b16 v[84:85], v116 offset:2560
ds_read_b64_tr_b16 v[86:87], v116 offset:2816
v_mfma_f32_32x32x16_bf16 v[48:63], v[68:71], a[4:7], 0
s_waitcnt lgkmcnt(6)
v_mfma_f32_32x32x16_bf16 v[128:143], v[72:75], a[0:3], 0
ds_read_b64_tr_b16 v[88:89], v116 offset:3072
ds_read_b64_tr_b16 v[90:91], v116 offset:3328
v_mfma_f32_32x32x16_bf16 v[144:159], v[72:75], a[4:7], 0
s_waitcnt lgkmcnt(6)
v_mfma_f32_32x32x16_bf16 v[160:175], v[76:79], a[0:3], 0
s_add_u32 m0, s63, 1024
s_add_u32 s65, s74, 1048704
buffer_load_dwordx4 v119, s[40:43], s65 offen offset:0 lds
ds_read_b64_tr_b16 v[92:93], v116 offset:3584
ds_read_b64_tr_b16 v[94:95], v116 offset:3840
v_mfma_f32_32x32x16_bf16 v[176:191], v[76:79], a[4:7], 0
s_waitcnt lgkmcnt(6)
v_mfma_f32_32x32x16_bf16 v[192:207], v[80:83], a[0:3], 0
ds_read_b64_tr_b16 v[64:65], v116 offset:8192
ds_read_b64_tr_b16 v[66:67], v116 offset:8448
v_mfma_f32_32x32x16_bf16 v[208:223], v[80:83], a[4:7], 0
s_waitcnt lgkmcnt(6)
v_mfma_f32_32x32x16_bf16 v[224:239], v[84:87], a[0:3], 0
s_add_u32 m0, s63, 2048
s_add_u32 s65, s74, 1048832
buffer_load_dwordx4 v119, s[40:43], s65 offen offset:0 lds
ds_read_b128 a[8:11], v113 offset:0
ds_read_b128 a[12:15], v113 offset:4096
v_xor_b32 v113, 0x30000, v113
v_mfma_f32_32x32x16_bf16 v[240:255], v[84:87], a[4:7], 0
ds_read_b64_tr_b16 v[68:69], v116 offset:8704
ds_read_b64_tr_b16 v[70:71], v116 offset:8960
s_waitcnt lgkmcnt(8)
v_mfma_f32_32x32x16_bf16 a[192:207], v[88:91], a[0:3], 0
ds_read_b64_tr_b16 v[72:73], v116 offset:9216
ds_read_b64_tr_b16 v[74:75], v116 offset:9472
v_mfma_f32_32x32x16_bf16 a[208:223], v[88:91], a[4:7], 0
s_waitcnt lgkmcnt(8)
v_mfma_f32_32x32x16_bf16 a[224:239], v[92:95], a[0:3], 0
s_add_u32 m0, s63, 3072
s_add_u32 s65, s74, 1048960
buffer_load_dwordx4 v119, s[40:43], s65 offen offset:0 lds
ds_read_b64_tr_b16 v[76:77], v116 offset:9728
ds_read_b64_tr_b16 v[78:79], v116 offset:9984
v_mfma_f32_32x32x16_bf16 a[240:255], v[92:95], a[4:7], 0
s_waitcnt lgkmcnt(7)
v_mfma_f32_32x32x16_bf16 v[0:15], v[64:67], a[8:11], v[0:15]
ds_read_b64_tr_b16 v[80:81], v116 offset:10240
ds_read_b64_tr_b16 v[82:83], v116 offset:10496
s_waitcnt lgkmcnt(8)
v_mfma_f32_32x32x16_bf16 v[16:31], v[64:67], a[12:15], v[16:31]
s_waitcnt lgkmcnt(6)
v_mfma_f32_32x32x16_bf16 v[32:47], v[68:71], a[8:11], v[32:47]
s_add_u32 m0, s63, 4096
s_add_u32 s65, s74, 1114112
buffer_load_dwordx4 v119, s[40:43], s65 offen offset:0 lds
ds_read_b64_tr_b16 v[84:85], v116 offset:10752
ds_read_b64_tr_b16 v[86:87], v116 offset:11008
v_mfma_f32_32x32x16_bf16 v[48:63], v[68:71], a[12:15], v[48:63]
s_waitcnt lgkmcnt(6)
v_mfma_f32_32x32x16_bf16 v[128:143], v[72:75], a[8:11], v[128:143]
ds_read_b64_tr_b16 v[88:89], v116 offset:11264
ds_read_b64_tr_b16 v[90:91], v116 offset:11520
v_mfma_f32_32x32x16_bf16 v[144:159], v[72:75], a[12:15], v[144:159]
s_waitcnt lgkmcnt(6)
v_mfma_f32_32x32x16_bf16 v[160:175], v[76:79], a[8:11], v[160:175]
s_add_u32 m0, s63, 5120
s_add_u32 s65, s74, 1114240
buffer_load_dwordx4 v119, s[40:43], s65 offen offset:0 lds
ds_read_b64_tr_b16 v[92:93], v116 offset:11776
ds_read_b64_tr_b16 v[94:95], v116 offset:12032
v_mfma_f32_32x32x16_bf16 v[176:191], v[76:79], a[12:15], v[176:191]
s_waitcnt lgkmcnt(6)
v_mfma_f32_32x32x16_bf16 v[192:207], v[80:83], a[8:11], v[192:207]
ds_read_b64_tr_b16 v[64:65], v116 offset:16384
ds_read_b64_tr_b16 v[66:67], v116 offset:16640
v_mfma_f32_32x32x16_bf16 v[208:223], v[80:83], a[12:15], v[208:223]
s_waitcnt lgkmcnt(6)
v_mfma_f32_32x32x16_bf16 v[224:239], v[84:87], a[8:11], v[224:239]
s_add_u32 m0, s63, 6144
s_add_u32 s65, s74, 1114368
buffer_load_dwordx4 v119, s[40:43], s65 offen offset:0 lds
ds_read_b128 a[16:19], v114 offset:0
ds_read_b128 a[20:23], v114 offset:4096
v_xor_b32 v114, 0x30000, v114
v_mfma_f32_32x32x16_bf16 v[240:255], v[84:87], a[12:15], v[240:255]
ds_read_b64_tr_b16 v[68:69], v116 offset:16896
ds_read_b64_tr_b16 v[70:71], v116 offset:17152
s_waitcnt lgkmcnt(8)
v_mfma_f32_32x32x16_bf16 a[192:207], v[88:91], a[8:11], a[192:207]
ds_read_b64_tr_b16 v[72:73], v116 offset:17408
ds_read_b64_tr_b16 v[74:75], v116 offset:17664
v_mfma_f32_32x32x16_bf16 a[208:223], v[88:91], a[12:15], a[208:223]
s_waitcnt lgkmcnt(8)
v_mfma_f32_32x32x16_bf16 a[224:239], v[92:95], a[8:11], a[224:239]
s_add_u32 m0, s63, 7168
s_add_u32 s65, s74, 1114496
buffer_load_dwordx4 v119, s[40:43], s65 offen offset:0 lds
ds_read_b64_tr_b16 v[76:77], v116 offset:17920
ds_read_b64_tr_b16 v[78:79], v116 offset:18176
v_mfma_f32_32x32x16_bf16 a[240:255], v[92:95], a[12:15], a[240:255]
s_waitcnt lgkmcnt(7)
v_mfma_f32_32x32x16_bf16 v[0:15], v[64:67], a[16:19], v[0:15]
ds_read_b64_tr_b16 v[80:81], v116 offset:18432
ds_read_b64_tr_b16 v[82:83], v116 offset:18688
s_waitcnt lgkmcnt(8)
v_mfma_f32_32x32x16_bf16 v[16:31], v[64:67], a[20:23], v[16:31]
s_waitcnt lgkmcnt(6)
v_mfma_f32_32x32x16_bf16 v[32:47], v[68:71], a[16:19], v[32:47]
ds_read_b64_tr_b16 v[84:85], v116 offset:18944
ds_read_b64_tr_b16 v[86:87], v116 offset:19200
v_mfma_f32_32x32x16_bf16 v[48:63], v[68:71], a[20:23], v[48:63]
s_waitcnt lgkmcnt(6)
v_mfma_f32_32x32x16_bf16 v[128:143], v[72:75], a[16:19], v[128:143]
ds_read_b64_tr_b16 v[88:89], v116 offset:19456
ds_read_b64_tr_b16 v[90:91], v116 offset:19712
v_mfma_f32_32x32x16_bf16 v[144:159], v[72:75], a[20:23], v[144:159]
s_waitcnt lgkmcnt(6)
v_mfma_f32_32x32x16_bf16 v[160:175], v[76:79], a[16:19], v[160:175]
ds_read_b64_tr_b16 v[92:93], v116 offset:19968
ds_read_b64_tr_b16 v[94:95], v116 offset:20224
v_mfma_f32_32x32x16_bf16 v[176:191], v[76:79], a[20:23], v[176:191]
s_waitcnt lgkmcnt(6)
v_mfma_f32_32x32x16_bf16 v[192:207], v[80:83], a[16:19], v[192:207]
ds_read_b64_tr_b16 v[64:65], v116 offset:24576
ds_read_b64_tr_b16 v[66:67], v116 offset:24832
v_mfma_f32_32x32x16_bf16 v[208:223], v[80:83], a[20:23], v[208:223]
s_waitcnt lgkmcnt(6)
v_mfma_f32_32x32x16_bf16 v[224:239], v[84:87], a[16:19], v[224:239]
ds_read_b128 a[24:27], v115 offset:0
ds_read_b128 a[28:31], v115 offset:4096
v_xor_b32 v115, 0x30000, v115
ds_read_b64_tr_b16 v[68:69], v116 offset:25088
ds_read_b64_tr_b16 v[70:71], v116 offset:25344
v_mfma_f32_32x32x16_bf16 v[240:255], v[84:87], a[20:23], v[240:255]
s_waitcnt lgkmcnt(8)
v_mfma_f32_32x32x16_bf16 a[192:207], v[88:91], a[16:19], a[192:207]
ds_read_b64_tr_b16 v[72:73], v116 offset:25600
ds_read_b64_tr_b16 v[74:75], v116 offset:25856
v_mfma_f32_32x32x16_bf16 a[208:223], v[88:91], a[20:23], a[208:223]
s_waitcnt lgkmcnt(8)
v_mfma_f32_32x32x16_bf16 a[224:239], v[92:95], a[16:19], a[224:239]
ds_read_b64_tr_b16 v[76:77], v116 offset:26112
ds_read_b64_tr_b16 v[78:79], v116 offset:26368
v_mfma_f32_32x32x16_bf16 a[240:255], v[92:95], a[20:23], a[240:255]
s_waitcnt lgkmcnt(7)
v_mfma_f32_32x32x16_bf16 v[0:15], v[64:67], a[24:27], v[0:15]
ds_read_b64_tr_b16 v[80:81], v116 offset:26624
ds_read_b64_tr_b16 v[82:83], v116 offset:26880
s_waitcnt lgkmcnt(8)
v_mfma_f32_32x32x16_bf16 v[16:31], v[64:67], a[28:31], v[16:31]
s_add_u32 m0, s63, 65536
s_add_u32 s64, s62, 256
buffer_load_dwordx4 v117, s[36:39], s64 offen offset:0 lds
s_waitcnt lgkmcnt(6)
v_mfma_f32_32x32x16_bf16 v[32:47], v[68:71], a[24:27], v[32:47]
s_add_u32 m0, s63, 66560
s_add_u32 s64, s62, 8448
buffer_load_dwordx4 v118, s[36:39], s64 offen offset:0 lds
ds_read_b64_tr_b16 v[84:85], v116 offset:27136
ds_read_b64_tr_b16 v[86:87], v116 offset:27392
v_mfma_f32_32x32x16_bf16 v[48:63], v[68:71], a[28:31], v[48:63]
s_add_u32 m0, s63, 67584
s_add_u32 s64, s62, 16640
buffer_load_dwordx4 v117, s[36:39], s64 offen offset:0 lds
s_waitcnt lgkmcnt(6)
v_mfma_f32_32x32x16_bf16 v[128:143], v[72:75], a[24:27], v[128:143]
s_add_u32 m0, s63, 68608
s_add_u32 s64, s62, 24832
buffer_load_dwordx4 v118, s[36:39], s64 offen offset:0 lds
ds_read_b64_tr_b16 v[88:89], v116 offset:27648
ds_read_b64_tr_b16 v[90:91], v116 offset:27904
v_mfma_f32_32x32x16_bf16 v[144:159], v[72:75], a[28:31], v[144:159]
s_add_u32 m0, s63, 69632
s_add_u32 s64, s62, 33024
buffer_load_dwordx4 v117, s[36:39], s64 offen offset:0 lds
s_waitcnt lgkmcnt(6)
v_mfma_f32_32x32x16_bf16 v[160:175], v[76:79], a[24:27], v[160:175]
s_add_u32 m0, s63, 70656
s_add_u32 s64, s62, 41216
buffer_load_dwordx4 v118, s[36:39], s64 offen offset:0 lds
ds_read_b64_tr_b16 v[92:93], v116 offset:28160
ds_read_b64_tr_b16 v[94:95], v116 offset:28416
v_add_u32 v116, 0x10000, v116
v_mfma_f32_32x32x16_bf16 v[176:191], v[76:79], a[28:31], v[176:191]
s_add_u32 m0, s63, 71680
s_add_u32 s64, s62, 49408
buffer_load_dwordx4 v117, s[36:39], s64 offen offset:0 lds
s_waitcnt lgkmcnt(6)
v_mfma_f32_32x32x16_bf16 v[192:207], v[80:83], a[24:27], v[192:207]
s_add_u32 m0, s63, 72704
s_add_u32 s64, s62, 57600
buffer_load_dwordx4 v118, s[36:39], s64 offen offset:0 lds
v_mfma_f32_32x32x16_bf16 v[208:223], v[80:83], a[28:31], v[208:223]
s_waitcnt vmcnt(25)
s_waitcnt vmcnt(17)
s_waitcnt lgkmcnt(0)
s_barrier
v_mfma_f32_32x32x16_bf16 v[224:239], v[84:87], a[24:27], v[224:239]
ds_read_b128 a[0:3], v112 offset:0
ds_read_b128 a[4:7], v112 offset:4096
v_xor_b32 v112, 0x30000, v112
ds_read_b64_tr_b16 v[64:65], v116 offset:0
ds_read_b64_tr_b16 v[66:67], v116 offset:256
ds_read_b64_tr_b16 v[68:69], v116 offset:512
v_mfma_f32_32x32x16_bf16 v[240:255], v[84:87], a[28:31], v[240:255]
ds_read_b64_tr_b16 v[70:71], v116 offset:768
v_mfma_f32_32x32x16_bf16 a[192:207], v[88:91], a[24:27], a[192:207]
ds_read_b64_tr_b16 v[72:73], v116 offset:1024
ds_read_b64_tr_b16 v[74:75], v116 offset:1280
v_mfma_f32_32x32x16_bf16 a[208:223], v[88:91], a[28:31], a[208:223]
v_mfma_f32_32x32x16_bf16 a[224:239], v[92:95], a[24:27], a[224:239]
ds_read_b64_tr_b16 v[76:77], v116 offset:1536
ds_read_b64_tr_b16 v[78:79], v116 offset:1792
v_mfma_f32_32x32x16_bf16 a[240:255], v[92:95], a[28:31], a[240:255]
s_waitcnt lgkmcnt(6)
v_mfma_f32_32x32x16_bf16 v[0:15], v[64:67], a[0:3], v[0:15]
ds_read_b64_tr_b16 v[80:81], v116 offset:2048
ds_read_b64_tr_b16 v[82:83], v116 offset:2304
v_mfma_f32_32x32x16_bf16 v[16:31], v[64:67], a[4:7], v[16:31]
s_waitcnt lgkmcnt(6)
v_mfma_f32_32x32x16_bf16 v[32:47], v[68:71], a[0:3], v[32:47]
s_add_u32 m0, s63, 32768
s_add_u32 s65, s74, 1572864
buffer_load_dwordx4 v119, s[40:43], s65 offen offset:0 lds
ds_read_b64_tr_b16 v[84:85], v116 offset:2560
ds_read_b64_tr_b16 v[86:87], v116 offset:2816
v_mfma_f32_32x32x16_bf16 v[48:63], v[68:71], a[4:7], v[48:63]
s_waitcnt lgkmcnt(6)
v_mfma_f32_32x32x16_bf16 v[128:143], v[72:75], a[0:3], v[128:143]
ds_read_b64_tr_b16 v[88:89], v116 offset:3072
ds_read_b64_tr_b16 v[90:91], v116 offset:3328
v_mfma_f32_32x32x16_bf16 v[144:159], v[72:75], a[4:7], v[144:159]
s_waitcnt lgkmcnt(6)
v_mfma_f32_32x32x16_bf16 v[160:175], v[76:79], a[0:3], v[160:175]
s_add_u32 m0, s63, 33792
s_add_u32 s65, s74, 1572992
buffer_load_dwordx4 v119, s[40:43], s65 offen offset:0 lds
ds_read_b64_tr_b16 v[92:93], v116 offset:3584
ds_read_b64_tr_b16 v[94:95], v116 offset:3840
v_mfma_f32_32x32x16_bf16 v[176:191], v[76:79], a[4:7], v[176:191]
s_waitcnt lgkmcnt(6)
v_mfma_f32_32x32x16_bf16 v[192:207], v[80:83], a[0:3], v[192:207]
ds_read_b64_tr_b16 v[64:65], v116 offset:8192
ds_read_b64_tr_b16 v[66:67], v116 offset:8448
v_mfma_f32_32x32x16_bf16 v[208:223], v[80:83], a[4:7], v[208:223]
s_waitcnt lgkmcnt(6)
v_mfma_f32_32x32x16_bf16 v[224:239], v[84:87], a[0:3], v[224:239]
s_add_u32 m0, s63, 34816
s_add_u32 s65, s74, 1573120
buffer_load_dwordx4 v119, s[40:43], s65 offen offset:0 lds
ds_read_b128 a[8:11], v113 offset:0
ds_read_b128 a[12:15], v113 offset:4096
v_xor_b32 v113, 0x30000, v113
v_mfma_f32_32x32x16_bf16 v[240:255], v[84:87], a[4:7], v[240:255]
ds_read_b64_tr_b16 v[68:69], v116 offset:8704
ds_read_b64_tr_b16 v[70:71], v116 offset:8960
s_waitcnt lgkmcnt(8)
v_mfma_f32_32x32x16_bf16 a[192:207], v[88:91], a[0:3], a[192:207]
ds_read_b64_tr_b16 v[72:73], v116 offset:9216
ds_read_b64_tr_b16 v[74:75], v116 offset:9472
v_mfma_f32_32x32x16_bf16 a[208:223], v[88:91], a[4:7], a[208:223]
s_waitcnt lgkmcnt(8)
v_mfma_f32_32x32x16_bf16 a[224:239], v[92:95], a[0:3], a[224:239]
s_add_u32 m0, s63, 35840
s_add_u32 s65, s74, 1573248
buffer_load_dwordx4 v119, s[40:43], s65 offen offset:0 lds
ds_read_b64_tr_b16 v[76:77], v116 offset:9728
ds_read_b64_tr_b16 v[78:79], v116 offset:9984
v_mfma_f32_32x32x16_bf16 a[240:255], v[92:95], a[4:7], a[240:255]
s_waitcnt lgkmcnt(7)
v_mfma_f32_32x32x16_bf16 v[0:15], v[64:67], a[8:11], v[0:15]
ds_read_b64_tr_b16 v[80:81], v116 offset:10240
ds_read_b64_tr_b16 v[82:83], v116 offset:10496
s_waitcnt lgkmcnt(8)
v_mfma_f32_32x32x16_bf16 v[16:31], v[64:67], a[12:15], v[16:31]
s_waitcnt lgkmcnt(6)
v_mfma_f32_32x32x16_bf16 v[32:47], v[68:71], a[8:11], v[32:47]
s_add_u32 m0, s63, 36864
s_add_u32 s65, s74, 1638400
buffer_load_dwordx4 v119, s[40:43], s65 offen offset:0 lds
ds_read_b64_tr_b16 v[84:85], v116 offset:10752
ds_read_b64_tr_b16 v[86:87], v116 offset:11008
v_mfma_f32_32x32x16_bf16 v[48:63], v[68:71], a[12:15], v[48:63]
s_waitcnt lgkmcnt(6)
v_mfma_f32_32x32x16_bf16 v[128:143], v[72:75], a[8:11], v[128:143]
ds_read_b64_tr_b16 v[88:89], v116 offset:11264
ds_read_b64_tr_b16 v[90:91], v116 offset:11520
v_mfma_f32_32x32x16_bf16 v[144:159], v[72:75], a[12:15], v[144:159]
s_waitcnt lgkmcnt(6)
v_mfma_f32_32x32x16_bf16 v[160:175], v[76:79], a[8:11], v[160:175]
s_add_u32 m0, s63, 37888
s_add_u32 s65, s74, 1638528
buffer_load_dwordx4 v119, s[40:43], s65 offen offset:0 lds
ds_read_b64_tr_b16 v[92:93], v116 offset:11776
ds_read_b64_tr_b16 v[94:95], v116 offset:12032
v_mfma_f32_32x32x16_bf16 v[176:191], v[76:79], a[12:15], v[176:191]
s_waitcnt lgkmcnt(6)
v_mfma_f32_32x32x16_bf16 v[192:207], v[80:83], a[8:11], v[192:207]
ds_read_b64_tr_b16 v[64:65], v116 offset:16384
ds_read_b64_tr_b16 v[66:67], v116 offset:16640
v_mfma_f32_32x32x16_bf16 v[208:223], v[80:83], a[12:15], v[208:223]
s_waitcnt lgkmcnt(6)
v_mfma_f32_32x32x16_bf16 v[224:239], v[84:87], a[8:11], v[224:239]
s_add_u32 m0, s63, 38912
s_add_u32 s65, s74, 1638656
buffer_load_dwordx4 v119, s[40:43], s65 offen offset:0 lds
ds_read_b128 a[16:19], v114 offset:0
ds_read_b128 a[20:23], v114 offset:4096
v_xor_b32 v114, 0x30000, v114
v_mfma_f32_32x32x16_bf16 v[240:255], v[84:87], a[12:15], v[240:255]
ds_read_b64_tr_b16 v[68:69], v116 offset:16896
ds_read_b64_tr_b16 v[70:71], v116 offset:17152
s_waitcnt lgkmcnt(8)
v_mfma_f32_32x32x16_bf16 a[192:207], v[88:91], a[8:11], a[192:207]
ds_read_b64_tr_b16 v[72:73], v116 offset:17408
ds_read_b64_tr_b16 v[74:75], v116 offset:17664
v_mfma_f32_32x32x16_bf16 a[208:223], v[88:91], a[12:15], a[208:223]
s_waitcnt lgkmcnt(8)
v_mfma_f32_32x32x16_bf16 a[224:239], v[92:95], a[8:11], a[224:239]
s_add_u32 m0, s63, 39936
s_add_u32 s65, s74, 1638784
buffer_load_dwordx4 v119, s[40:43], s65 offen offset:0 lds
ds_read_b64_tr_b16 v[76:77], v116 offset:17920
ds_read_b64_tr_b16 v[78:79], v116 offset:18176
v_mfma_f32_32x32x16_bf16 a[240:255], v[92:95], a[12:15], a[240:255]
s_waitcnt lgkmcnt(7)
v_mfma_f32_32x32x16_bf16 v[0:15], v[64:67], a[16:19], v[0:15]
ds_read_b64_tr_b16 v[80:81], v116 offset:18432
ds_read_b64_tr_b16 v[82:83], v116 offset:18688
s_waitcnt lgkmcnt(8)
v_mfma_f32_32x32x16_bf16 v[16:31], v[64:67], a[20:23], v[16:31]
s_waitcnt lgkmcnt(6)
v_mfma_f32_32x32x16_bf16 v[32:47], v[68:71], a[16:19], v[32:47]
ds_read_b64_tr_b16 v[84:85], v116 offset:18944
ds_read_b64_tr_b16 v[86:87], v116 offset:19200
v_mfma_f32_32x32x16_bf16 v[48:63], v[68:71], a[20:23], v[48:63]
s_waitcnt lgkmcnt(6)
v_mfma_f32_32x32x16_bf16 v[128:143], v[72:75], a[16:19], v[128:143]
ds_read_b64_tr_b16 v[88:89], v116 offset:19456
ds_read_b64_tr_b16 v[90:91], v116 offset:19712
v_mfma_f32_32x32x16_bf16 v[144:159], v[72:75], a[20:23], v[144:159]
s_waitcnt lgkmcnt(6)
v_mfma_f32_32x32x16_bf16 v[160:175], v[76:79], a[16:19], v[160:175]
ds_read_b64_tr_b16 v[92:93], v116 offset:19968
ds_read_b64_tr_b16 v[94:95], v116 offset:20224
v_mfma_f32_32x32x16_bf16 v[176:191], v[76:79], a[20:23], v[176:191]
s_waitcnt lgkmcnt(6)
v_mfma_f32_32x32x16_bf16 v[192:207], v[80:83], a[16:19], v[192:207]
s_waitcnt vmcnt(24)
v_readfirstlane_b32 s95, v108
v_readfirstlane_b32 s96, v109
v_readfirstlane_b32 s69, v110
v_add_u32 v108, 1, v108
v_add_u32 v109, 1, v109
s_cmp_lg_u32 s90, 0
s_cbranch_scc1 L_kcs
buffer_store_dwordx2 v[108:109], off, s[76:79], s93 offset:16 sc1
s_branch L_dcs
L_kcs:
buffer_store_dwordx2 v[108:109], off, s[76:79], s93 offset:80 sc1
L_dcs:
v_mov_b32 v110, 0
v_mov_b32 v111, 0
v_mfma_f32_32x32x16_bf16 v[208:223], v[80:83], a[20:23], v[208:223]
ds_read_b64_tr_b16 v[64:65], v116 offset:24576
ds_read_b64_tr_b16 v[66:67], v116 offset:24832
s_waitcnt lgkmcnt(6)
v_mfma_f32_32x32x16_bf16 v[224:239], v[84:87], a[16:19], v[224:239]
ds_read_b128 a[24:27], v115 offset:0
ds_read_b128 a[28:31], v115 offset:4096
v_xor_b32 v115, 0x30000, v115
ds_read_b64_tr_b16 v[68:69], v116 offset:25088
ds_read_b64_tr_b16 v[70:71], v116 offset:25344
v_mfma_f32_32x32x16_bf16 v[240:255], v[84:87], a[20:23], v[240:255]
s_waitcnt lgkmcnt(8)
v_mfma_f32_32x32x16_bf16 a[192:207], v[88:91], a[16:19], a[192:207]
ds_read_b64_tr_b16 v[72:73], v116 offset:25600
ds_read_b64_tr_b16 v[74:75], v116 offset:25856
v_mfma_f32_32x32x16_bf16 a[208:223], v[88:91], a[20:23], a[208:223]
s_waitcnt lgkmcnt(8)
v_mfma_f32_32x32x16_bf16 a[224:239], v[92:95], a[16:19], a[224:239]
ds_read_b64_tr_b16 v[76:77], v116 offset:26112
ds_read_b64_tr_b16 v[78:79], v116 offset:26368
v_mfma_f32_32x32x16_bf16 a[240:255], v[92:95], a[20:23], a[240:255]
s_waitcnt lgkmcnt(7)
v_mfma_f32_32x32x16_bf16 v[0:15], v[64:67], a[24:27], v[0:15]
ds_read_b64_tr_b16 v[80:81], v116 offset:26624
ds_read_b64_tr_b16 v[82:83], v116 offset:26880
s_waitcnt lgkmcnt(8)
v_mfma_f32_32x32x16_bf16 v[16:31], v[64:67], a[28:31], v[16:31]
s_add_u32 m0, s63, 131072
s_add_u32 s64, s62, 384
buffer_load_dwordx4 v117, s[36:39], s64 offen offset:0 lds
s_waitcnt lgkmcnt(6)
v_mfma_f32_32x32x16_bf16 v[32:47], v[68:71], a[24:27], v[32:47]
s_add_u32 m0, s63, 132096
s_add_u32 s64, s62, 8576
buffer_load_dwordx4 v118, s[36:39], s64 offen offset:0 lds
ds_read_b64_tr_b16 v[84:85], v116 offset:27136
ds_read_b64_tr_b16 v[86:87], v116 offset:27392
v_mfma_f32_32x32x16_bf16 v[48:63], v[68:71], a[28:31], v[48:63]
s_add_u32 m0, s63, 133120
s_add_u32 s64, s62, 16768
buffer_load_dwordx4 v117, s[36:39], s64 offen offset:0 lds
s_waitcnt lgkmcnt(6)
v_mfma_f32_32x32x16_bf16 v[128:143], v[72:75], a[24:27], v[128:143]
s_add_u32 m0, s63, 134144
s_add_u32 s64, s62, 24960
buffer_load_dwordx4 v118, s[36:39], s64 offen offset:0 lds
ds_read_b64_tr_b16 v[88:89], v116 offset:27648
ds_read_b64_tr_b16 v[90:91], v116 offset:27904
v_mfma_f32_32x32x16_bf16 v[144:159], v[72:75], a[28:31], v[144:159]
s_add_u32 m0, s63, 135168
s_add_u32 s64, s62, 33152
buffer_load_dwordx4 v117, s[36:39], s64 offen offset:0 lds
s_waitcnt lgkmcnt(6)
v_mfma_f32_32x32x16_bf16 v[160:175], v[76:79], a[24:27], v[160:175]
s_add_u32 m0, s63, 136192
s_add_u32 s64, s62, 41344
buffer_load_dwordx4 v118, s[36:39], s64 offen offset:0 lds
ds_read_b64_tr_b16 v[92:93], v116 offset:28160
ds_read_b64_tr_b16 v[94:95], v116 offset:28416
v_add_u32 v116, 0xfffe8000, v116
v_mfma_f32_32x32x16_bf16 v[176:191], v[76:79], a[28:31], v[176:191]
s_add_u32 m0, s63, 137216
s_add_u32 s64, s62, 49536
buffer_load_dwordx4 v117, s[36:39], s64 offen offset:0 lds
s_waitcnt lgkmcnt(6)
v_mfma_f32_32x32x16_bf16 v[192:207], v[80:83], a[24:27], v[192:207]
s_add_u32 m0, s63, 138240
s_add_u32 s64, s62, 57728
buffer_load_dwordx4 v118, s[36:39], s64 offen offset:0 lds
v_mfma_f32_32x32x16_bf16 v[208:223], v[80:83], a[28:31], v[208:223]
s_waitcnt vmcnt(17)
s_waitcnt lgkmcnt(0)
s_barrier
v_mfma_f32_32x32x16_bf16 v[224:239], v[84:87], a[24:27], v[224:239]
ds_read_b128 a[0:3], v112 offset:0
ds_read_b128 a[4:7], v112 offset:4096
v_xor_b32 v112, 0x30000, v112
ds_read_b64_tr_b16 v[64:65], v116 offset:0
ds_read_b64_tr_b16 v[66:67], v116 offset:256
ds_read_b64_tr_b16 v[68:69], v116 offset:512
v_mfma_f32_32x32x16_bf16 v[240:255], v[84:87], a[28:31], v[240:255]
ds_read_b64_tr_b16 v[70:71], v116 offset:768
v_mfma_f32_32x32x16_bf16 a[192:207], v[88:91], a[24:27], a[192:207]
ds_read_b64_tr_b16 v[72:73], v116 offset:1024
ds_read_b64_tr_b16 v[74:75], v116 offset:1280
v_mfma_f32_32x32x16_bf16 a[208:223], v[88:91], a[28:31], a[208:223]
v_mfma_f32_32x32x16_bf16 a[224:239], v[92:95], a[24:27], a[224:239]
ds_read_b64_tr_b16 v[76:77], v116 offset:1536
ds_read_b64_tr_b16 v[78:79], v116 offset:1792
v_mfma_f32_32x32x16_bf16 a[240:255], v[92:95], a[28:31], a[240:255]
s_waitcnt lgkmcnt(6)
v_mfma_f32_32x32x16_bf16 v[0:15], v[64:67], a[0:3], v[0:15]
ds_read_b64_tr_b16 v[80:81], v116 offset:2048
ds_read_b64_tr_b16 v[82:83], v116 offset:2304
v_mfma_f32_32x32x16_bf16 v[16:31], v[64:67], a[4:7], v[16:31]
s_waitcnt lgkmcnt(6)
v_mfma_f32_32x32x16_bf16 v[32:47], v[68:71], a[0:3], v[32:47]
s_add_u32 m0, s63, 98304
s_add_u32 s65, s74, 2097152
buffer_load_dwordx4 v119, s[40:43], s65 offen offset:0 lds
ds_read_b64_tr_b16 v[84:85], v116 offset:2560
ds_read_b64_tr_b16 v[86:87], v116 offset:2816
v_mfma_f32_32x32x16_bf16 v[48:63], v[68:71], a[4:7], v[48:63]
s_waitcnt lgkmcnt(6)
v_mfma_f32_32x32x16_bf16 v[128:143], v[72:75], a[0:3], v[128:143]
ds_read_b64_tr_b16 v[88:89], v116 offset:3072
ds_read_b64_tr_b16 v[90:91], v116 offset:3328
v_mfma_f32_32x32x16_bf16 v[144:159], v[72:75], a[4:7], v[144:159]
s_waitcnt lgkmcnt(6)
v_mfma_f32_32x32x16_bf16 v[160:175], v[76:79], a[0:3], v[160:175]
s_add_u32 m0, s63, 99328
s_add_u32 s65, s74, 2097280
buffer_load_dwordx4 v119, s[40:43], s65 offen offset:0 lds
ds_read_b64_tr_b16 v[92:93], v116 offset:3584
ds_read_b64_tr_b16 v[94:95], v116 offset:3840
v_mfma_f32_32x32x16_bf16 v[176:191], v[76:79], a[4:7], v[176:191]
s_waitcnt lgkmcnt(6)
v_mfma_f32_32x32x16_bf16 v[192:207], v[80:83], a[0:3], v[192:207]
ds_read_b64_tr_b16 v[64:65], v116 offset:8192
ds_read_b64_tr_b16 v[66:67], v116 offset:8448
v_mfma_f32_32x32x16_bf16 v[208:223], v[80:83], a[4:7], v[208:223]
s_waitcnt lgkmcnt(6)
v_mfma_f32_32x32x16_bf16 v[224:239], v[84:87], a[0:3], v[224:239]
s_add_u32 m0, s63, 100352
s_add_u32 s65, s74, 2097408
buffer_load_dwordx4 v119, s[40:43], s65 offen offset:0 lds
ds_read_b128 a[8:11], v113 offset:0
ds_read_b128 a[12:15], v113 offset:4096
v_xor_b32 v113, 0x30000, v113
v_mfma_f32_32x32x16_bf16 v[240:255], v[84:87], a[4:7], v[240:255]
ds_read_b64_tr_b16 v[68:69], v116 offset:8704
ds_read_b64_tr_b16 v[70:71], v116 offset:8960
s_waitcnt lgkmcnt(8)
v_mfma_f32_32x32x16_bf16 a[192:207], v[88:91], a[0:3], a[192:207]
ds_read_b64_tr_b16 v[72:73], v116 offset:9216
ds_read_b64_tr_b16 v[74:75], v116 offset:9472
v_mfma_f32_32x32x16_bf16 a[208:223], v[88:91], a[4:7], a[208:223]
s_waitcnt lgkmcnt(8)
v_mfma_f32_32x32x16_bf16 a[224:239], v[92:95], a[0:3], a[224:239]
s_add_u32 m0, s63, 101376
s_add_u32 s65, s74, 2097536
buffer_load_dwordx4 v119, s[40:43], s65 offen offset:0 lds
ds_read_b64_tr_b16 v[76:77], v116 offset:9728
ds_read_b64_tr_b16 v[78:79], v116 offset:9984
v_mfma_f32_32x32x16_bf16 a[240:255], v[92:95], a[4:7], a[240:255]
s_waitcnt lgkmcnt(7)
v_mfma_f32_32x32x16_bf16 v[0:15], v[64:67], a[8:11], v[0:15]
ds_read_b64_tr_b16 v[80:81], v116 offset:10240
ds_read_b64_tr_b16 v[82:83], v116 offset:10496
s_waitcnt lgkmcnt(8)
v_mfma_f32_32x32x16_bf16 v[16:31], v[64:67], a[12:15], v[16:31]
s_waitcnt lgkmcnt(6)
v_mfma_f32_32x32x16_bf16 v[32:47], v[68:71], a[8:11], v[32:47]
s_add_u32 m0, s63, 102400
s_add_u32 s65, s74, 2162688
buffer_load_dwordx4 v119, s[40:43], s65 offen offset:0 lds
ds_read_b64_tr_b16 v[84:85], v116 offset:10752
ds_read_b64_tr_b16 v[86:87], v116 offset:11008
v_mfma_f32_32x32x16_bf16 v[48:63], v[68:71], a[12:15], v[48:63]
s_waitcnt lgkmcnt(6)
v_mfma_f32_32x32x16_bf16 v[128:143], v[72:75], a[8:11], v[128:143]
ds_read_b64_tr_b16 v[88:89], v116 offset:11264
ds_read_b64_tr_b16 v[90:91], v116 offset:11520
v_mfma_f32_32x32x16_bf16 v[144:159], v[72:75], a[12:15], v[144:159]
s_waitcnt lgkmcnt(6)
v_mfma_f32_32x32x16_bf16 v[160:175], v[76:79], a[8:11], v[160:175]
s_add_u32 m0, s63, 103424
s_add_u32 s65, s74, 2162816
buffer_load_dwordx4 v119, s[40:43], s65 offen offset:0 lds
ds_read_b64_tr_b16 v[92:93], v116 offset:11776
ds_read_b64_tr_b16 v[94:95], v116 offset:12032
v_mfma_f32_32x32x16_bf16 v[176:191], v[76:79], a[12:15], v[176:191]
s_waitcnt lgkmcnt(6)
v_mfma_f32_32x32x16_bf16 v[192:207], v[80:83], a[8:11], v[192:207]
ds_read_b64_tr_b16 v[64:65], v116 offset:16384
ds_read_b64_tr_b16 v[66:67], v116 offset:16640
v_mfma_f32_32x32x16_bf16 v[208:223], v[80:83], a[12:15], v[208:223]
s_waitcnt lgkmcnt(6)
v_mfma_f32_32x32x16_bf16 v[224:239], v[84:87], a[8:11], v[224:239]
s_add_u32 m0, s63, 104448
s_add_u32 s65, s74, 2162944
buffer_load_dwordx4 v119, s[40:43], s65 offen offset:0 lds
ds_read_b128 a[16:19], v114 offset:0
ds_read_b128 a[20:23], v114 offset:4096
v_xor_b32 v114, 0x30000, v114
v_mfma_f32_32x32x16_bf16 v[240:255], v[84:87], a[12:15], v[240:255]
ds_read_b64_tr_b16 v[68:69], v116 offset:16896
ds_read_b64_tr_b16 v[70:71], v116 offset:17152
s_waitcnt lgkmcnt(8)
v_mfma_f32_32x32x16_bf16 a[192:207], v[88:91], a[8:11], a[192:207]
ds_read_b64_tr_b16 v[72:73], v116 offset:17408
ds_read_b64_tr_b16 v[74:75], v116 offset:17664
v_mfma_f32_32x32x16_bf16 a[208:223], v[88:91], a[12:15], a[208:223]
s_waitcnt lgkmcnt(8)
v_mfma_f32_32x32x16_bf16 a[224:239], v[92:95], a[8:11], a[224:239]
s_add_u32 m0, s63, 105472
s_add_u32 s65, s74, 2163072
buffer_load_dwordx4 v119, s[40:43], s65 offen offset:0 lds
ds_read_b64_tr_b16 v[76:77], v116 offset:17920
ds_read_b64_tr_b16 v[78:79], v116 offset:18176
v_mfma_f32_32x32x16_bf16 a[240:255], v[92:95], a[12:15], a[240:255]
s_waitcnt lgkmcnt(7)
v_mfma_f32_32x32x16_bf16 v[0:15], v[64:67], a[16:19], v[0:15]
ds_read_b64_tr_b16 v[80:81], v116 offset:18432
ds_read_b64_tr_b16 v[82:83], v116 offset:18688
s_waitcnt lgkmcnt(8)
v_mfma_f32_32x32x16_bf16 v[16:31], v[64:67], a[20:23], v[16:31]
s_waitcnt lgkmcnt(6)
v_mfma_f32_32x32x16_bf16 v[32:47], v[68:71], a[16:19], v[32:47]
ds_read_b64_tr_b16 v[84:85], v116 offset:18944
ds_read_b64_tr_b16 v[86:87], v116 offset:19200
v_mfma_f32_32x32x16_bf16 v[48:63], v[68:71], a[20:23], v[48:63]
s_waitcnt lgkmcnt(6)
v_mfma_f32_32x32x16_bf16 v[128:143], v[72:75], a[16:19], v[128:143]
ds_read_b64_tr_b16 v[88:89], v116 offset:19456
ds_read_b64_tr_b16 v[90:91], v116 offset:19712
v_mfma_f32_32x32x16_bf16 v[144:159], v[72:75], a[20:23], v[144:159]
s_waitcnt lgkmcnt(6)
v_mfma_f32_32x32x16_bf16 v[160:175], v[76:79], a[16:19], v[160:175]
ds_read_b64_tr_b16 v[92:93], v116 offset:19968
ds_read_b64_tr_b16 v[94:95], v116 offset:20224
v_mfma_f32_32x32x16_bf16 v[176:191], v[76:79], a[20:23], v[176:191]
s_waitcnt lgkmcnt(6)
v_mfma_f32_32x32x16_bf16 v[192:207], v[80:83], a[16:19], v[192:207]
ds_read_b64_tr_b16 v[64:65], v116 offset:24576
ds_read_b64_tr_b16 v[66:67], v116 offset:24832
v_mfma_f32_32x32x16_bf16 v[208:223], v[80:83], a[20:23], v[208:223]
s_waitcnt lgkmcnt(6)
v_mfma_f32_32x32x16_bf16 v[224:239], v[84:87], a[16:19], v[224:239]
ds_read_b128 a[24:27], v115 offset:0
ds_read_b128 a[28:31], v115 offset:4096
v_xor_b32 v115, 0x30000, v115
ds_read_b64_tr_b16 v[68:69], v116 offset:25088
ds_read_b64_tr_b16 v[70:71], v116 offset:25344
v_mfma_f32_32x32x16_bf16 v[240:255], v[84:87], a[20:23], v[240:255]
s_waitcnt lgkmcnt(8)
v_mfma_f32_32x32x16_bf16 a[192:207], v[88:91], a[16:19], a[192:207]
ds_read_b64_tr_b16 v[72:73], v116 offset:25600
ds_read_b64_tr_b16 v[74:75], v116 offset:25856
v_mfma_f32_32x32x16_bf16 a[208:223], v[88:91], a[20:23], a[208:223]
s_waitcnt lgkmcnt(8)
v_mfma_f32_32x32x16_bf16 a[224:239], v[92:95], a[16:19], a[224:239]
ds_read_b64_tr_b16 v[76:77], v116 offset:26112
ds_read_b64_tr_b16 v[78:79], v116 offset:26368
v_mfma_f32_32x32x16_bf16 a[240:255], v[92:95], a[20:23], a[240:255]
s_waitcnt lgkmcnt(7)
v_mfma_f32_32x32x16_bf16 v[0:15], v[64:67], a[24:27], v[0:15]
ds_read_b64_tr_b16 v[80:81], v116 offset:26624
ds_read_b64_tr_b16 v[82:83], v116 offset:26880
s_waitcnt lgkmcnt(8)
v_mfma_f32_32x32x16_bf16 v[16:31], v[64:67], a[28:31], v[16:31]
s_add_u32 m0, s63, 65536
s_add_u32 s64, s62, 512
buffer_load_dwordx4 v117, s[36:39], s64 offen offset:0 lds
s_waitcnt lgkmcnt(6)
v_mfma_f32_32x32x16_bf16 v[32:47], v[68:71], a[24:27], v[32:47]
s_add_u32 m0, s63, 66560
s_add_u32 s64, s62, 8704
buffer_load_dwordx4 v118, s[36:39], s64 offen offset:0 lds
ds_read_b64_tr_b16 v[84:85], v116 offset:27136
ds_read_b64_tr_b16 v[86:87], v116 offset:27392
v_mfma_f32_32x32x16_bf16 v[48:63], v[68:71], a[28:31], v[48:63]
s_add_u32 m0, s63, 67584
s_add_u32 s64, s62, 16896
buffer_load_dwordx4 v117, s[36:39], s64 offen offset:0 lds
s_waitcnt lgkmcnt(6)
v_mfma_f32_32x32x16_bf16 v[128:143], v[72:75], a[24:27], v[128:143]
s_add_u32 m0, s63, 68608
s_add_u32 s64, s62, 25088
buffer_load_dwordx4 v118, s[36:39], s64 offen offset:0 lds
ds_read_b64_tr_b16 v[88:89], v116 offset:27648
ds_read_b64_tr_b16 v[90:91], v116 offset:27904
v_mfma_f32_32x32x16_bf16 v[144:159], v[72:75], a[28:31], v[144:159]
s_add_u32 m0, s63, 69632
s_add_u32 s64, s62, 33280
buffer_load_dwordx4 v117, s[36:39], s64 offen offset:0 lds
s_waitcnt lgkmcnt(6)
v_mfma_f32_32x32x16_bf16 v[160:175], v[76:79], a[24:27], v[160:175]
s_add_u32 m0, s63, 70656
s_add_u32 s64, s62, 41472
buffer_load_dwordx4 v118, s[36:39], s64 offen offset:0 lds
ds_read_b64_tr_b16 v[92:93], v116 offset:28160
ds_read_b64_tr_b16 v[94:95], v116 offset:28416
v_add_u32 v116, 0x8000, v116
v_mfma_f32_32x32x16_bf16 v[176:191], v[76:79], a[28:31], v[176:191]
s_add_u32 m0, s63, 71680
s_add_u32 s64, s62, 49664
buffer_load_dwordx4 v117, s[36:39], s64 offen offset:0 lds
s_waitcnt lgkmcnt(6)
v_mfma_f32_32x32x16_bf16 v[192:207], v[80:83], a[24:27], v[192:207]
s_add_u32 m0, s63, 72704
s_add_u32 s64, s62, 57856
buffer_load_dwordx4 v118, s[36:39], s64 offen offset:0 lds
v_mfma_f32_32x32x16_bf16 v[208:223], v[80:83], a[28:31], v[208:223]
s_waitcnt vmcnt(16)
s_waitcnt lgkmcnt(0)
s_barrier
v_mfma_f32_32x32x16_bf16 v[224:239], v[84:87], a[24:27], v[224:239]
ds_read_b128 a[0:3], v112 offset:0
ds_read_b128 a[4:7], v112 offset:4096
v_xor_b32 v112, 0x30000, v112
ds_read_b64_tr_b16 v[64:65], v116 offset:0
ds_read_b64_tr_b16 v[66:67], v116 offset:256
ds_read_b64_tr_b16 v[68:69], v116 offset:512
v_mfma_f32_32x32x16_bf16 v[240:255], v[84:87], a[28:31], v[240:255]
ds_read_b64_tr_b16 v[70:71], v116 offset:768
v_mfma_f32_32x32x16_bf16 a[192:207], v[88:91], a[24:27], a[192:207]
ds_read_b64_tr_b16 v[72:73], v116 offset:1024
ds_read_b64_tr_b16 v[74:75], v116 offset:1280
v_mfma_f32_32x32x16_bf16 a[208:223], v[88:91], a[28:31], a[208:223]
v_mfma_f32_32x32x16_bf16 a[224:239], v[92:95], a[24:27], a[224:239]
ds_read_b64_tr_b16 v[76:77], v116 offset:1536
ds_read_b64_tr_b16 v[78:79], v116 offset:1792
v_mfma_f32_32x32x16_bf16 a[240:255], v[92:95], a[28:31], a[240:255]
s_waitcnt lgkmcnt(6)
v_mfma_f32_32x32x16_bf16 v[0:15], v[64:67], a[0:3], v[0:15]
ds_read_b64_tr_b16 v[80:81], v116 offset:2048
ds_read_b64_tr_b16 v[82:83], v116 offset:2304
v_mfma_f32_32x32x16_bf16 v[16:31], v[64:67], a[4:7], v[16:31]
s_waitcnt lgkmcnt(6)
v_mfma_f32_32x32x16_bf16 v[32:47], v[68:71], a[0:3], v[32:47]
s_add_u32 m0, s63, 0
s_add_u32 s65, s74, 2621440
buffer_load_dwordx4 v119, s[40:43], s65 offen offset:0 lds
ds_read_b64_tr_b16 v[84:85], v116 offset:2560
ds_read_b64_tr_b16 v[86:87], v116 offset:2816
v_mfma_f32_32x32x16_bf16 v[48:63], v[68:71], a[4:7], v[48:63]
s_waitcnt lgkmcnt(6)
v_mfma_f32_32x32x16_bf16 v[128:143], v[72:75], a[0:3], v[128:143]
ds_read_b64_tr_b16 v[88:89], v116 offset:3072
ds_read_b64_tr_b16 v[90:91], v116 offset:3328
v_mfma_f32_32x32x16_bf16 v[144:159], v[72:75], a[4:7], v[144:159]
s_waitcnt lgkmcnt(6)
v_mfma_f32_32x32x16_bf16 v[160:175], v[76:79], a[0:3], v[160:175]
s_add_u32 m0, s63, 1024
s_add_u32 s65, s74, 2621568
buffer_load_dwordx4 v119, s[40:43], s65 offen offset:0 lds
ds_read_b64_tr_b16 v[92:93], v116 offset:3584
ds_read_b64_tr_b16 v[94:95], v116 offset:3840
v_mfma_f32_32x32x16_bf16 v[176:191], v[76:79], a[4:7], v[176:191]
s_waitcnt lgkmcnt(6)
v_mfma_f32_32x32x16_bf16 v[192:207], v[80:83], a[0:3], v[192:207]
ds_read_b64_tr_b16 v[64:65], v116 offset:8192
ds_read_b64_tr_b16 v[66:67], v116 offset:8448
v_mfma_f32_32x32x16_bf16 v[208:223], v[80:83], a[4:7], v[208:223]
s_waitcnt lgkmcnt(6)
v_mfma_f32_32x32x16_bf16 v[224:239], v[84:87], a[0:3], v[224:239]
s_add_u32 m0, s63, 2048
s_add_u32 s65, s74, 2621696
buffer_load_dwordx4 v119, s[40:43], s65 offen offset:0 lds
ds_read_b128 a[8:11], v113 offset:0
ds_read_b128 a[12:15], v113 offset:4096
v_xor_b32 v113, 0x30000, v113
v_mfma_f32_32x32x16_bf16 v[240:255], v[84:87], a[4:7], v[240:255]
ds_read_b64_tr_b16 v[68:69], v116 offset:8704
ds_read_b64_tr_b16 v[70:71], v116 offset:8960
s_waitcnt lgkmcnt(8)
v_mfma_f32_32x32x16_bf16 a[192:207], v[88:91], a[0:3], a[192:207]
ds_read_b64_tr_b16 v[72:73], v116 offset:9216
ds_read_b64_tr_b16 v[74:75], v116 offset:9472
v_mfma_f32_32x32x16_bf16 a[208:223], v[88:91], a[4:7], a[208:223]
s_waitcnt lgkmcnt(8)
v_mfma_f32_32x32x16_bf16 a[224:239], v[92:95], a[0:3], a[224:239]
s_add_u32 m0, s63, 3072
s_add_u32 s65, s74, 2621824
buffer_load_dwordx4 v119, s[40:43], s65 offen offset:0 lds
ds_read_b64_tr_b16 v[76:77], v116 offset:9728
ds_read_b64_tr_b16 v[78:79], v116 offset:9984
v_mfma_f32_32x32x16_bf16 a[240:255], v[92:95], a[4:7], a[240:255]
s_waitcnt lgkmcnt(7)
v_mfma_f32_32x32x16_bf16 v[0:15], v[64:67], a[8:11], v[0:15]
ds_read_b64_tr_b16 v[80:81], v116 offset:10240
ds_read_b64_tr_b16 v[82:83], v116 offset:10496
s_waitcnt lgkmcnt(8)
v_mfma_f32_32x32x16_bf16 v[16:31], v[64:67], a[12:15], v[16:31]
s_waitcnt lgkmcnt(6)
v_mfma_f32_32x32x16_bf16 v[32:47], v[68:71], a[8:11], v[32:47]
s_add_u32 m0, s63, 4096
s_add_u32 s65, s74, 2686976
buffer_load_dwordx4 v119, s[40:43], s65 offen offset:0 lds
ds_read_b64_tr_b16 v[84:85], v116 offset:10752
ds_read_b64_tr_b16 v[86:87], v116 offset:11008
v_mfma_f32_32x32x16_bf16 v[48:63], v[68:71], a[12:15], v[48:63]
s_waitcnt lgkmcnt(6)
v_mfma_f32_32x32x16_bf16 v[128:143], v[72:75], a[8:11], v[128:143]
ds_read_b64_tr_b16 v[88:89], v116 offset:11264
ds_read_b64_tr_b16 v[90:91], v116 offset:11520
v_mfma_f32_32x32x16_bf16 v[144:159], v[72:75], a[12:15], v[144:159]
s_waitcnt lgkmcnt(6)
v_mfma_f32_32x32x16_bf16 v[160:175], v[76:79], a[8:11], v[160:175]
s_add_u32 m0, s63, 5120
s_add_u32 s65, s74, 2687104
buffer_load_dwordx4 v119, s[40:43], s65 offen offset:0 lds
ds_read_b64_tr_b16 v[92:93], v116 offset:11776
ds_read_b64_tr_b16 v[94:95], v116 offset:12032
v_mfma_f32_32x32x16_bf16 v[176:191], v[76:79], a[12:15], v[176:191]
s_waitcnt lgkmcnt(6)
v_mfma_f32_32x32x16_bf16 v[192:207], v[80:83], a[8:11], v[192:207]
ds_read_b64_tr_b16 v[64:65], v116 offset:16384
ds_read_b64_tr_b16 v[66:67], v116 offset:16640
v_mfma_f32_32x32x16_bf16 v[208:223], v[80:83], a[12:15], v[208:223]
s_waitcnt lgkmcnt(6)
v_mfma_f32_32x32x16_bf16 v[224:239], v[84:87], a[8:11], v[224:239]
s_add_u32 m0, s63, 6144
s_add_u32 s65, s74, 2687232
buffer_load_dwordx4 v119, s[40:43], s65 offen offset:0 lds
ds_read_b128 a[16:19], v114 offset:0
ds_read_b128 a[20:23], v114 offset:4096
v_xor_b32 v114, 0x30000, v114
v_mfma_f32_32x32x16_bf16 v[240:255], v[84:87], a[12:15], v[240:255]
ds_read_b64_tr_b16 v[68:69], v116 offset:16896
ds_read_b64_tr_b16 v[70:71], v116 offset:17152
s_waitcnt lgkmcnt(8)
v_mfma_f32_32x32x16_bf16 a[192:207], v[88:91], a[8:11], a[192:207]
ds_read_b64_tr_b16 v[72:73], v116 offset:17408
ds_read_b64_tr_b16 v[74:75], v116 offset:17664
v_mfma_f32_32x32x16_bf16 a[208:223], v[88:91], a[12:15], a[208:223]
s_waitcnt lgkmcnt(8)
v_mfma_f32_32x32x16_bf16 a[224:239], v[92:95], a[8:11], a[224:239]
s_add_u32 m0, s63, 7168
s_add_u32 s65, s74, 2687360
buffer_load_dwordx4 v119, s[40:43], s65 offen offset:0 lds
ds_read_b64_tr_b16 v[76:77], v116 offset:17920
ds_read_b64_tr_b16 v[78:79], v116 offset:18176
v_mfma_f32_32x32x16_bf16 a[240:255], v[92:95], a[12:15], a[240:255]
s_waitcnt lgkmcnt(7)
v_mfma_f32_32x32x16_bf16 v[0:15], v[64:67], a[16:19], v[0:15]
ds_read_b64_tr_b16 v[80:81], v116 offset:18432
ds_read_b64_tr_b16 v[82:83], v116 offset:18688
s_waitcnt lgkmcnt(8)
v_mfma_f32_32x32x16_bf16 v[16:31], v[64:67], a[20:23], v[16:31]
s_waitcnt lgkmcnt(6)
v_mfma_f32_32x32x16_bf16 v[32:47], v[68:71], a[16:19], v[32:47]
ds_read_b64_tr_b16 v[84:85], v116 offset:18944
ds_read_b64_tr_b16 v[86:87], v116 offset:19200
v_mfma_f32_32x32x16_bf16 v[48:63], v[68:71], a[20:23], v[48:63]
s_waitcnt lgkmcnt(6)
v_mfma_f32_32x32x16_bf16 v[128:143], v[72:75], a[16:19], v[128:143]
ds_read_b64_tr_b16 v[88:89], v116 offset:19456
ds_read_b64_tr_b16 v[90:91], v116 offset:19712
v_mfma_f32_32x32x16_bf16 v[144:159], v[72:75], a[20:23], v[144:159]
s_waitcnt lgkmcnt(6)
v_mfma_f32_32x32x16_bf16 v[160:175], v[76:79], a[16:19], v[160:175]
ds_read_b64_tr_b16 v[92:93], v116 offset:19968
ds_read_b64_tr_b16 v[94:95], v116 offset:20224
v_mfma_f32_32x32x16_bf16 v[176:191], v[76:79], a[20:23], v[176:191]
s_waitcnt lgkmcnt(6)
v_mfma_f32_32x32x16_bf16 v[192:207], v[80:83], a[16:19], v[192:207]
ds_read_b64_tr_b16 v[64:65], v116 offset:24576
ds_read_b64_tr_b16 v[66:67], v116 offset:24832
v_mfma_f32_32x32x16_bf16 v[208:223], v[80:83], a[20:23], v[208:223]
s_waitcnt lgkmcnt(6)
v_mfma_f32_32x32x16_bf16 v[224:239], v[84:87], a[16:19], v[224:239]
ds_read_b128 a[24:27], v115 offset:0
ds_read_b128 a[28:31], v115 offset:4096
v_xor_b32 v115, 0x30000, v115
ds_read_b64_tr_b16 v[68:69], v116 offset:25088
ds_read_b64_tr_b16 v[70:71], v116 offset:25344
v_mfma_f32_32x32x16_bf16 v[240:255], v[84:87], a[20:23], v[240:255]
s_waitcnt lgkmcnt(8)
v_mfma_f32_32x32x16_bf16 a[192:207], v[88:91], a[16:19], a[192:207]
ds_read_b64_tr_b16 v[72:73], v116 offset:25600
ds_read_b64_tr_b16 v[74:75], v116 offset:25856
v_mfma_f32_32x32x16_bf16 a[208:223], v[88:91], a[20:23], a[208:223]
s_waitcnt lgkmcnt(8)
v_mfma_f32_32x32x16_bf16 a[224:239], v[92:95], a[16:19], a[224:239]
ds_read_b64_tr_b16 v[76:77], v116 offset:26112
ds_read_b64_tr_b16 v[78:79], v116 offset:26368
v_mfma_f32_32x32x16_bf16 a[240:255], v[92:95], a[20:23], a[240:255]
s_waitcnt lgkmcnt(7)
v_mfma_f32_32x32x16_bf16 v[0:15], v[64:67], a[24:27], v[0:15]
ds_read_b64_tr_b16 v[80:81], v116 offset:26624
ds_read_b64_tr_b16 v[82:83], v116 offset:26880
s_waitcnt lgkmcnt(8)
v_mfma_f32_32x32x16_bf16 v[16:31], v[64:67], a[28:31], v[16:31]
s_add_u32 m0, s63, 131072
s_add_u32 s64, s62, 640
buffer_load_dwordx4 v117, s[36:39], s64 offen offset:0 lds
s_waitcnt lgkmcnt(6)
v_mfma_f32_32x32x16_bf16 v[32:47], v[68:71], a[24:27], v[32:47]
s_add_u32 m0, s63, 132096
s_add_u32 s64, s62, 8832
buffer_load_dwordx4 v118, s[36:39], s64 offen offset:0 lds
ds_read_b64_tr_b16 v[84:85], v116 offset:27136
ds_read_b64_tr_b16 v[86:87], v116 offset:27392
v_mfma_f32_32x32x16_bf16 v[48:63], v[68:71], a[28:31], v[48:63]
s_add_u32 m0, s63, 133120
s_add_u32 s64, s62, 17024
buffer_load_dwordx4 v117, s[36:39], s64 offen offset:0 lds
s_waitcnt lgkmcnt(6)
v_mfma_f32_32x32x16_bf16 v[128:143], v[72:75], a[24:27], v[128:143]
s_add_u32 m0, s63, 134144
s_add_u32 s64, s62, 25216
buffer_load_dwordx4 v118, s[36:39], s64 offen offset:0 lds
ds_read_b64_tr_b16 v[88:89], v116 offset:27648
ds_read_b64_tr_b16 v[90:91], v116 offset:27904
v_mfma_f32_32x32x16_bf16 v[144:159], v[72:75], a[28:31], v[144:159]
s_add_u32 m0, s63, 135168
s_add_u32 s64, s62, 33408
buffer_load_dwordx4 v117, s[36:39], s64 offen offset:0 lds
s_waitcnt lgkmcnt(6)
v_mfma_f32_32x32x16_bf16 v[160:175], v[76:79], a[24:27], v[160:175]
s_add_u32 m0, s63, 136192
s_add_u32 s64, s62, 41600
buffer_load_dwordx4 v118, s[36:39], s64 offen offset:0 lds
ds_read_b64_tr_b16 v[92:93], v116 offset:28160
ds_read_b64_tr_b16 v[94:95], v116 offset:28416
v_add_u32 v116, 0x10000, v116
v_mfma_f32_32x32x16_bf16 v[176:191], v[76:79], a[28:31], v[176:191]
s_add_u32 m0, s63, 137216
s_add_u32 s64, s62, 49792
buffer_load_dwordx4 v117, s[36:39], s64 offen offset:0 lds
s_waitcnt lgkmcnt(6)
v_mfma_f32_32x32x16_bf16 v[192:207], v[80:83], a[24:27], v[192:207]
s_add_u32 m0, s63, 138240
s_add_u32 s64, s62, 57984
buffer_load_dwordx4 v118, s[36:39], s64 offen offset:0 lds
v_mfma_f32_32x32x16_bf16 v[208:223], v[80:83], a[28:31], v[208:223]
s_waitcnt vmcnt(16)
s_waitcnt lgkmcnt(0)
s_barrier
v_mfma_f32_32x32x16_bf16 v[224:239], v[84:87], a[24:27], v[224:239]
ds_read_b128 a[0:3], v112 offset:0
ds_read_b128 a[4:7], v112 offset:4096
v_xor_b32 v112, 0x30000, v112
ds_read_b64_tr_b16 v[64:65], v116 offset:0
ds_read_b64_tr_b16 v[66:67], v116 offset:256
ds_read_b64_tr_b16 v[68:69], v116 offset:512
v_mfma_f32_32x32x16_bf16 v[240:255], v[84:87], a[28:31], v[240:255]
ds_read_b64_tr_b16 v[70:71], v116 offset:768
v_mfma_f32_32x32x16_bf16 a[192:207], v[88:91], a[24:27], a[192:207]
ds_read_b64_tr_b16 v[72:73], v116 offset:1024
ds_read_b64_tr_b16 v[74:75], v116 offset:1280
v_mfma_f32_32x32x16_bf16 a[208:223], v[88:91], a[28:31], a[208:223]
v_mfma_f32_32x32x16_bf16 a[224:239], v[92:95], a[24:27], a[224:239]
ds_read_b64_tr_b16 v[76:77], v116 offset:1536
ds_read_b64_tr_b16 v[78:79], v116 offset:1792
v_mfma_f32_32x32x16_bf16 a[240:255], v[92:95], a[28:31], a[240:255]
s_waitcnt lgkmcnt(6)
v_mfma_f32_32x32x16_bf16 v[0:15], v[64:67], a[0:3], v[0:15]
ds_read_b64_tr_b16 v[80:81], v116 offset:2048
ds_read_b64_tr_b16 v[82:83], v116 offset:2304
v_mfma_f32_32x32x16_bf16 v[16:31], v[64:67], a[4:7], v[16:31]
s_waitcnt lgkmcnt(6)
v_mfma_f32_32x32x16_bf16 v[32:47], v[68:71], a[0:3], v[32:47]
s_add_u32 m0, s63, 32768
s_add_u32 s65, s74, 3145728
buffer_load_dwordx4 v119, s[40:43], s65 offen offset:0 lds
ds_read_b64_tr_b16 v[84:85], v116 offset:2560
ds_read_b64_tr_b16 v[86:87], v116 offset:2816
v_mfma_f32_32x32x16_bf16 v[48:63], v[68:71], a[4:7], v[48:63]
s_waitcnt lgkmcnt(6)
v_mfma_f32_32x32x16_bf16 v[128:143], v[72:75], a[0:3], v[128:143]
ds_read_b64_tr_b16 v[88:89], v116 offset:3072
ds_read_b64_tr_b16 v[90:91], v116 offset:3328
v_mfma_f32_32x32x16_bf16 v[144:159], v[72:75], a[4:7], v[144:159]
s_waitcnt lgkmcnt(6)
v_mfma_f32_32x32x16_bf16 v[160:175], v[76:79], a[0:3], v[160:175]
s_add_u32 m0, s63, 33792
s_add_u32 s65, s74, 3145856
buffer_load_dwordx4 v119, s[40:43], s65 offen offset:0 lds
ds_read_b64_tr_b16 v[92:93], v116 offset:3584
ds_read_b64_tr_b16 v[94:95], v116 offset:3840
v_mfma_f32_32x32x16_bf16 v[176:191], v[76:79], a[4:7], v[176:191]
s_waitcnt lgkmcnt(6)
v_mfma_f32_32x32x16_bf16 v[192:207], v[80:83], a[0:3], v[192:207]
ds_read_b64_tr_b16 v[64:65], v116 offset:8192
ds_read_b64_tr_b16 v[66:67], v116 offset:8448
v_mfma_f32_32x32x16_bf16 v[208:223], v[80:83], a[4:7], v[208:223]
s_waitcnt lgkmcnt(6)
v_mfma_f32_32x32x16_bf16 v[224:239], v[84:87], a[0:3], v[224:239]
s_add_u32 m0, s63, 34816
s_add_u32 s65, s74, 3145984
buffer_load_dwordx4 v119, s[40:43], s65 offen offset:0 lds
ds_read_b128 a[8:11], v113 offset:0
ds_read_b128 a[12:15], v113 offset:4096
v_xor_b32 v113, 0x30000, v113
v_mfma_f32_32x32x16_bf16 v[240:255], v[84:87], a[4:7], v[240:255]
ds_read_b64_tr_b16 v[68:69], v116 offset:8704
ds_read_b64_tr_b16 v[70:71], v116 offset:8960
s_waitcnt lgkmcnt(8)
v_mfma_f32_32x32x16_bf16 a[192:207], v[88:91], a[0:3], a[192:207]
ds_read_b64_tr_b16 v[72:73], v116 offset:9216
ds_read_b64_tr_b16 v[74:75], v116 offset:9472
v_mfma_f32_32x32x16_bf16 a[208:223], v[88:91], a[4:7], a[208:223]
s_waitcnt lgkmcnt(8)
v_mfma_f32_32x32x16_bf16 a[224:239], v[92:95], a[0:3], a[224:239]
s_add_u32 m0, s63, 35840
s_add_u32 s65, s74, 3146112
buffer_load_dwordx4 v119, s[40:43], s65 offen offset:0 lds
ds_read_b64_tr_b16 v[76:77], v116 offset:9728
ds_read_b64_tr_b16 v[78:79], v116 offset:9984
v_mfma_f32_32x32x16_bf16 a[240:255], v[92:95], a[4:7], a[240:255]
s_waitcnt lgkmcnt(7)
v_mfma_f32_32x32x16_bf16 v[0:15], v[64:67], a[8:11], v[0:15]
ds_read_b64_tr_b16 v[80:81], v116 offset:10240
ds_read_b64_tr_b16 v[82:83], v116 offset:10496
s_waitcnt lgkmcnt(8)
v_mfma_f32_32x32x16_bf16 v[16:31], v[64:67], a[12:15], v[16:31]
s_waitcnt lgkmcnt(6)
v_mfma_f32_32x32x16_bf16 v[32:47], v[68:71], a[8:11], v[32:47]
s_add_u32 m0, s63, 36864
s_add_u32 s65, s74, 3211264
buffer_load_dwordx4 v119, s[40:43], s65 offen offset:0 lds
ds_read_b64_tr_b16 v[84:85], v116 offset:10752
ds_read_b64_tr_b16 v[86:87], v116 offset:11008
v_mfma_f32_32x32x16_bf16 v[48:63], v[68:71], a[12:15], v[48:63]
s_waitcnt lgkmcnt(6)
v_mfma_f32_32x32x16_bf16 v[128:143], v[72:75], a[8:11], v[128:143]
ds_read_b64_tr_b16 v[88:89], v116 offset:11264
ds_read_b64_tr_b16 v[90:91], v116 offset:11520
v_mfma_f32_32x32x16_bf16 v[144:159], v[72:75], a[12:15], v[144:159]
s_waitcnt lgkmcnt(6)
v_mfma_f32_32x32x16_bf16 v[160:175], v[76:79], a[8:11], v[160:175]
s_add_u32 m0, s63, 37888
s_add_u32 s65, s74, 3211392
buffer_load_dwordx4 v119, s[40:43], s65 offen offset:0 lds
ds_read_b64_tr_b16 v[92:93], v116 offset:11776
ds_read_b64_tr_b16 v[94:95], v116 offset:12032
v_mfma_f32_32x32x16_bf16 v[176:191], v[76:79], a[12:15], v[176:191]
s_waitcnt lgkmcnt(6)
v_mfma_f32_32x32x16_bf16 v[192:207], v[80:83], a[8:11], v[192:207]
ds_read_b64_tr_b16 v[64:65], v116 offset:16384
ds_read_b64_tr_b16 v[66:67], v116 offset:16640
v_mfma_f32_32x32x16_bf16 v[208:223], v[80:83], a[12:15], v[208:223]
s_waitcnt lgkmcnt(6)
v_mfma_f32_32x32x16_bf16 v[224:239], v[84:87], a[8:11], v[224:239]
s_add_u32 m0, s63, 38912
s_add_u32 s65, s74, 3211520
buffer_load_dwordx4 v119, s[40:43], s65 offen offset:0 lds
ds_read_b128 a[16:19], v114 offset:0
ds_read_b128 a[20:23], v114 offset:4096
v_xor_b32 v114, 0x30000, v114
v_mfma_f32_32x32x16_bf16 v[240:255], v[84:87], a[12:15], v[240:255]
ds_read_b64_tr_b16 v[68:69], v116 offset:16896
ds_read_b64_tr_b16 v[70:71], v116 offset:17152
s_waitcnt lgkmcnt(8)
v_mfma_f32_32x32x16_bf16 a[192:207], v[88:91], a[8:11], a[192:207]
ds_read_b64_tr_b16 v[72:73], v116 offset:17408
ds_read_b64_tr_b16 v[74:75], v116 offset:17664
v_mfma_f32_32x32x16_bf16 a[208:223], v[88:91], a[12:15], a[208:223]
s_waitcnt lgkmcnt(8)
v_mfma_f32_32x32x16_bf16 a[224:239], v[92:95], a[8:11], a[224:239]
s_add_u32 m0, s63, 39936
s_add_u32 s65, s74, 3211648
buffer_load_dwordx4 v119, s[40:43], s65 offen offset:0 lds
ds_read_b64_tr_b16 v[76:77], v116 offset:17920
ds_read_b64_tr_b16 v[78:79], v116 offset:18176
v_mfma_f32_32x32x16_bf16 a[240:255], v[92:95], a[12:15], a[240:255]
s_waitcnt lgkmcnt(7)
v_mfma_f32_32x32x16_bf16 v[0:15], v[64:67], a[16:19], v[0:15]
ds_read_b64_tr_b16 v[80:81], v116 offset:18432
ds_read_b64_tr_b16 v[82:83], v116 offset:18688
s_waitcnt lgkmcnt(8)
v_mfma_f32_32x32x16_bf16 v[16:31], v[64:67], a[20:23], v[16:31]
s_waitcnt lgkmcnt(6)
v_mfma_f32_32x32x16_bf16 v[32:47], v[68:71], a[16:19], v[32:47]
ds_read_b64_tr_b16 v[84:85], v116 offset:18944
ds_read_b64_tr_b16 v[86:87], v116 offset:19200
v_mfma_f32_32x32x16_bf16 v[48:63], v[68:71], a[20:23], v[48:63]
s_waitcnt lgkmcnt(6)
v_mfma_f32_32x32x16_bf16 v[128:143], v[72:75], a[16:19], v[128:143]
ds_read_b64_tr_b16 v[88:89], v116 offset:19456
ds_read_b64_tr_b16 v[90:91], v116 offset:19712
v_mfma_f32_32x32x16_bf16 v[144:159], v[72:75], a[20:23], v[144:159]
s_waitcnt lgkmcnt(6)
v_mfma_f32_32x32x16_bf16 v[160:175], v[76:79], a[16:19], v[160:175]
ds_read_b64_tr_b16 v[92:93], v116 offset:19968
ds_read_b64_tr_b16 v[94:95], v116 offset:20224
v_mfma_f32_32x32x16_bf16 v[176:191], v[76:79], a[20:23], v[176:191]
s_waitcnt lgkmcnt(6)
v_mfma_f32_32x32x16_bf16 v[192:207], v[80:83], a[16:19], v[192:207]
ds_read_b64_tr_b16 v[64:65], v116 offset:24576
ds_read_b64_tr_b16 v[66:67], v116 offset:24832
v_mfma_f32_32x32x16_bf16 v[208:223], v[80:83], a[20:23], v[208:223]
s_waitcnt lgkmcnt(6)
v_mfma_f32_32x32x16_bf16 v[224:239], v[84:87], a[16:19], v[224:239]
ds_read_b128 a[24:27], v115 offset:0
ds_read_b128 a[28:31], v115 offset:4096
v_xor_b32 v115, 0x30000, v115
ds_read_b64_tr_b16 v[68:69], v116 offset:25088
ds_read_b64_tr_b16 v[70:71], v116 offset:25344
v_mfma_f32_32x32x16_bf16 v[240:255], v[84:87], a[20:23], v[240:255]
s_waitcnt lgkmcnt(8)
v_mfma_f32_32x32x16_bf16 a[192:207], v[88:91], a[16:19], a[192:207]
ds_read_b64_tr_b16 v[72:73], v116 offset:25600
ds_read_b64_tr_b16 v[74:75], v116 offset:25856
v_mfma_f32_32x32x16_bf16 a[208:223], v[88:91], a[20:23], a[208:223]
s_waitcnt lgkmcnt(8)
v_mfma_f32_32x32x16_bf16 a[224:239], v[92:95], a[16:19], a[224:239]
ds_read_b64_tr_b16 v[76:77], v116 offset:26112
ds_read_b64_tr_b16 v[78:79], v116 offset:26368
v_mfma_f32_32x32x16_bf16 a[240:255], v[92:95], a[20:23], a[240:255]
s_waitcnt lgkmcnt(7)
v_mfma_f32_32x32x16_bf16 v[0:15], v[64:67], a[24:27], v[0:15]
ds_read_b64_tr_b16 v[80:81], v116 offset:26624
ds_read_b64_tr_b16 v[82:83], v116 offset:26880
s_waitcnt lgkmcnt(8)
v_mfma_f32_32x32x16_bf16 v[16:31], v[64:67], a[28:31], v[16:31]
s_add_u32 m0, s63, 65536
s_add_u32 s64, s62, 768
buffer_load_dwordx4 v117, s[36:39], s64 offen offset:0 lds
s_waitcnt lgkmcnt(6)
v_mfma_f32_32x32x16_bf16 v[32:47], v[68:71], a[24:27], v[32:47]
s_add_u32 m0, s63, 66560
s_add_u32 s64, s62, 8960
buffer_load_dwordx4 v118, s[36:39], s64 offen offset:0 lds
ds_read_b64_tr_b16 v[84:85], v116 offset:27136
ds_read_b64_tr_b16 v[86:87], v116 offset:27392
v_mfma_f32_32x32x16_bf16 v[48:63], v[68:71], a[28:31], v[48:63]
s_add_u32 m0, s63, 67584
s_add_u32 s64, s62, 17152
buffer_load_dwordx4 v117, s[36:39], s64 offen offset:0 lds
s_waitcnt lgkmcnt(6)
v_mfma_f32_32x32x16_bf16 v[128:143], v[72:75], a[24:27], v[128:143]
s_add_u32 m0, s63, 68608
s_add_u32 s64, s62, 25344
buffer_load_dwordx4 v118, s[36:39], s64 offen offset:0 lds
ds_read_b64_tr_b16 v[88:89], v116 offset:27648
ds_read_b64_tr_b16 v[90:91], v116 offset:27904
v_mfma_f32_32x32x16_bf16 v[144:159], v[72:75], a[28:31], v[144:159]
s_add_u32 m0, s63, 69632
s_add_u32 s64, s62, 33536
buffer_load_dwordx4 v117, s[36:39], s64 offen offset:0 lds
s_waitcnt lgkmcnt(6)
v_mfma_f32_32x32x16_bf16 v[160:175], v[76:79], a[24:27], v[160:175]
s_add_u32 m0, s63, 70656
s_add_u32 s64, s62, 41728
buffer_load_dwordx4 v118, s[36:39], s64 offen offset:0 lds
ds_read_b64_tr_b16 v[92:93], v116 offset:28160
ds_read_b64_tr_b16 v[94:95], v116 offset:28416
v_add_u32 v116, 0xfffe8000, v116
v_mfma_f32_32x32x16_bf16 v[176:191], v[76:79], a[28:31], v[176:191]
s_add_u32 m0, s63, 71680
s_add_u32 s64, s62, 49920
buffer_load_dwordx4 v117, s[36:39], s64 offen offset:0 lds
s_waitcnt lgkmcnt(6)
v_mfma_f32_32x32x16_bf16 v[192:207], v[80:83], a[24:27], v[192:207]
s_add_u32 m0, s63, 72704
s_add_u32 s64, s62, 58112
buffer_load_dwordx4 v118, s[36:39], s64 offen offset:0 lds
v_mfma_f32_32x32x16_bf16 v[208:223], v[80:83], a[28:31], v[208:223]
s_waitcnt vmcnt(16)
s_waitcnt lgkmcnt(0)
s_barrier
v_mfma_f32_32x32x16_bf16 v[224:239], v[84:87], a[24:27], v[224:239]
ds_read_b128 a[0:3], v112 offset:0
ds_read_b128 a[4:7], v112 offset:4096
v_xor_b32 v112, 0x30000, v112
ds_read_b64_tr_b16 v[64:65], v116 offset:0
ds_read_b64_tr_b16 v[66:67], v116 offset:256
ds_read_b64_tr_b16 v[68:69], v116 offset:512
v_mfma_f32_32x32x16_bf16 v[240:255], v[84:87], a[28:31], v[240:255]
ds_read_b64_tr_b16 v[70:71], v116 offset:768
v_mfma_f32_32x32x16_bf16 a[192:207], v[88:91], a[24:27], a[192:207]
ds_read_b64_tr_b16 v[72:73], v116 offset:1024
ds_read_b64_tr_b16 v[74:75], v116 offset:1280
v_mfma_f32_32x32x16_bf16 a[208:223], v[88:91], a[28:31], a[208:223]
v_mfma_f32_32x32x16_bf16 a[224:239], v[92:95], a[24:27], a[224:239]
ds_read_b64_tr_b16 v[76:77], v116 offset:1536
ds_read_b64_tr_b16 v[78:79], v116 offset:1792
v_mfma_f32_32x32x16_bf16 a[240:255], v[92:95], a[28:31], a[240:255]
s_waitcnt lgkmcnt(6)
v_mfma_f32_32x32x16_bf16 v[0:15], v[64:67], a[0:3], v[0:15]
ds_read_b64_tr_b16 v[80:81], v116 offset:2048
ds_read_b64_tr_b16 v[82:83], v116 offset:2304
v_mfma_f32_32x32x16_bf16 v[16:31], v[64:67], a[4:7], v[16:31]
s_waitcnt lgkmcnt(6)
v_mfma_f32_32x32x16_bf16 v[32:47], v[68:71], a[0:3], v[32:47]
s_add_u32 m0, s63, 98304
s_add_u32 s65, s74, 3670016
buffer_load_dwordx4 v119, s[40:43], s65 offen offset:0 lds
ds_read_b64_tr_b16 v[84:85], v116 offset:2560
ds_read_b64_tr_b16 v[86:87], v116 offset:2816
v_mfma_f32_32x32x16_bf16 v[48:63], v[68:71], a[4:7], v[48:63]
s_waitcnt lgkmcnt(6)
v_mfma_f32_32x32x16_bf16 v[128:143], v[72:75], a[0:3], v[128:143]
ds_read_b64_tr_b16 v[88:89], v116 offset:3072
ds_read_b64_tr_b16 v[90:91], v116 offset:3328
v_mfma_f32_32x32x16_bf16 v[144:159], v[72:75], a[4:7], v[144:159]
s_waitcnt lgkmcnt(6)
v_mfma_f32_32x32x16_bf16 v[160:175], v[76:79], a[0:3], v[160:175]
s_add_u32 m0, s63, 99328
s_add_u32 s65, s74, 3670144
buffer_load_dwordx4 v119, s[40:43], s65 offen offset:0 lds
ds_read_b64_tr_b16 v[92:93], v116 offset:3584
ds_read_b64_tr_b16 v[94:95], v116 offset:3840
v_mfma_f32_32x32x16_bf16 v[176:191], v[76:79], a[4:7], v[176:191]
s_waitcnt lgkmcnt(6)
v_mfma_f32_32x32x16_bf16 v[192:207], v[80:83], a[0:3], v[192:207]
ds_read_b64_tr_b16 v[64:65], v116 offset:8192
ds_read_b64_tr_b16 v[66:67], v116 offset:8448
v_mfma_f32_32x32x16_bf16 v[208:223], v[80:83], a[4:7], v[208:223]
s_waitcnt lgkmcnt(6)
v_mfma_f32_32x32x16_bf16 v[224:239], v[84:87], a[0:3], v[224:239]
s_add_u32 m0, s63, 100352
s_add_u32 s65, s74, 3670272
buffer_load_dwordx4 v119, s[40:43], s65 offen offset:0 lds
ds_read_b128 a[8:11], v113 offset:0
ds_read_b128 a[12:15], v113 offset:4096
v_xor_b32 v113, 0x30000, v113
v_mfma_f32_32x32x16_bf16 v[240:255], v[84:87], a[4:7], v[240:255]
ds_read_b64_tr_b16 v[68:69], v116 offset:8704
ds_read_b64_tr_b16 v[70:71], v116 offset:8960
s_waitcnt lgkmcnt(8)
v_mfma_f32_32x32x16_bf16 a[192:207], v[88:91], a[0:3], a[192:207]
ds_read_b64_tr_b16 v[72:73], v116 offset:9216
ds_read_b64_tr_b16 v[74:75], v116 offset:9472
v_mfma_f32_32x32x16_bf16 a[208:223], v[88:91], a[4:7], a[208:223]
s_waitcnt lgkmcnt(8)
v_mfma_f32_32x32x16_bf16 a[224:239], v[92:95], a[0:3], a[224:239]
s_add_u32 m0, s63, 101376
s_add_u32 s65, s74, 3670400
buffer_load_dwordx4 v119, s[40:43], s65 offen offset:0 lds
ds_read_b64_tr_b16 v[76:77], v116 offset:9728
ds_read_b64_tr_b16 v[78:79], v116 offset:9984
v_mfma_f32_32x32x16_bf16 a[240:255], v[92:95], a[4:7], a[240:255]
s_waitcnt lgkmcnt(7)
v_mfma_f32_32x32x16_bf16 v[0:15], v[64:67], a[8:11], v[0:15]
ds_read_b64_tr_b16 v[80:81], v116 offset:10240
ds_read_b64_tr_b16 v[82:83], v116 offset:10496
s_waitcnt lgkmcnt(8)
v_mfma_f32_32x32x16_bf16 v[16:31], v[64:67], a[12:15], v[16:31]
s_waitcnt lgkmcnt(6)
v_mfma_f32_32x32x16_bf16 v[32:47], v[68:71], a[8:11], v[32:47]
s_add_u32 m0, s63, 102400
s_add_u32 s65, s74, 3735552
buffer_load_dwordx4 v119, s[40:43], s65 offen offset:0 lds
ds_read_b64_tr_b16 v[84:85], v116 offset:10752
ds_read_b64_tr_b16 v[86:87], v116 offset:11008
v_mfma_f32_32x32x16_bf16 v[48:63], v[68:71], a[12:15], v[48:63]
s_waitcnt lgkmcnt(6)
v_mfma_f32_32x32x16_bf16 v[128:143], v[72:75], a[8:11], v[128:143]
ds_read_b64_tr_b16 v[88:89], v116 offset:11264
ds_read_b64_tr_b16 v[90:91], v116 offset:11520
v_mfma_f32_32x32x16_bf16 v[144:159], v[72:75], a[12:15], v[144:159]
s_waitcnt lgkmcnt(6)
v_mfma_f32_32x32x16_bf16 v[160:175], v[76:79], a[8:11], v[160:175]
s_add_u32 m0, s63, 103424
s_add_u32 s65, s74, 3735680
buffer_load_dwordx4 v119, s[40:43], s65 offen offset:0 lds
ds_read_b64_tr_b16 v[92:93], v116 offset:11776
ds_read_b64_tr_b16 v[94:95], v116 offset:12032
v_mfma_f32_32x32x16_bf16 v[176:191], v[76:79], a[12:15], v[176:191]
s_waitcnt lgkmcnt(6)
v_mfma_f32_32x32x16_bf16 v[192:207], v[80:83], a[8:11], v[192:207]
ds_read_b64_tr_b16 v[64:65], v116 offset:16384
ds_read_b64_tr_b16 v[66:67], v116 offset:16640
v_mfma_f32_32x32x16_bf16 v[208:223], v[80:83], a[12:15], v[208:223]
s_waitcnt lgkmcnt(6)
v_mfma_f32_32x32x16_bf16 v[224:239], v[84:87], a[8:11], v[224:239]
s_add_u32 m0, s63, 104448
s_add_u32 s65, s74, 3735808
buffer_load_dwordx4 v119, s[40:43], s65 offen offset:0 lds
ds_read_b128 a[16:19], v114 offset:0
ds_read_b128 a[20:23], v114 offset:4096
v_xor_b32 v114, 0x30000, v114
v_mfma_f32_32x32x16_bf16 v[240:255], v[84:87], a[12:15], v[240:255]
ds_read_b64_tr_b16 v[68:69], v116 offset:16896
ds_read_b64_tr_b16 v[70:71], v116 offset:17152
s_waitcnt lgkmcnt(8)
v_mfma_f32_32x32x16_bf16 a[192:207], v[88:91], a[8:11], a[192:207]
ds_read_b64_tr_b16 v[72:73], v116 offset:17408
ds_read_b64_tr_b16 v[74:75], v116 offset:17664
v_mfma_f32_32x32x16_bf16 a[208:223], v[88:91], a[12:15], a[208:223]
s_waitcnt lgkmcnt(8)
v_mfma_f32_32x32x16_bf16 a[224:239], v[92:95], a[8:11], a[224:239]
s_add_u32 m0, s63, 105472
s_add_u32 s65, s74, 3735936
buffer_load_dwordx4 v119, s[40:43], s65 offen offset:0 lds
ds_read_b64_tr_b16 v[76:77], v116 offset:17920
ds_read_b64_tr_b16 v[78:79], v116 offset:18176
v_mfma_f32_32x32x16_bf16 a[240:255], v[92:95], a[12:15], a[240:255]
s_waitcnt lgkmcnt(7)
v_mfma_f32_32x32x16_bf16 v[0:15], v[64:67], a[16:19], v[0:15]
ds_read_b64_tr_b16 v[80:81], v116 offset:18432
ds_read_b64_tr_b16 v[82:83], v116 offset:18688
s_waitcnt lgkmcnt(8)
v_mfma_f32_32x32x16_bf16 v[16:31], v[64:67], a[20:23], v[16:31]
s_waitcnt lgkmcnt(6)
v_mfma_f32_32x32x16_bf16 v[32:47], v[68:71], a[16:19], v[32:47]
ds_read_b64_tr_b16 v[84:85], v116 offset:18944
ds_read_b64_tr_b16 v[86:87], v116 offset:19200
v_mfma_f32_32x32x16_bf16 v[48:63], v[68:71], a[20:23], v[48:63]
s_waitcnt lgkmcnt(6)
v_mfma_f32_32x32x16_bf16 v[128:143], v[72:75], a[16:19], v[128:143]
ds_read_b64_tr_b16 v[88:89], v116 offset:19456
ds_read_b64_tr_b16 v[90:91], v116 offset:19712
v_mfma_f32_32x32x16_bf16 v[144:159], v[72:75], a[20:23], v[144:159]
s_waitcnt lgkmcnt(6)
v_mfma_f32_32x32x16_bf16 v[160:175], v[76:79], a[16:19], v[160:175]
ds_read_b64_tr_b16 v[92:93], v116 offset:19968
ds_read_b64_tr_b16 v[94:95], v116 offset:20224
v_mfma_f32_32x32x16_bf16 v[176:191], v[76:79], a[20:23], v[176:191]
s_waitcnt lgkmcnt(6)
v_mfma_f32_32x32x16_bf16 v[192:207], v[80:83], a[16:19], v[192:207]
ds_read_b64_tr_b16 v[64:65], v116 offset:24576
ds_read_b64_tr_b16 v[66:67], v116 offset:24832
v_mfma_f32_32x32x16_bf16 v[208:223], v[80:83], a[20:23], v[208:223]
s_waitcnt lgkmcnt(6)
v_mfma_f32_32x32x16_bf16 v[224:239], v[84:87], a[16:19], v[224:239]
ds_read_b128 a[24:27], v115 offset:0
ds_read_b128 a[28:31], v115 offset:4096
v_xor_b32 v115, 0x30000, v115
ds_read_b64_tr_b16 v[68:69], v116 offset:25088
ds_read_b64_tr_b16 v[70:71], v116 offset:25344
v_mfma_f32_32x32x16_bf16 v[240:255], v[84:87], a[20:23], v[240:255]
s_waitcnt lgkmcnt(8)
v_mfma_f32_32x32x16_bf16 a[192:207], v[88:91], a[16:19], a[192:207]
ds_read_b64_tr_b16 v[72:73], v116 offset:25600
ds_read_b64_tr_b16 v[74:75], v116 offset:25856
v_mfma_f32_32x32x16_bf16 a[208:223], v[88:91], a[20:23], a[208:223]
s_waitcnt lgkmcnt(8)
v_mfma_f32_32x32x16_bf16 a[224:239], v[92:95], a[16:19], a[224:239]
ds_read_b64_tr_b16 v[76:77], v116 offset:26112
ds_read_b64_tr_b16 v[78:79], v116 offset:26368
v_mfma_f32_32x32x16_bf16 a[240:255], v[92:95], a[20:23], a[240:255]
s_waitcnt lgkmcnt(7)
v_mfma_f32_32x32x16_bf16 v[0:15], v[64:67], a[24:27], v[0:15]
ds_read_b64_tr_b16 v[80:81], v116 offset:26624
ds_read_b64_tr_b16 v[82:83], v116 offset:26880
s_waitcnt lgkmcnt(8)
v_mfma_f32_32x32x16_bf16 v[16:31], v[64:67], a[28:31], v[16:31]
s_add_u32 m0, s63, 131072
s_add_u32 s64, s62, 896
buffer_load_dwordx4 v117, s[36:39], s64 offen offset:0 lds
s_waitcnt lgkmcnt(6)
v_mfma_f32_32x32x16_bf16 v[32:47], v[68:71], a[24:27], v[32:47]
s_add_u32 m0, s63, 132096
s_add_u32 s64, s62, 9088
buffer_load_dwordx4 v118, s[36:39], s64 offen offset:0 lds
ds_read_b64_tr_b16 v[84:85], v116 offset:27136
ds_read_b64_tr_b16 v[86:87], v116 offset:27392
v_mfma_f32_32x32x16_bf16 v[48:63], v[68:71], a[28:31], v[48:63]
s_add_u32 m0, s63, 133120
s_add_u32 s64, s62, 17280
buffer_load_dwordx4 v117, s[36:39], s64 offen offset:0 lds
s_waitcnt lgkmcnt(6)
v_mfma_f32_32x32x16_bf16 v[128:143], v[72:75], a[24:27], v[128:143]
s_add_u32 m0, s63, 134144
s_add_u32 s64, s62, 25472
buffer_load_dwordx4 v118, s[36:39], s64 offen offset:0 lds
ds_read_b64_tr_b16 v[88:89], v116 offset:27648
ds_read_b64_tr_b16 v[90:91], v116 offset:27904
v_mfma_f32_32x32x16_bf16 v[144:159], v[72:75], a[28:31], v[144:159]
s_add_u32 m0, s63, 135168
s_add_u32 s64, s62, 33664
buffer_load_dwordx4 v117, s[36:39], s64 offen offset:0 lds
s_waitcnt lgkmcnt(6)
v_mfma_f32_32x32x16_bf16 v[160:175], v[76:79], a[24:27], v[160:175]
s_add_u32 m0, s63, 136192
s_add_u32 s64, s62, 41856
buffer_load_dwordx4 v118, s[36:39], s64 offen offset:0 lds
ds_read_b64_tr_b16 v[92:93], v116 offset:28160
ds_read_b64_tr_b16 v[94:95], v116 offset:28416
v_add_u32 v116, 0x8000, v116
v_mfma_f32_32x32x16_bf16 v[176:191], v[76:79], a[28:31], v[176:191]
s_add_u32 m0, s63, 137216
s_add_u32 s64, s62, 50048
buffer_load_dwordx4 v117, s[36:39], s64 offen offset:0 lds
s_waitcnt lgkmcnt(6)
v_mfma_f32_32x32x16_bf16 v[192:207], v[80:83], a[24:27], v[192:207]
s_add_u32 m0, s63, 138240
s_add_u32 s64, s62, 58240
buffer_load_dwordx4 v118, s[36:39], s64 offen offset:0 lds
v_mfma_f32_32x32x16_bf16 v[208:223], v[80:83], a[28:31], v[208:223]
s_waitcnt vmcnt(16)
s_waitcnt lgkmcnt(0)
s_barrier
v_mfma_f32_32x32x16_bf16 v[224:239], v[84:87], a[24:27], v[224:239]
ds_read_b128 a[0:3], v112 offset:0
ds_read_b128 a[4:7], v112 offset:4096
v_xor_b32 v112, 0x30000, v112
ds_read_b64_tr_b16 v[64:65], v116 offset:0
ds_read_b64_tr_b16 v[66:67], v116 offset:256
ds_read_b64_tr_b16 v[68:69], v116 offset:512
v_mfma_f32_32x32x16_bf16 v[240:255], v[84:87], a[28:31], v[240:255]
ds_read_b64_tr_b16 v[70:71], v116 offset:768
v_mfma_f32_32x32x16_bf16 a[192:207], v[88:91], a[24:27], a[192:207]
ds_read_b64_tr_b16 v[72:73], v116 offset:1024
ds_read_b64_tr_b16 v[74:75], v116 offset:1280
v_mfma_f32_32x32x16_bf16 a[208:223], v[88:91], a[28:31], a[208:223]
v_mfma_f32_32x32x16_bf16 a[224:239], v[92:95], a[24:27], a[224:239]
ds_read_b64_tr_b16 v[76:77], v116 offset:1536
ds_read_b64_tr_b16 v[78:79], v116 offset:1792
v_mfma_f32_32x32x16_bf16 a[240:255], v[92:95], a[28:31], a[240:255]
s_waitcnt lgkmcnt(6)
v_mfma_f32_32x32x16_bf16 v[0:15], v[64:67], a[0:3], v[0:15]
ds_read_b64_tr_b16 v[80:81], v116 offset:2048
ds_read_b64_tr_b16 v[82:83], v116 offset:2304
v_mfma_f32_32x32x16_bf16 v[16:31], v[64:67], a[4:7], v[16:31]
s_waitcnt lgkmcnt(6)
v_mfma_f32_32x32x16_bf16 v[32:47], v[68:71], a[0:3], v[32:47]
s_add_u32 m0, s61, 0
s_add_u32 s66, s61, 0
buffer_load_dwordx4 v120, s[44:47], s66 offen offset:0 nt lds
ds_read_b64_tr_b16 v[84:85], v116 offset:2560
ds_read_b64_tr_b16 v[86:87], v116 offset:2816
v_mfma_f32_32x32x16_bf16 v[48:63], v[68:71], a[4:7], v[48:63]
s_waitcnt lgkmcnt(6)
v_mfma_f32_32x32x16_bf16 v[128:143], v[72:75], a[0:3], v[128:143]
ds_read_b64_tr_b16 v[88:89], v116 offset:3072
ds_read_b64_tr_b16 v[90:91], v116 offset:3328
v_mfma_f32_32x32x16_bf16 v[144:159], v[72:75], a[4:7], v[144:159]
s_waitcnt lgkmcnt(6)
v_mfma_f32_32x32x16_bf16 v[160:175], v[76:79], a[0:3], v[160:175]
buffer_load_dwordx4 v121, s[44:47], s66 offen offset:1024 nt lds
ds_read_b64_tr_b16 v[92:93], v116 offset:3584
ds_read_b64_tr_b16 v[94:95], v116 offset:3840
v_mfma_f32_32x32x16_bf16 v[176:191], v[76:79], a[4:7], v[176:191]
s_waitcnt lgkmcnt(6)
v_mfma_f32_32x32x16_bf16 v[192:207], v[80:83], a[0:3], v[192:207]
ds_read_b64_tr_b16 v[64:65], v116 offset:8192
ds_read_b64_tr_b16 v[66:67], v116 offset:8448
v_mfma_f32_32x32x16_bf16 v[208:223], v[80:83], a[4:7], v[208:223]
s_waitcnt lgkmcnt(6)
v_mfma_f32_32x32x16_bf16 v[224:239], v[84:87], a[0:3], v[224:239]
buffer_load_dwordx4 v122, s[44:47], s66 offen offset:2048 nt lds
ds_read_b128 a[8:11], v113 offset:0
ds_read_b128 a[12:15], v113 offset:4096
v_xor_b32 v113, 0x30000, v113
ds_read_b64_tr_b16 v[68:69], v116 offset:8704
ds_read_b64_tr_b16 v[70:71], v116 offset:8960
v_mfma_f32_32x32x16_bf16 v[240:255], v[84:87], a[4:7], v[240:255]
s_waitcnt lgkmcnt(8)
v_mfma_f32_32x32x16_bf16 a[192:207], v[88:91], a[0:3], a[192:207]
ds_read_b64_tr_b16 v[72:73], v116 offset:9216
ds_read_b64_tr_b16 v[74:75], v116 offset:9472
v_mfma_f32_32x32x16_bf16 a[208:223], v[88:91], a[4:7], a[208:223]
s_waitcnt lgkmcnt(8)
v_mfma_f32_32x32x16_bf16 a[224:239], v[92:95], a[0:3], a[224:239]
buffer_load_dwordx4 v123, s[44:47], s66 offen offset:3072 nt lds
ds_read_b64_tr_b16 v[76:77], v116 offset:9728
ds_read_b64_tr_b16 v[78:79], v116 offset:9984
v_mfma_f32_32x32x16_bf16 a[240:255], v[92:95], a[4:7], a[240:255]
s_waitcnt lgkmcnt(7)
v_mfma_f32_32x32x16_bf16 v[0:15], v[64:67], a[8:11], v[0:15]
ds_read_b64_tr_b16 v[80:81], v116 offset:10240
ds_read_b64_tr_b16 v[82:83], v116 offset:10496
s_waitcnt lgkmcnt(8)
v_mfma_f32_32x32x16_bf16 v[16:31], v[64:67], a[12:15], v[16:31]
s_waitcnt lgkmcnt(6)
v_mfma_f32_32x32x16_bf16 v[32:47], v[68:71], a[8:11], v[32:47]
s_add_u32 m0, s61, 16384
s_nop 0
buffer_load_dwordx4 v124, s[48:51], s66 offen offset:0 nt lds
ds_read_b64_tr_b16 v[84:85], v116 offset:10752
ds_read_b64_tr_b16 v[86:87], v116 offset:11008
v_mfma_f32_32x32x16_bf16 v[48:63], v[68:71], a[12:15], v[48:63]
s_waitcnt lgkmcnt(6)
v_mfma_f32_32x32x16_bf16 v[128:143], v[72:75], a[8:11], v[128:143]
ds_read_b64_tr_b16 v[88:89], v116 offset:11264
ds_read_b64_tr_b16 v[90:91], v116 offset:11520
v_mfma_f32_32x32x16_bf16 v[144:159], v[72:75], a[12:15], v[144:159]
s_waitcnt lgkmcnt(6)
v_mfma_f32_32x32x16_bf16 v[160:175], v[76:79], a[8:11], v[160:175]
buffer_load_dwordx4 v125, s[48:51], s66 offen offset:1024 nt lds
ds_read_b64_tr_b16 v[92:93], v116 offset:11776
ds_read_b64_tr_b16 v[94:95], v116 offset:12032
v_mfma_f32_32x32x16_bf16 v[176:191], v[76:79], a[12:15], v[176:191]
s_waitcnt lgkmcnt(6)
v_mfma_f32_32x32x16_bf16 v[192:207], v[80:83], a[8:11], v[192:207]
ds_read_b64_tr_b16 v[64:65], v116 offset:16384
ds_read_b64_tr_b16 v[66:67], v116 offset:16640
v_mfma_f32_32x32x16_bf16 v[208:223], v[80:83], a[12:15], v[208:223]
s_waitcnt lgkmcnt(6)
v_mfma_f32_32x32x16_bf16 v[224:239], v[84:87], a[8:11], v[224:239]
buffer_load_dwordx4 v124, s[48:51], s66 offen offset:2048 nt lds
ds_read_b128 a[16:19], v114 offset:0
ds_read_b128 a[20:23], v114 offset:4096
v_xor_b32 v114, 0x30000, v114
ds_read_b64_tr_b16 v[68:69], v116 offset:16896
ds_read_b64_tr_b16 v[70:71], v116 offset:17152
v_mfma_f32_32x32x16_bf16 v[240:255], v[84:87], a[12:15], v[240:255]
s_waitcnt lgkmcnt(8)
v_mfma_f32_32x32x16_bf16 a[192:207], v[88:91], a[8:11], a[192:207]
ds_read_b64_tr_b16 v[72:73], v116 offset:17408
ds_read_b64_tr_b16 v[74:75], v116 offset:17664
v_mfma_f32_32x32x16_bf16 a[208:223], v[88:91], a[12:15], a[208:223]
s_waitcnt lgkmcnt(8)
v_mfma_f32_32x32x16_bf16 a[224:239], v[92:95], a[8:11], a[224:239]
buffer_load_dwordx4 v125, s[48:51], s66 offen offset:3072 nt lds
ds_read_b64_tr_b16 v[76:77], v116 offset:17920
ds_read_b64_tr_b16 v[78:79], v116 offset:18176
v_mfma_f32_32x32x16_bf16 a[240:255], v[92:95], a[12:15], a[240:255]
s_waitcnt lgkmcnt(7)
v_mfma_f32_32x32x16_bf16 v[0:15], v[64:67], a[16:19], v[0:15]
ds_read_b64_tr_b16 v[80:81], v116 offset:18432
ds_read_b64_tr_b16 v[82:83], v116 offset:18688
s_waitcnt lgkmcnt(8)
v_mfma_f32_32x32x16_bf16 v[16:31], v[64:67], a[20:23], v[16:31]
s_waitcnt lgkmcnt(6)
v_mfma_f32_32x32x16_bf16 v[32:47], v[68:71], a[16:19], v[32:47]
ds_read_b64_tr_b16 v[84:85], v116 offset:18944
ds_read_b64_tr_b16 v[86:87], v116 offset:19200
v_mfma_f32_32x32x16_bf16 v[48:63], v[68:71], a[20:23], v[48:63]
s_waitcnt lgkmcnt(6)
v_mfma_f32_32x32x16_bf16 v[128:143], v[72:75], a[16:19], v[128:143]
ds_read_b64_tr_b16 v[88:89], v116 offset:19456
ds_read_b64_tr_b16 v[90:91], v116 offset:19712
v_mfma_f32_32x32x16_bf16 v[144:159], v[72:75], a[20:23], v[144:159]
s_waitcnt lgkmcnt(6)
v_mfma_f32_32x32x16_bf16 v[160:175], v[76:79], a[16:19], v[160:175]
ds_read_b64_tr_b16 v[92:93], v116 offset:19968
ds_read_b64_tr_b16 v[94:95], v116 offset:20224
v_mfma_f32_32x32x16_bf16 v[176:191], v[76:79], a[20:23], v[176:191]
s_waitcnt lgkmcnt(6)
v_mfma_f32_32x32x16_bf16 v[192:207], v[80:83], a[16:19], v[192:207]
ds_read_b64_tr_b16 v[64:65], v116 offset:24576
ds_read_b64_tr_b16 v[66:67], v116 offset:24832
v_mfma_f32_32x32x16_bf16 v[208:223], v[80:83], a[20:23], v[208:223]
s_waitcnt lgkmcnt(6)
v_mfma_f32_32x32x16_bf16 v[224:239], v[84:87], a[16:19], v[224:239]
ds_read_b128 a[24:27], v115 offset:0
ds_read_b128 a[28:31], v115 offset:4096
v_xor_b32 v115, 0x30000, v115
ds_read_b64_tr_b16 v[68:69], v116 offset:25088
ds_read_b64_tr_b16 v[70:71], v116 offset:25344
v_mfma_f32_32x32x16_bf16 v[240:255], v[84:87], a[20:23], v[240:255]
s_waitcnt lgkmcnt(8)
v_mfma_f32_32x32x16_bf16 a[192:207], v[88:91], a[16:19], a[192:207]
ds_read_b64_tr_b16 v[72:73], v116 offset:25600
ds_read_b64_tr_b16 v[74:75], v116 offset:25856
v_mfma_f32_32x32x16_bf16 a[208:223], v[88:91], a[20:23], a[208:223]
s_waitcnt lgkmcnt(8)
v_mfma_f32_32x32x16_bf16 a[224:239], v[92:95], a[16:19], a[224:239]
ds_read_b64_tr_b16 v[76:77], v116 offset:26112
ds_read_b64_tr_b16 v[78:79], v116 offset:26368
v_mfma_f32_32x32x16_bf16 a[240:255], v[92:95], a[20:23], a[240:255]
s_waitcnt lgkmcnt(7)
v_mfma_f32_32x32x16_bf16 v[0:15], v[64:67], a[24:27], v[0:15]
ds_read_b64_tr_b16 v[80:81], v116 offset:26624
ds_read_b64_tr_b16 v[82:83], v116 offset:26880
s_waitcnt lgkmcnt(8)
v_mfma_f32_32x32x16_bf16 v[16:31], v[64:67], a[28:31], v[16:31]
s_waitcnt lgkmcnt(6)
v_mfma_f32_32x32x16_bf16 v[32:47], v[68:71], a[24:27], v[32:47]
ds_read_b64_tr_b16 v[84:85], v116 offset:27136
ds_read_b64_tr_b16 v[86:87], v116 offset:27392
v_mfma_f32_32x32x16_bf16 v[48:63], v[68:71], a[28:31], v[48:63]
s_waitcnt lgkmcnt(6)
v_mfma_f32_32x32x16_bf16 v[128:143], v[72:75], a[24:27], v[128:143]
ds_read_b64_tr_b16 v[88:89], v116 offset:27648
ds_read_b64_tr_b16 v[90:91], v116 offset:27904
v_mfma_f32_32x32x16_bf16 v[144:159], v[72:75], a[28:31], v[144:159]
s_waitcnt lgkmcnt(6)
v_mfma_f32_32x32x16_bf16 v[160:175], v[76:79], a[24:27], v[160:175]
ds_read_b64_tr_b16 v[92:93], v116 offset:28160
ds_read_b64_tr_b16 v[94:95], v116 offset:28416
v_add_u32 v116, 0x10000, v116
v_mfma_f32_32x32x16_bf16 v[176:191], v[76:79], a[28:31], v[176:191]
s_waitcnt lgkmcnt(6)
v_mfma_f32_32x32x16_bf16 v[192:207], v[80:83], a[24:27], v[192:207]
v_mfma_f32_32x32x16_bf16 v[208:223], v[80:83], a[28:31], v[208:223]
s_waitcnt vmcnt(8)
s_waitcnt lgkmcnt(0)
s_barrier
v_mfma_f32_32x32x16_bf16 v[224:239], v[84:87], a[24:27], v[224:239]
ds_read_b128 a[0:3], v112 offset:0
ds_read_b128 a[4:7], v112 offset:4096
ds_read_b64_tr_b16 v[64:65], v116 offset:0
ds_read_b64_tr_b16 v[66:67], v116 offset:256
ds_read_b64_tr_b16 v[68:69], v116 offset:8192
ds_read_b64_tr_b16 v[70:71], v116 offset:8448
v_mfma_f32_32x32x16_bf16 v[240:255], v[84:87], a[28:31], v[240:255]
v_mfma_f32_32x32x16_bf16 a[192:207], v[88:91], a[24:27], a[192:207]
ds_read_b128 a[8:11], v113 offset:0
ds_read_b128 a[12:15], v113 offset:4096
ds_read_b64_tr_b16 v[72:73], v116 offset:16384
ds_read_b64_tr_b16 v[74:75], v116 offset:16640
v_mfma_f32_32x32x16_bf16 a[208:223], v[88:91], a[28:31], a[208:223]
v_mfma_f32_32x32x16_bf16 a[224:239], v[92:95], a[24:27], a[224:239]
ds_read_b128 a[16:19], v114 offset:0
ds_read_b128 a[20:23], v114 offset:4096
ds_read_b64_tr_b16 v[76:77], v116 offset:24576
ds_read_b64_tr_b16 v[78:79], v116 offset:24832
v_mfma_f32_32x32x16_bf16 a[240:255], v[92:95], a[28:31], a[240:255]
s_waitcnt lgkmcnt(10)
v_mfma_f32_32x32x16_bf16 v[0:15], v[64:67], a[0:3], v[0:15]
ds_read_b128 a[24:27], v115 offset:0
ds_read_b128 a[28:31], v115 offset:4096
ds_read_b64_tr_b16 v[80:81], v116 offset:512
ds_read_b64_tr_b16 v[82:83], v116 offset:768
v_mfma_f32_32x32x16_bf16 v[16:31], v[64:67], a[4:7], v[16:31]
s_waitcnt lgkmcnt(11)
v_mfma_f32_32x32x16_bf16 v[0:15], v[68:71], a[8:11], v[0:15]
s_add_u32 m0, s61, 32768
s_add_u32 s66, s61, 16384
buffer_load_dwordx4 v120, s[44:47], s66 offen offset:0 nt lds
ds_read_b64_tr_b16 v[84:85], v116 offset:8704
ds_read_b64_tr_b16 v[86:87], v116 offset:8960
s_waitcnt lgkmcnt(12)
v_mfma_f32_32x32x16_bf16 v[16:31], v[68:71], a[12:15], v[16:31]
s_waitcnt lgkmcnt(9)
v_mfma_f32_32x32x16_bf16 v[0:15], v[72:75], a[16:19], v[0:15]
buffer_load_dwordx4 v121, s[44:47], s66 offen offset:1024 nt lds
ds_read_b64_tr_b16 v[88:89], v116 offset:16896
ds_read_b64_tr_b16 v[90:91], v116 offset:17152
s_waitcnt lgkmcnt(10)
v_mfma_f32_32x32x16_bf16 v[16:31], v[72:75], a[20:23], v[16:31]
s_waitcnt lgkmcnt(7)
v_mfma_f32_32x32x16_bf16 v[0:15], v[76:79], a[24:27], v[0:15]
buffer_load_dwordx4 v122, s[44:47], s66 offen offset:2048 nt lds
ds_read_b64_tr_b16 v[92:93], v116 offset:25088
ds_read_b64_tr_b16 v[94:95], v116 offset:25344
s_waitcnt lgkmcnt(8)
v_mfma_f32_32x32x16_bf16 v[16:31], v[76:79], a[28:31], v[16:31]
s_waitcnt lgkmcnt(6)
v_mfma_f32_32x32x16_bf16 v[32:47], v[80:83], a[0:3], v[32:47]
buffer_load_dwordx4 v123, s[44:47], s66 offen offset:3072 nt lds
ds_read_b64_tr_b16 v[64:65], v116 offset:1024
ds_read_b64_tr_b16 v[66:67], v116 offset:1280
v_mfma_f32_32x32x16_bf16 v[48:63], v[80:83], a[4:7], v[48:63]
s_waitcnt lgkmcnt(6)
v_mfma_f32_32x32x16_bf16 v[32:47], v[84:87], a[8:11], v[32:47]
s_add_u32 m0, s61, 49152
s_nop 0
buffer_load_dwordx4 v124, s[48:51], s66 offen offset:0 nt lds
ds_read_b64_tr_b16 v[68:69], v116 offset:9216
ds_read_b64_tr_b16 v[70:71], v116 offset:9472
v_mul_f32 v0, s68, v0
v_mfma_f32_32x32x16_bf16 v[48:63], v[84:87], a[12:15], v[48:63]
v_mul_f32 v1, s68, v1
v_cvt_pk_bf16_f32 v0, v0, v1
v_accvgpr_write_b32 a128, v0
v_mul_f32 v2, s68, v2
v_mul_f32 v3, s68, v3
s_waitcnt lgkmcnt(6)
v_mfma_f32_32x32x16_bf16 v[32:47], v[88:91], a[16:19], v[32:47]
buffer_load_dwordx4 v125, s[48:51], s66 offen offset:1024 nt lds
ds_read_b64_tr_b16 v[72:73], v116 offset:17408
ds_read_b64_tr_b16 v[74:75], v116 offset:17664
v_cvt_pk_bf16_f32 v2, v2, v3
v_accvgpr_write_b32 a129, v2
v_mul_f32 v4, s68, v4
v_mfma_f32_32x32x16_bf16 v[48:63], v[88:91], a[20:23], v[48:63]
v_mul_f32 v5, s68, v5
v_cvt_pk_bf16_f32 v4, v4, v5
v_accvgpr_write_b32 a130, v4
v_mul_f32 v6, s68, v6
v_mul_f32 v7, s68, v7
s_waitcnt lgkmcnt(6)
v_mfma_f32_32x32x16_bf16 v[32:47], v[92:95], a[24:27], v[32:47]
buffer_load_dwordx4 v124, s[48:51], s66 offen offset:2048 nt lds
ds_read_b64_tr_b16 v[76:77], v116 offset:25600
ds_read_b64_tr_b16 v[78:79], v116 offset:25856
v_cvt_pk_bf16_f32 v6, v6, v7
v_accvgpr_write_b32 a131, v6
v_mul_f32 v8, s68, v8
v_mfma_f32_32x32x16_bf16 v[48:63], v[92:95], a[28:31], v[48:63]
v_mul_f32 v9, s68, v9
v_cvt_pk_bf16_f32 v8, v8, v9
v_accvgpr_write_b32 a132, v8
v_mul_f32 v10, s68, v10
v_mul_f32 v11, s68, v11
s_waitcnt lgkmcnt(6)
v_mfma_f32_32x32x16_bf16 v[128:143], v[64:67], a[0:3], v[128:143]
buffer_load_dwordx4 v125, s[48:51], s66 offen offset:3072 nt lds
ds_read_b64_tr_b16 v[80:81], v116 offset:1536
ds_read_b64_tr_b16 v[82:83], v116 offset:1792
v_cvt_pk_bf16_f32 v10, v10, v11
v_accvgpr_write_b32 a133, v10
v_mul_f32 v12, s68, v12
v_mfma_f32_32x32x16_bf16 v[144:159], v[64:67], a[4:7], v[144:159]
v_mul_f32 v13, s68, v13
v_cvt_pk_bf16_f32 v12, v12, v13
v_accvgpr_write_b32 a134, v12
v_mul_f32 v14, s68, v14
v_mul_f32 v15, s68, v15
s_waitcnt lgkmcnt(6)
v_mfma_f32_32x32x16_bf16 v[128:143], v[68:71], a[8:11], v[128:143]
s_add_u32 m0, s61, 65536
s_add_u32 s66, s61, 32768
buffer_load_dwordx4 v120, s[44:47], s66 offen offset:0 nt lds
ds_read_b64_tr_b16 v[84:85], v116 offset:9728
ds_read_b64_tr_b16 v[86:87], v116 offset:9984
v_cvt_pk_bf16_f32 v14, v14, v15
v_mfma_f32_32x32x16_bf16 v[144:159], v[68:71], a[12:15], v[144:159]
v_accvgpr_write_b32 a135, v14
v_mul_f32 v16, s68, v16
v_mul_f32 v17, s68, v17
v_cvt_pk_bf16_f32 v16, v16, v17
v_accvgpr_write_b32 a160, v16
s_waitcnt lgkmcnt(6)
v_mfma_f32_32x32x16_bf16 v[128:143], v[72:75], a[16:19], v[128:143]
buffer_load_dwordx4 v121, s[44:47], s66 offen offset:1024 nt lds
ds_read_b64_tr_b16 v[88:89], v116 offset:17920
ds_read_b64_tr_b16 v[90:91], v116 offset:18176
v_mul_f32 v18, s68, v18
v_mul_f32 v19, s68, v19
v_cvt_pk_bf16_f32 v18, v18, v19
v_mfma_f32_32x32x16_bf16 v[144:159], v[72:75], a[20:23], v[144:159]
v_accvgpr_write_b32 a161, v18
v_mul_f32 v20, s68, v20
v_mul_f32 v21, s68, v21
v_cvt_pk_bf16_f32 v20, v20, v21
v_accvgpr_write_b32 a162, v20
s_waitcnt lgkmcnt(6)
v_mfma_f32_32x32x16_bf16 v[128:143], v[76:79], a[24:27], v[128:143]
buffer_load_dwordx4 v122, s[44:47], s66 offen offset:2048 nt lds
ds_read_b64_tr_b16 v[92:93], v116 offset:26112
ds_read_b64_tr_b16 v[94:95], v116 offset:26368
v_mul_f32 v22, s68, v22
v_mul_f32 v23, s68, v23
v_cvt_pk_bf16_f32 v22, v22, v23
v_mfma_f32_32x32x16_bf16 v[144:159], v[76:79], a[28:31], v[144:159]
v_accvgpr_write_b32 a163, v22
v_mul_f32 v24, s68, v24
v_mul_f32 v25, s68, v25
v_cvt_pk_bf16_f32 v24, v24, v25
v_accvgpr_write_b32 a164, v24
s_waitcnt lgkmcnt(6)
v_mfma_f32_32x32x16_bf16 v[160:175], v[80:83], a[0:3], v[160:175]
buffer_load_dwordx4 v123, s[44:47], s66 offen offset:3072 nt lds
ds_read_b64_tr_b16 v[64:65], v116 offset:2048
ds_read_b64_tr_b16 v[66:67], v116 offset:2304
v_mul_f32 v26, s68, v26
v_mul_f32 v27, s68, v27
v_cvt_pk_bf16_f32 v26, v26, v27
v_mfma_f32_32x32x16_bf16 v[176:191], v[80:83], a[4:7], v[176:191]
v_accvgpr_write_b32 a165, v26
v_mul_f32 v28, s68, v28
v_mul_f32 v29, s68, v29
v_cvt_pk_bf16_f32 v28, v28, v29
v_accvgpr_write_b32 a166, v28
s_waitcnt lgkmcnt(6)
v_mfma_f32_32x32x16_bf16 v[160:175], v[84:87], a[8:11], v[160:175]
s_add_u32 m0, s61, 81920
s_nop 0
buffer_load_dwordx4 v124, s[48:51], s66 offen offset:0 nt lds
ds_read_b64_tr_b16 v[68:69], v116 offset:2560
ds_read_b64_tr_b16 v[70:71], v116 offset:2816
v_mul_f32 v30, s68, v30
v_mfma_f32_32x32x16_bf16 v[176:191], v[84:87], a[12:15], v[176:191]
v_mul_f32 v31, s68, v31
v_cvt_pk_bf16_f32 v30, v30, v31
v_accvgpr_write_b32 a167, v30
v_mul_f32 v32, s68, v32
v_mul_f32 v33, s68, v33
s_waitcnt lgkmcnt(6)
v_mfma_f32_32x32x16_bf16 v[160:175], v[88:91], a[16:19], v[160:175]
buffer_load_dwordx4 v125, s[48:51], s66 offen offset:1024 nt lds
ds_read_b64_tr_b16 v[72:73], v116 offset:3072
ds_read_b64_tr_b16 v[74:75], v116 offset:3328
v_cvt_pk_bf16_f32 v32, v32, v33
v_accvgpr_write_b32 a136, v32
v_mul_f32 v34, s68, v34
v_mfma_f32_32x32x16_bf16 v[176:191], v[88:91], a[20:23], v[176:191]
v_mul_f32 v35, s68, v35
v_cvt_pk_bf16_f32 v34, v34, v35
v_accvgpr_write_b32 a137, v34
v_mul_f32 v36, s68, v36
v_mul_f32 v37, s68, v37
s_waitcnt lgkmcnt(6)
v_mfma_f32_32x32x16_bf16 v[160:175], v[92:95], a[24:27], v[160:175]
buffer_load_dwordx4 v124, s[48:51], s66 offen offset:2048 nt lds
ds_read_b64_tr_b16 v[76:77], v116 offset:3584
ds_read_b64_tr_b16 v[78:79], v116 offset:3840
v_cvt_pk_bf16_f32 v36, v36, v37
v_accvgpr_write_b32 a138, v36
v_mul_f32 v38, s68, v38
v_mfma_f32_32x32x16_bf16 v[176:191], v[92:95], a[28:31], v[176:191]
v_mul_f32 v39, s68, v39
v_cvt_pk_bf16_f32 v38, v38, v39
v_accvgpr_write_b32 a139, v38
v_mul_f32 v40, s68, v40
v_mul_f32 v41, s68, v41
s_waitcnt lgkmcnt(6)
v_mfma_f32_32x32x16_bf16 v[192:207], v[64:67], a[0:3], v[192:207]
buffer_load_dwordx4 v125, s[48:51], s66 offen offset:3072 nt lds
ds_read_b64_tr_b16 v[80:81], v116 offset:10240
ds_read_b64_tr_b16 v[82:83], v116 offset:10496
v_cvt_pk_bf16_f32 v40, v40, v41
v_accvgpr_write_b32 a140, v40
v_mul_f32 v42, s68, v42
v_mfma_f32_32x32x16_bf16 v[208:223], v[64:67], a[4:7], v[208:223]
v_mul_f32 v43, s68, v43
v_cvt_pk_bf16_f32 v42, v42, v43
v_accvgpr_write_b32 a141, v42
v_mul_f32 v44, s68, v44
v_mul_f32 v45, s68, v45
s_waitcnt lgkmcnt(6)
v_mfma_f32_32x32x16_bf16 v[224:239], v[68:71], a[0:3], v[224:239]
ds_read_b64_tr_b16 v[84:85], v116 offset:10752
ds_read_b64_tr_b16 v[86:87], v116 offset:11008
v_cvt_pk_bf16_f32 v44, v44, v45
v_accvgpr_write_b32 a142, v44
v_mul_f32 v46, s68, v46
v_mul_f32 v47, s68, v47
v_mfma_f32_32x32x16_bf16 v[240:255], v[68:71], a[4:7], v[240:255]
v_cvt_pk_bf16_f32 v46, v46, v47
v_accvgpr_write_b32 a143, v46
v_mul_f32 v48, s68, v48
v_mul_f32 v49, s68, v49
v_cvt_pk_bf16_f32 v48, v48, v49
s_waitcnt lgkmcnt(6)
v_mfma_f32_32x32x16_bf16 a[192:207], v[72:75], a[0:3], a[192:207]
s_waitcnt vmcnt(16)
s_barrier
ds_read_b64_tr_b16 v[88:89], v116 offset:11264
ds_read_b64_tr_b16 v[90:91], v116 offset:11520
v_accvgpr_write_b32 a168, v48
v_mul_f32 v50, s68, v50
v_mfma_f32_32x32x16_bf16 a[208:223], v[72:75], a[4:7], a[208:223]
v_mul_f32 v51, s68, v51
v_cvt_pk_bf16_f32 v50, v50, v51
v_accvgpr_write_b32 a169, v50
v_mul_f32 v52, s68, v52
v_mul_f32 v53, s68, v53
s_waitcnt lgkmcnt(6)
v_mfma_f32_32x32x16_bf16 a[224:239], v[76:79], a[0:3], a[224:239]
ds_read_b64_tr_b16 v[92:93], v116 offset:11776
ds_read_b64_tr_b16 v[94:95], v116 offset:12032
v_cvt_pk_bf16_f32 v52, v52, v53
v_accvgpr_write_b32 a170, v52
v_mul_f32 v54, s68, v54
v_mul_f32 v55, s68, v55
v_mfma_f32_32x32x16_bf16 a[240:255], v[76:79], a[4:7], a[240:255]
v_cvt_pk_bf16_f32 v54, v54, v55
v_accvgpr_write_b32 a171, v54
v_mul_f32 v56, s68, v56
v_mul_f32 v57, s68, v57
v_cvt_pk_bf16_f32 v56, v56, v57
s_waitcnt lgkmcnt(6)
v_mfma_f32_32x32x16_bf16 v[192:207], v[80:83], a[8:11], v[192:207]
ds_read_b128 v[64:67], v96 offset:0
v_accvgpr_write_b32 a172, v56
v_mul_f32 v58, s68, v58
v_mul_f32 v59, s68, v59
v_cvt_pk_bf16_f32 v58, v58, v59
v_accvgpr_write_b32 a173, v58
v_mfma_f32_32x32x16_bf16 v[208:223], v[80:83], a[12:15], v[208:223]
v_mul_f32 v60, s68, v60
v_mul_f32 v61, s68, v61
v_cvt_pk_bf16_f32 v60, v60, v61
v_accvgpr_write_b32 a174, v60
v_mul_f32 v62, s68, v62
s_waitcnt lgkmcnt(5)
v_mfma_f32_32x32x16_bf16 v[224:239], v[84:87], a[8:11], v[224:239]
ds_read_b128 v[68:71], v97 offset:0
v_mul_f32 v63, s68, v63
v_cvt_pk_bf16_f32 v62, v62, v63
v_accvgpr_write_b32 a175, v62
v_mul_f32 v128, s68, v128
v_mul_f32 v129, s68, v129
v_mfma_f32_32x32x16_bf16 v[240:255], v[84:87], a[12:15], v[240:255]
v_cvt_pk_bf16_f32 v128, v128, v129
v_accvgpr_write_b32 a144, v128
v_mul_f32 v130, s68, v130
v_mul_f32 v131, s68, v131
v_cvt_pk_bf16_f32 v130, v130, v131
s_waitcnt lgkmcnt(4)
v_mfma_f32_32x32x16_bf16 a[192:207], v[88:91], a[8:11], a[192:207]
ds_read_b128 v[72:75], v98 offset:0
v_accvgpr_write_b32 a145, v130
v_mul_f32 v132, s68, v132
v_mul_f32 v133, s68, v133
v_cvt_pk_bf16_f32 v132, v132, v133
v_accvgpr_write_b32 a146, v132
v_mfma_f32_32x32x16_bf16 a[208:223], v[88:91], a[12:15], a[208:223]
v_mul_f32 v134, s68, v134
v_mul_f32 v135, s68, v135
v_cvt_pk_bf16_f32 v134, v134, v135
v_accvgpr_write_b32 a147, v134
v_mul_f32 v136, s68, v136
s_waitcnt lgkmcnt(3)
v_mfma_f32_32x32x16_bf16 a[224:239], v[92:95], a[8:11], a[224:239]
ds_read_b128 v[76:79], v99 offset:0
v_mul_f32 v137, s68, v137
v_cvt_pk_bf16_f32 v136, v136, v137
v_accvgpr_write_b32 a148, v136
v_mul_f32 v138, s68, v138
v_mul_f32 v139, s68, v139
v_mfma_f32_32x32x16_bf16 a[240:255], v[92:95], a[12:15], a[240:255]
v_cvt_pk_bf16_f32 v138, v138, v139
v_accvgpr_write_b32 a149, v138
v_mul_f32 v140, s68, v140
v_mul_f32 v141, s68, v141
v_cvt_pk_bf16_f32 v140, v140, v141
s_waitcnt lgkmcnt(3)
v_mfma_f32_32x32x16_bf16 v[0:15], v[64:67], a[128:131], 0
ds_read_b128 v[80:83], v100 offset:0
v_accvgpr_write_b32 a150, v140
v_mul_f32 v142, s68, v142
v_mul_f32 v143, s68, v143
v_cvt_pk_bf16_f32 v142, v142, v143
v_accvgpr_write_b32 a151, v142
v_mfma_f32_32x32x16_bf16 v[16:31], v[64:67], a[160:163], 0
v_mul_f32 v144, s68, v144
v_mul_f32 v145, s68, v145
v_cvt_pk_bf16_f32 v144, v144, v145
v_accvgpr_write_b32 a176, v144
v_mul_f32 v146, s68, v146
s_waitcnt lgkmcnt(3)
v_mfma_f32_32x32x16_bf16 v[0:15], v[68:71], a[132:135], v[0:15]
ds_read_b128 v[84:87], v101 offset:0
v_mul_f32 v147, s68, v147
v_cvt_pk_bf16_f32 v146, v146, v147
v_accvgpr_write_b32 a177, v146
v_mul_f32 v148, s68, v148
v_mul_f32 v149, s68, v149
v_mfma_f32_32x32x16_bf16 v[16:31], v[68:71], a[164:167], v[16:31]
v_cvt_pk_bf16_f32 v148, v148, v149
v_accvgpr_write_b32 a178, v148
v_mul_f32 v150, s68, v150
v_mul_f32 v151, s68, v151
v_cvt_pk_bf16_f32 v150, v150, v151
s_waitcnt lgkmcnt(3)
v_mfma_f32_32x32x16_bf16 v[0:15], v[72:75], a[136:139], v[0:15]
ds_read_b128 v[88:91], v102 offset:0
v_accvgpr_write_b32 a179, v150
v_mul_f32 v152, s68, v152
v_mul_f32 v153, s68, v153
v_cvt_pk_bf16_f32 v152, v152, v153
v_accvgpr_write_b32 a180, v152
v_mfma_f32_32x32x16_bf16 v[16:31], v[72:75], a[168:171], v[16:31]
v_mul_f32 v154, s68, v154
v_mul_f32 v155, s68, v155
v_cvt_pk_bf16_f32 v154, v154, v155
v_accvgpr_write_b32 a181, v154
v_mul_f32 v156, s68, v156
v_mul_f32 v157, s68, v157
v_cvt_pk_bf16_f32 v156, v156, v157
v_accvgpr_write_b32 a182, v156
v_mul_f32 v158, s68, v158
v_mul_f32 v159, s68, v159
v_cvt_pk_bf16_f32 v158, v158, v159
v_accvgpr_write_b32 a183, v158
s_waitcnt lgkmcnt(3)
v_mfma_f32_32x32x16_bf16 v[0:15], v[76:79], a[140:143], v[0:15]
ds_read_b128 v[92:95], v103 offset:0
v_mul_f32 v160, s68, v160
v_mul_f32 v161, s68, v161
v_cvt_pk_bf16_f32 v160, v160, v161
v_accvgpr_write_b32 a152, v160
v_mul_f32 v162, s68, v162
v_mfma_f32_32x32x16_bf16 v[16:31], v[76:79], a[172:175], v[16:31]
v_mul_f32 v163, s68, v163
v_cvt_pk_bf16_f32 v162, v162, v163
v_accvgpr_write_b32 a153, v162
v_mul_f32 v164, s68, v164
v_mul_f32 v165, s68, v165
s_waitcnt lgkmcnt(3)
v_mfma_f32_32x32x16_bf16 v[0:15], v[80:83], a[144:147], v[0:15]
ds_read_b64_tr_b16 v[64:65], v116 offset:18432
ds_read_b64_tr_b16 v[66:67], v116 offset:18688
v_cvt_pk_bf16_f32 v164, v164, v165
v_accvgpr_write_b32 a154, v164
v_mul_f32 v166, s68, v166
v_mul_f32 v167, s68, v167
v_mfma_f32_32x32x16_bf16 v[16:31], v[80:83], a[176:179], v[16:31]
v_cvt_pk_bf16_f32 v166, v166, v167
v_accvgpr_write_b32 a155, v166
v_mul_f32 v168, s68, v168
v_mul_f32 v169, s68, v169
v_cvt_pk_bf16_f32 v168, v168, v169
v_accvgpr_write_b32 a156, v168
v_mul_f32 v170, s68, v170
v_mul_f32 v171, s68, v171
v_cvt_pk_bf16_f32 v170, v170, v171
v_accvgpr_write_b32 a157, v170
v_mul_f32 v172, s68, v172
v_mul_f32 v173, s68, v173
v_cvt_pk_bf16_f32 v172, v172, v173
v_accvgpr_write_b32 a158, v172
v_mul_f32 v174, s68, v174
v_mul_f32 v175, s68, v175
v_cvt_pk_bf16_f32 v174, v174, v175
v_accvgpr_write_b32 a159, v174
v_mul_f32 v176, s68, v176
v_mul_f32 v177, s68, v177
v_cvt_pk_bf16_f32 v176, v176, v177
v_accvgpr_write_b32 a184, v176
v_mul_f32 v178, s68, v178
v_mul_f32 v179, s68, v179
v_cvt_pk_bf16_f32 v178, v178, v179
v_accvgpr_write_b32 a185, v178
v_mul_f32 v180, s68, v180
v_mul_f32 v181, s68, v181
v_cvt_pk_bf16_f32 v180, v180, v181
v_accvgpr_write_b32 a186, v180
v_mul_f32 v182, s68, v182
v_mul_f32 v183, s68, v183
v_cvt_pk_bf16_f32 v182, v182, v183
v_accvgpr_write_b32 a187, v182
v_mul_f32 v184, s68, v184
v_mul_f32 v185, s68, v185
v_cvt_pk_bf16_f32 v184, v184, v185
v_accvgpr_write_b32 a188, v184
v_mul_f32 v186, s68, v186
v_mul_f32 v187, s68, v187
v_cvt_pk_bf16_f32 v186, v186, v187
v_accvgpr_write_b32 a189, v186
v_mul_f32 v188, s68, v188
v_mul_f32 v189, s68, v189
v_cvt_pk_bf16_f32 v188, v188, v189
v_accvgpr_write_b32 a190, v188
v_mul_f32 v190, s68, v190
v_mul_f32 v191, s68, v191
v_cvt_pk_bf16_f32 v190, v190, v191
v_accvgpr_write_b32 a191, v190
s_waitcnt lgkmcnt(4)
v_mfma_f32_32x32x16_bf16 v[0:15], v[84:87], a[148:151], v[0:15]
ds_read_b64_tr_b16 v[68:69], v116 offset:18944
ds_read_b64_tr_b16 v[70:71], v116 offset:19200
v_mov_b32 v128, 0
v_mov_b32 v129, 0
v_mov_b32 v130, 0
v_mov_b32 v131, 0
v_mfma_f32_32x32x16_bf16 v[16:31], v[84:87], a[180:183], v[16:31]
v_mov_b32 v132, 0
v_mov_b32 v133, 0
v_mov_b32 v134, 0
v_mov_b32 v135, 0
v_mov_b32 v136, 0
s_waitcnt lgkmcnt(5)
v_mfma_f32_32x32x16_bf16 v[0:15], v[88:91], a[152:155], v[0:15]
ds_read_b64_tr_b16 v[72:73], v116 offset:19456
ds_read_b64_tr_b16 v[74:75], v116 offset:19712
v_mov_b32 v137, 0
v_mov_b32 v138, 0
v_mov_b32 v139, 0
v_mov_b32 v140, 0
v_mfma_f32_32x32x16_bf16 v[16:31], v[88:91], a[184:187], v[16:31]
v_mov_b32 v141, 0
v_mov_b32 v142, 0
v_mov_b32 v143, 0
v_mov_b32 v144, 0
v_mov_b32 v145, 0
s_waitcnt lgkmcnt(6)
v_mfma_f32_32x32x16_bf16 v[0:15], v[92:95], a[156:159], v[0:15]
ds_read_b64_tr_b16 v[76:77], v116 offset:19968
ds_read_b64_tr_b16 v[78:79], v116 offset:20224
v_mov_b32 v146, 0
v_mov_b32 v147, 0
v_mov_b32 v148, 0
v_mov_b32 v149, 0
v_mfma_f32_32x32x16_bf16 v[16:31], v[92:95], a[188:191], v[16:31]
v_mov_b32 v150, 0
v_mov_b32 v151, 0
v_mov_b32 v152, 0
v_mov_b32 v153, 0
v_mov_b32 v154, 0
s_waitcnt lgkmcnt(6)
v_mfma_f32_32x32x16_bf16 v[192:207], v[64:67], a[16:19], v[192:207]
ds_read_b64_tr_b16 v[80:81], v116 offset:26624
ds_read_b64_tr_b16 v[82:83], v116 offset:26880
v_mov_b32 v155, 0
v_mov_b32 v156, 0
v_mov_b32 v157, 0
v_mov_b32 v158, 0
v_mfma_f32_32x32x16_bf16 v[208:223], v[64:67], a[20:23], v[208:223]
v_mov_b32 v159, 0
s_waitcnt lgkmcnt(6)
v_mfma_f32_32x32x16_bf16 v[224:239], v[68:71], a[16:19], v[224:239]
ds_read_b64_tr_b16 v[84:85], v116 offset:27136
ds_read_b64_tr_b16 v[86:87], v116 offset:27392
v_exp_f32 v0, v0
v_exp_f32 v16, v16
v_mfma_f32_32x32x16_bf16 v[240:255], v[68:71], a[20:23], v[240:255]
v_exp_f32 v1, v1
v_exp_f32 v17, v17
s_waitcnt lgkmcnt(6)
v_mfma_f32_32x32x16_bf16 a[192:207], v[72:75], a[16:19], a[192:207]
ds_read_b64_tr_b16 v[88:89], v116 offset:27648
ds_read_b64_tr_b16 v[90:91], v116 offset:27904
v_exp_f32 v2, v2
v_exp_f32 v18, v18
v_mfma_f32_32x32x16_bf16 a[208:223], v[72:75], a[20:23], a[208:223]
v_add_f32 v108, v0, v1
v_add_f32 v109, v16, v17
v_cvt_pk_bf16_f32 v0, v0, v1
v_cvt_pk_bf16_f32 v16, v16, v17
s_waitcnt lgkmcnt(6)
v_mfma_f32_32x32x16_bf16 a[224:239], v[76:79], a[16:19], a[224:239]
ds_read_b64_tr_b16 v[92:93], v116 offset:28160
ds_read_b64_tr_b16 v[94:95], v116 offset:28416
v_exp_f32 v3, v3
v_exp_f32 v19, v19
v_mfma_f32_32x32x16_bf16 a[240:255], v[76:79], a[20:23], a[240:255]
v_add_f32 v108, v108, v2
v_add_f32 v109, v109, v18
v_exp_f32 v4, v4
s_waitcnt lgkmcnt(6)
v_mfma_f32_32x32x16_bf16 v[192:207], v[80:83], a[24:27], v[192:207]
ds_read_b128 v[64:67], v96 offset:8192
v_exp_f32 v20, v20
v_add_f32 v108, v108, v3
v_add_f32 v109, v109, v19
v_cvt_pk_bf16_f32 v1, v2, v3
v_mfma_f32_32x32x16_bf16 v[208:223], v[80:83], a[28:31], v[208:223]
v_cvt_pk_bf16_f32 v17, v18, v19
v_exp_f32 v5, v5
v_exp_f32 v21, v21
s_waitcnt lgkmcnt(5)
v_mfma_f32_32x32x16_bf16 v[224:239], v[84:87], a[24:27], v[224:239]
ds_read_b128 v[68:71], v97 offset:8192
v_add_f32 v108, v108, v4
v_add_f32 v109, v109, v20
v_exp_f32 v6, v6
v_mfma_f32_32x32x16_bf16 v[240:255], v[84:87], a[28:31], v[240:255]
v_exp_f32 v22, v22
v_add_f32 v108, v108, v5
v_add_f32 v109, v109, v21
v_cvt_pk_bf16_f32 v2, v4, v5
s_waitcnt lgkmcnt(4)
v_mfma_f32_32x32x16_bf16 a[192:207], v[88:91], a[24:27], a[192:207]
ds_read_b128 v[72:75], v98 offset:8192
v_cvt_pk_bf16_f32 v18, v20, v21
v_exp_f32 v7, v7
v_exp_f32 v23, v23
v_mfma_f32_32x32x16_bf16 a[208:223], v[88:91], a[28:31], a[208:223]
v_add_f32 v108, v108, v6
v_add_f32 v109, v109, v22
v_exp_f32 v8, v8
s_waitcnt lgkmcnt(3)
v_mfma_f32_32x32x16_bf16 a[224:239], v[92:95], a[24:27], a[224:239]
ds_read_b128 v[76:79], v99 offset:8192
v_exp_f32 v24, v24
v_add_f32 v108, v108, v7
v_add_f32 v109, v109, v23
v_cvt_pk_bf16_f32 v3, v6, v7
v_mfma_f32_32x32x16_bf16 a[240:255], v[92:95], a[28:31], a[240:255]
v_cvt_pk_bf16_f32 v19, v22, v23
v_exp_f32 v9, v9
v_exp_f32 v25, v25
s_waitcnt lgkmcnt(3)
v_mfma_f32_32x32x16_bf16 v[32:47], v[64:67], a[128:131], 0
ds_read_b128 v[80:83], v100 offset:8192
v_add_f32 v108, v108, v8
v_add_f32 v109, v109, v24
v_exp_f32 v10, v10
v_mfma_f32_32x32x16_bf16 v[48:63], v[64:67], a[160:163], 0
v_exp_f32 v26, v26
v_add_f32 v108, v108, v9
v_add_f32 v109, v109, v25
v_cvt_pk_bf16_f32 v8, v8, v9
s_waitcnt lgkmcnt(3)
v_mfma_f32_32x32x16_bf16 v[32:47], v[68:71], a[132:135], v[32:47]
ds_read_b128 v[84:87], v101 offset:8192
v_cvt_pk_bf16_f32 v24, v24, v25
v_exp_f32 v11, v11
v_exp_f32 v27, v27
v_mfma_f32_32x32x16_bf16 v[48:63], v[68:71], a[164:167], v[48:63]
v_add_f32 v108, v108, v10
v_add_f32 v109, v109, v26
v_exp_f32 v12, v12
s_waitcnt lgkmcnt(3)
v_mfma_f32_32x32x16_bf16 v[32:47], v[72:75], a[136:139], v[32:47]
ds_read_b128 v[88:91], v102 offset:8192
v_exp_f32 v28, v28
v_add_f32 v108, v108, v11
v_add_f32 v109, v109, v27
v_cvt_pk_bf16_f32 v9, v10, v11
v_mfma_f32_32x32x16_bf16 v[48:63], v[72:75], a[168:171], v[48:63]
v_cvt_pk_bf16_f32 v25, v26, v27
v_exp_f32 v13, v13
v_exp_f32 v29, v29
s_waitcnt lgkmcnt(3)
v_mfma_f32_32x32x16_bf16 v[32:47], v[76:79], a[140:143], v[32:47]
ds_read_b128 v[92:95], v103 offset:8192
v_add_f32 v108, v108, v12
v_add_f32 v109, v109, v28
v_exp_f32 v14, v14
v_mul_f32 v192, s68, v192
v_mfma_f32_32x32x16_bf16 v[48:63], v[76:79], a[172:175], v[48:63]
v_exp_f32 v30, v30
v_add_f32 v108, v108, v13
v_add_f32 v109, v109, v29
v_cvt_pk_bf16_f32 v10, v12, v13
s_waitcnt lgkmcnt(3)
v_mfma_f32_32x32x16_bf16 v[32:47], v[80:83], a[144:147], v[32:47]
ds_read_b64_tr_b16 v[64:65], v104 offset:0
ds_read_b64_tr_b16 v[66:67], v104 offset:2048
v_cvt_pk_bf16_f32 v26, v28, v29
v_exp_f32 v15, v15
v_mul_f32 v193, s68, v193
v_mfma_f32_32x32x16_bf16 v[48:63], v[80:83], a[176:179], v[48:63]
v_exp_f32 v31, v31
v_add_f32 v108, v108, v14
v_add_f32 v109, v109, v30
v_add_f32 v108, v108, v15
s_waitcnt lgkmcnt(4)
v_mfma_f32_32x32x16_bf16 v[32:47], v[84:87], a[148:151], v[32:47]
ds_read_b64_tr_b16 v[68:69], v104 offset:512
ds_read_b64_tr_b16 v[70:71], v104 offset:2560
v_add_f32 v109, v109, v31
v_cvt_pk_bf16_f32 v11, v14, v15
v_cvt_pk_bf16_f32 v27, v30, v31
v_max_f32 v160, v108, v109
v_mfma_f32_32x32x16_bf16 v[48:63], v[84:87], a[180:183], v[48:63]
v_min_f32 v161, v108, v109
v_cmp_nle_f32 vcc, 0x39800000, v161
s_mov_b64 s[64:65], vcc
v_cmp_nge_f32 vcc, 0x45800000, v160
s_or_b64 vcc, vcc, s[64:65]
s_waitcnt lgkmcnt(5)
v_mfma_f32_32x32x16_bf16 v[32:47], v[88:91], a[152:155], v[32:47]
s_waitcnt vmcnt(8)
s_barrier
ds_read_b64_tr_b16 v[72:73], v104 offset:1024
ds_read_b64_tr_b16 v[74:75], v104 offset:3072
v_cvt_pk_bf16_f32 v192, v192, v193
v_mul_f32 v194, s68, v194
v_mfma_f32_32x32x16_bf16 v[48:63], v[88:91], a[184:187], v[48:63]
v_mul_f32 v195, s68, v195
v_cvt_pk_bf16_f32 v193, v194, v195
v_mul_f32 v196, s68, v196
v_mul_f32 v197, s68, v197
v_cvt_pk_bf16_f32 v194, v196, v197
s_waitcnt lgkmcnt(6)
v_mfma_f32_32x32x16_bf16 v[32:47], v[92:95], a[156:159], v[32:47]
ds_read_b64_tr_b16 v[76:77], v104 offset:1536
ds_read_b64_tr_b16 v[78:79], v104 offset:3584
v_mul_f32 v198, s68, v198
v_mul_f32 v199, s68, v199
v_cvt_pk_bf16_f32 v195, v198, v199
v_mul_f32 v200, s68, v200
v_mfma_f32_32x32x16_bf16 v[48:63], v[92:95], a[188:191], v[48:63]
s_cbranch_vccnz L_tr_0
L_ret_0:
v_add_f32 v106, v106, v108
v_add_f32 v107, v107, v109
v_mul_f32 v201, s68, v201
v_cvt_pk_bf16_f32 v196, v200, v201
s_waitcnt lgkmcnt(6)
v_mfma_f32_32x32x16_bf16 a[0:15], v[64:67], v[0:3], 0
ds_read_b64_tr_b16 v[80:81], v104 offset:4096
ds_read_b64_tr_b16 v[82:83], v104 offset:6144
v_mul_f32 v202, s68, v202
v_mul_f32 v203, s68, v203
v_cvt_pk_bf16_f32 v197, v202, v203
v_mul_f32 v204, s68, v204
v_mfma_f32_32x32x16_bf16 a[16:31], v[64:67], v[16:19], 0
s_add_u32 m0, s61, 98304
s_add_u32 s66, s61, 49152
buffer_load_dwordx4 v120, s[44:47], s66 offen offset:0 nt lds
v_mul_f32 v205, s68, v205
v_cvt_pk_bf16_f32 v198, v204, v205
s_waitcnt lgkmcnt(6)
v_mfma_f32_32x32x16_bf16 a[32:47], v[68:71], v[0:3], 0
buffer_load_dwordx4 v121, s[44:47], s66 offen offset:1024 nt lds
ds_read_b64_tr_b16 v[84:85], v104 offset:4608
ds_read_b64_tr_b16 v[86:87], v104 offset:6656
v_exp_f32 v32, v32
v_mul_f32 v206, s68, v206
v_mfma_f32_32x32x16_bf16 a[48:63], v[68:71], v[16:19], 0
v_exp_f32 v48, v48
v_exp_f32 v33, v33
v_mul_f32 v207, s68, v207
s_waitcnt lgkmcnt(6)
v_mfma_f32_32x32x16_bf16 a[64:79], v[72:75], v[0:3], 0
buffer_load_dwordx4 v122, s[44:47], s66 offen offset:2048 nt lds
ds_read_b64_tr_b16 v[88:89], v104 offset:5120
ds_read_b64_tr_b16 v[90:91], v104 offset:7168
v_exp_f32 v49, v49
v_cvt_pk_bf16_f32 v199, v206, v207
v_mfma_f32_32x32x16_bf16 a[80:95], v[72:75], v[16:19], 0
v_exp_f32 v34, v34
v_exp_f32 v50, v50
v_add_f32 v108, v32, v33
s_waitcnt lgkmcnt(6)
v_mfma_f32_32x32x16_bf16 a[96:111], v[76:79], v[0:3], 0
buffer_load_dwordx4 v123, s[44:47], s66 offen offset:3072 nt lds
ds_read_b64_tr_b16 v[92:93], v104 offset:5632
ds_read_b64_tr_b16 v[94:95], v104 offset:7680
v_add_u32 v96, 0x8000, v96
v_add_f32 v109, v48, v49
v_cvt_pk_bf16_f32 v32, v32, v33
v_mfma_f32_32x32x16_bf16 a[112:127], v[76:79], v[16:19], 0
v_cvt_pk_bf16_f32 v48, v48, v49
v_exp_f32 v35, v35
v_exp_f32 v51, v51
s_waitcnt lgkmcnt(6)
v_mfma_f32_32x32x16_bf16 a[0:15], v[80:83], v[8:11], a[0:15]
s_add_u32 m0, s61, 114688
s_nop 0
buffer_load_dwordx4 v124, s[48:51], s66 offen offset:0 nt lds
ds_read_b128 v[64:67], v96 offset:0
v_add_u32 v97, 0x8000, v97
v_add_f32 v108, v108, v34
v_mfma_f32_32x32x16_bf16 a[16:31], v[80:83], v[24:27], a[16:31]
v_add_f32 v109, v109, v50
v_exp_f32 v36, v36
v_exp_f32 v52, v52
s_waitcnt lgkmcnt(5)
v_mfma_f32_32x32x16_bf16 a[32:47], v[84:87], v[8:11], a[32:47]
buffer_load_dwordx4 v125, s[48:51], s66 offen offset:1024 nt lds
ds_read_b128 v[68:71], v97 offset:0
v_add_u32 v98, 0x8000, v98
v_add_f32 v108, v108, v35
v_add_f32 v109, v109, v51
v_cvt_pk_bf16_f32 v33, v34, v35
v_mfma_f32_32x32x16_bf16 a[48:63], v[84:87], v[24:27], a[48:63]
v_cvt_pk_bf16_f32 v49, v50, v51
v_exp_f32 v37, v37
v_exp_f32 v53, v53
s_waitcnt lgkmcnt(4)
v_mfma_f32_32x32x16_bf16 a[64:79], v[88:91], v[8:11], a[64:79]
buffer_load_dwordx4 v124, s[48:51], s66 offen offset:2048 nt lds
ds_read_b128 v[72:75], v98 offset:0
v_add_u32 v99, 0x8000, v99
v_add_f32 v108, v108, v36
v_add_f32 v109, v109, v52
v_mul_f32 v208, s68, v208
v_mfma_f32_32x32x16_bf16 a[80:95], v[88:91], v[24:27], a[80:95]
v_exp_f32 v38, v38
v_exp_f32 v54, v54
v_add_f32 v108, v108, v37
s_waitcnt lgkmcnt(3)
v_mfma_f32_32x32x16_bf16 a[96:111], v[92:95], v[8:11], a[96:111]
buffer_load_dwordx4 v125, s[48:51], s66 offen offset:3072 nt lds
ds_read_b128 v[76:79], v99 offset:0
v_add_u32 v100, 0x8000, v100
v_add_f32 v109, v109, v53
v_cvt_pk_bf16_f32 v34, v36, v37
v_cvt_pk_bf16_f32 v50, v52, v53
v_mfma_f32_32x32x16_bf16 a[112:127], v[92:95], v[24:27], a[112:127]
v_exp_f32 v39, v39
v_exp_f32 v55, v55
v_add_f32 v108, v108, v38
s_waitcnt lgkmcnt(3)
v_mfma_f32_32x32x16_bf16 v[0:15], v[64:67], a[128:131], v[128:143]
s_add_u32 m0, s61, 131072
s_add_u32 s66, s61, 65536
buffer_load_dwordx4 v120, s[44:47], s66 offen offset:0 nt lds
ds_read_b128 v[80:83], v100 offset:0
v_add_u32 v101, 0x8000, v101
v_add_f32 v109, v109, v54
v_mfma_f32_32x32x16_bf16 v[16:31], v[64:67], a[160:163], v[144:159]
buffer_load_dwordx4 v121, s[44:47], s66 offen offset:1024 nt lds
v_exp_f32 v40, v40
v_exp_f32 v56, v56
s_waitcnt lgkmcnt(3)
v_mfma_f32_32x32x16_bf16 v[0:15], v[68:71], a[132:135], v[0:15]
ds_read_b128 v[84:87], v101 offset:0
v_add_u32 v102, 0x8000, v102
v_add_f32 v108, v108, v39
v_add_f32 v109, v109, v55
v_cvt_pk_bf16_f32 v35, v38, v39
v_cvt_pk_bf16_f32 v51, v54, v55
v_mfma_f32_32x32x16_bf16 v[16:31], v[68:71], a[164:167], v[16:31]
buffer_load_dwordx4 v122, s[44:47], s66 offen offset:2048 nt lds
v_exp_f32 v41, v41
v_exp_f32 v57, v57
s_waitcnt lgkmcnt(3)
v_mfma_f32_32x32x16_bf16 v[0:15], v[72:75], a[136:139], v[0:15]
ds_read_b128 v[88:91], v102 offset:0
v_add_u32 v103, 0x8000, v103
v_add_f32 v108, v108, v40
v_add_f32 v109, v109, v56
v_exp_f32 v42, v42
v_mfma_f32_32x32x16_bf16 v[16:31], v[72:75], a[168:171], v[16:31]
buffer_load_dwordx4 v123, s[44:47], s66 offen offset:3072 nt lds
v_exp_f32 v58, v58
v_add_f32 v108, v108, v41
v_add_f32 v109, v109, v57
s_waitcnt lgkmcnt(3)
v_mfma_f32_32x32x16_bf16 v[0:15], v[76:79], a[140:143], v[0:15]
ds_read_b128 v[92:95], v103 offset:0
v_cvt_pk_bf16_f32 v40, v40, v41
v_cvt_pk_bf16_f32 v56, v56, v57
v_exp_f32 v43, v43
v_mul_f32 v209, s68, v209
v_mfma_f32_32x32x16_bf16 v[16:31], v[76:79], a[172:175], v[16:31]
s_add_u32 m0, s61, 147456
s_nop 0
buffer_load_dwordx4 v124, s[48:51], s66 offen offset:0 nt lds
v_exp_f32 v59, v59
s_waitcnt lgkmcnt(3)
v_mfma_f32_32x32x16_bf16 v[0:15], v[80:83], a[144:147], v[0:15]
ds_read_b64_tr_b16 v[64:65], v104 offset:8192
ds_read_b64_tr_b16 v[66:67], v104 offset:10240
v_add_f32 v108, v108, v42
v_add_f32 v109, v109, v58
v_exp_f32 v44, v44
v_mfma_f32_32x32x16_bf16 v[16:31], v[80:83], a[176:179], v[16:31]
buffer_load_dwordx4 v125, s[48:51], s66 offen offset:1024 nt lds
v_exp_f32 v60, v60
v_add_f32 v108, v108, v43
v_add_f32 v109, v109, v59
s_waitcnt lgkmcnt(4)
v_mfma_f32_32x32x16_bf16 v[0:15], v[84:87], a[148:151], v[0:15]
ds_read_b64_tr_b16 v[68:69], v104 offset:8704
ds_read_b64_tr_b16 v[70:71], v104 offset:10752
v_cvt_pk_bf16_f32 v41, v42, v43
v_cvt_pk_bf16_f32 v57, v58, v59
v_exp_f32 v45, v45
v_mfma_f32_32x32x16_bf16 v[16:31], v[84:87], a[180:183], v[16:31]
buffer_load_dwordx4 v124, s[48:51], s66 offen offset:2048 nt lds
v_exp_f32 v61, v61
v_add_f32 v108, v108, v44
v_add_f32 v109, v109, v60
s_waitcnt lgkmcnt(5)
v_mfma_f32_32x32x16_bf16 v[0:15], v[88:91], a[152:155], v[0:15]
ds_read_b64_tr_b16 v[72:73], v104 offset:9216
ds_read_b64_tr_b16 v[74:75], v104 offset:11264
v_exp_f32 v46, v46
v_exp_f32 v62, v62
v_mfma_f32_32x32x16_bf16 v[16:31], v[88:91], a[184:187], v[16:31]
buffer_load_dwordx4 v125, s[48:51], s66 offen offset:3072 nt lds
v_add_f32 v108, v108, v45
v_add_f32 v109, v109, v61
v_cvt_pk_bf16_f32 v42, v44, v45
v_cvt_pk_bf16_f32 v58, v60, v61
s_waitcnt lgkmcnt(6)
v_mfma_f32_32x32x16_bf16 v[0:15], v[92:95], a[156:159], v[0:15]
ds_read_b64_tr_b16 v[76:77], v104 offset:9728
ds_read_b64_tr_b16 v[78:79], v104 offset:11776
v_exp_f32 v47, v47
v_exp_f32 v63, v63
v_mfma_f32_32x32x16_bf16 v[16:31], v[92:95], a[188:191], v[16:31]
v_add_f32 v108, v108, v46
v_add_f32 v109, v109, v62
v_add_f32 v108, v108, v47
v_add_f32 v109, v109, v63
v_cvt_pk_bf16_f32 v43, v46, v47
v_cvt_pk_bf16_f32 v59, v62, v63
v_max_f32 v160, v108, v109
v_cmp_nge_f32 vcc, 0x45800000, v160
s_cbranch_vccnz L_tr_1
L_ret_1:
v_add_f32 v106, v106, v108
v_add_f32 v107, v107, v109
s_waitcnt lgkmcnt(6)
v_mfma_f32_32x32x16_bf16 a[0:15], v[64:67], v[32:35], a[0:15]
ds_read_b64_tr_b16 v[80:81], v104 offset:12288
ds_read_b64_tr_b16 v[82:83], v104 offset:14336
v_cvt_pk_bf16_f32 v208, v208, v209
v_mul_f32 v210, s68, v210
v_mul_f32 v211, s68, v211
v_cvt_pk_bf16_f32 v209, v210, v211
v_mfma_f32_32x32x16_bf16 a[16:31], v[64:67], v[48:51], a[16:31]
v_mul_f32 v212, s68, v212
v_mul_f32 v213, s68, v213
v_cvt_pk_bf16_f32 v210, v212, v213
v_mul_f32 v214, s68, v214
v_mul_f32 v215, s68, v215
s_waitcnt lgkmcnt(6)
v_mfma_f32_32x32x16_bf16 a[32:47], v[68:71], v[32:35], a[32:47]
ds_read_b64_tr_b16 v[84:85], v104 offset:12800
ds_read_b64_tr_b16 v[86:87], v104 offset:14848
v_exp_f32 v0, v0
v_exp_f32 v16, v16
v_mfma_f32_32x32x16_bf16 a[48:63], v[68:71], v[48:51], a[48:63]
v_exp_f32 v1, v1
v_exp_f32 v17, v17
v_cvt_pk_bf16_f32 v211, v214, v215
s_waitcnt lgkmcnt(6)
v_mfma_f32_32x32x16_bf16 a[64:79], v[72:75], v[32:35], a[64:79]
ds_read_b64_tr_b16 v[88:89], v104 offset:13312
ds_read_b64_tr_b16 v[90:91], v104 offset:15360
v_exp_f32 v2, v2
v_exp_f32 v18, v18
v_mfma_f32_32x32x16_bf16 a[80:95], v[72:75], v[48:51], a[80:95]
v_add_f32 v108, v0, v1
v_add_f32 v109, v16, v17
v_cvt_pk_bf16_f32 v0, v0, v1
v_cvt_pk_bf16_f32 v16, v16, v17
v_mul_f32 v216, s68, v216
s_waitcnt lgkmcnt(6)
v_mfma_f32_32x32x16_bf16 a[96:111], v[76:79], v[32:35], a[96:111]
ds_read_b64_tr_b16 v[92:93], v104 offset:13824
ds_read_b64_tr_b16 v[94:95], v104 offset:15872
v_exp_f32 v3, v3
v_exp_f32 v19, v19
v_mfma_f32_32x32x16_bf16 a[112:127], v[76:79], v[48:51], a[112:127]
v_add_f32 v108, v108, v2
v_add_f32 v109, v109, v18
v_exp_f32 v4, v4
v_mul_f32 v217, s68, v217
s_waitcnt lgkmcnt(6)
v_mfma_f32_32x32x16_bf16 a[0:15], v[80:83], v[40:43], a[0:15]
ds_read_b128 v[64:67], v96 offset:8192
v_exp_f32 v20, v20
v_add_f32 v108, v108, v3
v_add_f32 v109, v109, v19
v_cvt_pk_bf16_f32 v1, v2, v3
v_mfma_f32_32x32x16_bf16 a[16:31], v[80:83], v[56:59], a[16:31]
v_cvt_pk_bf16_f32 v17, v18, v19
v_exp_f32 v5, v5
v_exp_f32 v21, v21
s_waitcnt lgkmcnt(5)
v_mfma_f32_32x32x16_bf16 a[32:47], v[84:87], v[40:43], a[32:47]
ds_read_b128 v[68:71], v97 offset:8192
v_add_f32 v108, v108, v4
v_add_f32 v109, v109, v20
v_exp_f32 v6, v6
v_cvt_pk_bf16_f32 v212, v216, v217
v_mfma_f32_32x32x16_bf16 a[48:63], v[84:87], v[56:59], a[48:63]
v_exp_f32 v22, v22
v_add_f32 v108, v108, v5
v_add_f32 v109, v109, v21
v_cvt_pk_bf16_f32 v2, v4, v5
s_waitcnt lgkmcnt(4)
v_mfma_f32_32x32x16_bf16 a[64:79], v[88:91], v[40:43], a[64:79]
ds_read_b128 v[72:75], v98 offset:8192
v_cvt_pk_bf16_f32 v18, v20, v21
v_exp_f32 v7, v7
v_exp_f32 v23, v23
v_mfma_f32_32x32x16_bf16 a[80:95], v[88:91], v[56:59], a[80:95]
v_add_f32 v108, v108, v6
v_add_f32 v109, v109, v22
v_exp_f32 v8, v8
v_mul_f32 v218, s68, v218
s_waitcnt lgkmcnt(3)
v_mfma_f32_32x32x16_bf16 a[96:111], v[92:95], v[40:43], a[96:111]
ds_read_b128 v[76:79], v99 offset:8192
v_exp_f32 v24, v24
v_add_f32 v108, v108, v7
v_add_f32 v109, v109, v23
v_cvt_pk_bf16_f32 v3, v6, v7
v_mfma_f32_32x32x16_bf16 a[112:127], v[92:95], v[56:59], a[112:127]
v_cvt_pk_bf16_f32 v19, v22, v23
v_exp_f32 v9, v9
v_exp_f32 v25, v25
s_waitcnt lgkmcnt(3)
v_mfma_f32_32x32x16_bf16 v[32:47], v[64:67], a[128:131], v[128:143]
ds_read_b128 v[80:83], v100 offset:8192
v_add_f32 v108, v108, v8
v_add_f32 v109, v109, v24
v_exp_f32 v10, v10
v_mul_f32 v219, s68, v219
v_mfma_f32_32x32x16_bf16 v[48:63], v[64:67], a[160:163], v[144:159]
v_exp_f32 v26, v26
v_add_f32 v108, v108, v9
v_add_f32 v109, v109, v25
v_cvt_pk_bf16_f32 v8, v8, v9
s_waitcnt lgkmcnt(3)
v_mfma_f32_32x32x16_bf16 v[32:47], v[68:71], a[132:135], v[32:47]
ds_read_b128 v[84:87], v101 offset:8192
v_cvt_pk_bf16_f32 v24, v24, v25
v_exp_f32 v11, v11
v_exp_f32 v27, v27
v_mfma_f32_32x32x16_bf16 v[48:63], v[68:71], a[164:167], v[48:63]
v_add_f32 v108, v108, v10
v_add_f32 v109, v109, v26
v_exp_f32 v12, v12
v_cvt_pk_bf16_f32 v213, v218, v219
s_waitcnt lgkmcnt(3)
v_mfma_f32_32x32x16_bf16 v[32:47], v[72:75], a[136:139], v[32:47]
ds_read_b128 v[88:91], v102 offset:8192
v_exp_f32 v28, v28
v_add_f32 v108, v108, v11
v_add_f32 v109, v109, v27
v_cvt_pk_bf16_f32 v9, v10, v11
v_mfma_f32_32x32x16_bf16 v[48:63], v[72:75], a[168:171], v[48:63]
v_cvt_pk_bf16_f32 v25, v26, v27
v_exp_f32 v13, v13
v_exp_f32 v29, v29
s_waitcnt lgkmcnt(3)
v_mfma_f32_32x32x16_bf16 v[32:47], v[76:79], a[140:143], v[32:47]
ds_read_b128 v[92:95], v103 offset:8192
v_add_u32 v104, 0x8000, v104
v_add_f32 v108, v108, v12
v_add_f32 v109, v109, v28
v_exp_f32 v14, v14
v_mfma_f32_32x32x16_bf16 v[48:63], v[76:79], a[172:175], v[48:63]
v_exp_f32 v30, v30
v_add_f32 v108, v108, v13
v_add_f32 v109, v109, v29
v_cvt_pk_bf16_f32 v10, v12, v13
s_waitcnt lgkmcnt(3)
v_mfma_f32_32x32x16_bf16 v[32:47], v[80:83], a[144:147], v[32:47]
ds_read_b64_tr_b16 v[64:65], v104 offset:0
ds_read_b64_tr_b16 v[66:67], v104 offset:2048
v_cvt_pk_bf16_f32 v26, v28, v29
v_exp_f32 v15, v15
v_mul_f32 v220, s68, v220
v_mfma_f32_32x32x16_bf16 v[48:63], v[80:83], a[176:179], v[48:63]
v_exp_f32 v31, v31
v_add_f32 v108, v108, v14
v_add_f32 v109, v109, v30
v_add_f32 v108, v108, v15
s_waitcnt lgkmcnt(4)
v_mfma_f32_32x32x16_bf16 v[32:47], v[84:87], a[148:151], v[32:47]
ds_read_b64_tr_b16 v[68:69], v104 offset:512
ds_read_b64_tr_b16 v[70:71], v104 offset:2560
v_add_f32 v109, v109, v31
v_cvt_pk_bf16_f32 v11, v14, v15
v_cvt_pk_bf16_f32 v27, v30, v31
v_max_f32 v160, v108, v109
v_mfma_f32_32x32x16_bf16 v[48:63], v[84:87], a[180:183], v[48:63]
v_cmp_nge_f32 vcc, 0x45800000, v160
v_mul_f32 v221, s68, v221
v_cvt_pk_bf16_f32 v214, v220, v221
v_mul_f32 v222, s68, v222
v_mul_f32 v223, s68, v223
s_waitcnt lgkmcnt(5)
v_mfma_f32_32x32x16_bf16 v[32:47], v[88:91], a[152:155], v[32:47]
s_waitcnt vmcnt(16)
s_barrier
ds_read_b64_tr_b16 v[72:73], v104 offset:1024
ds_read_b64_tr_b16 v[74:75], v104 offset:3072
v_cvt_pk_bf16_f32 v215, v222, v223
v_mul_f32 v224, s68, v224
v_mfma_f32_32x32x16_bf16 v[48:63], v[88:91], a[184:187], v[48:63]
v_mul_f32 v225, s68, v225
v_cvt_pk_bf16_f32 v224, v224, v225
v_mul_f32 v226, s68, v226
v_mul_f32 v227, s68, v227
v_cvt_pk_bf16_f32 v225, v226, v227
s_waitcnt lgkmcnt(6)
v_mfma_f32_32x32x16_bf16 v[32:47], v[92:95], a[156:159], v[32:47]
ds_read_b64_tr_b16 v[76:77], v104 offset:1536
ds_read_b64_tr_b16 v[78:79], v104 offset:3584
v_mul_f32 v228, s68, v228
v_mul_f32 v229, s68, v229
v_cvt_pk_bf16_f32 v226, v228, v229
v_mul_f32 v230, s68, v230
v_mfma_f32_32x32x16_bf16 v[48:63], v[92:95], a[188:191], v[48:63]
s_cbranch_vccnz L_tr_2
L_ret_2:
v_add_f32 v106, v106, v108
v_add_f32 v107, v107, v109
v_mul_f32 v231, s68, v231
v_cvt_pk_bf16_f32 v227, v230, v231
s_waitcnt lgkmcnt(6)
v_mfma_f32_32x32x16_bf16 a[0:15], v[64:67], v[0:3], a[0:15]
ds_read_b64_tr_b16 v[80:81], v104 offset:4096
ds_read_b64_tr_b16 v[82:83], v104 offset:6144
v_mul_f32 v232, s68, v232
v_mul_f32 v233, s68, v233
v_cvt_pk_bf16_f32 v228, v232, v233
v_mul_f32 v234, s68, v234
v_mfma_f32_32x32x16_bf16 a[16:31], v[64:67], v[16:19], a[16:31]
s_add_u32 m0, s61, 0
s_add_u32 s66, s61, 81920
buffer_load_dwordx4 v120, s[44:47], s66 offen offset:0 nt lds
v_mul_f32 v235, s68, v235
v_cvt_pk_bf16_f32 v229, v234, v235
s_waitcnt lgkmcnt(6)
v_mfma_f32_32x32x16_bf16 a[32:47], v[68:71], v[0:3], a[32:47]
ds_read_b64_tr_b16 v[84:85], v104 offset:4608
ds_read_b64_tr_b16 v[86:87], v104 offset:6656
v_exp_f32 v32, v32
v_exp_f32 v48, v48
v_mfma_f32_32x32x16_bf16 a[48:63], v[68:71], v[16:19], a[48:63]
buffer_load_dwordx4 v121, s[44:47], s66 offen offset:1024 nt lds
v_exp_f32 v33, v33
v_exp_f32 v49, v49
s_waitcnt lgkmcnt(6)
v_mfma_f32_32x32x16_bf16 a[64:79], v[72:75], v[0:3], a[64:79]
ds_read_b64_tr_b16 v[88:89], v104 offset:5120
ds_read_b64_tr_b16 v[90:91], v104 offset:7168
v_exp_f32 v34, v34
v_exp_f32 v50, v50
v_mfma_f32_32x32x16_bf16 a[80:95], v[72:75], v[16:19], a[80:95]
v_add_f32 v108, v32, v33
v_add_f32 v109, v48, v49
v_cvt_pk_bf16_f32 v32, v32, v33
v_cvt_pk_bf16_f32 v48, v48, v49
v_mul_f32 v236, s68, v236
s_waitcnt lgkmcnt(6)
v_mfma_f32_32x32x16_bf16 a[96:111], v[76:79], v[0:3], a[96:111]
buffer_load_dwordx4 v122, s[44:47], s66 offen offset:2048 nt lds
ds_read_b64_tr_b16 v[92:93], v104 offset:5632
ds_read_b64_tr_b16 v[94:95], v104 offset:7680
v_add_u32 v96, 0x8000, v96
v_exp_f32 v35, v35
v_mfma_f32_32x32x16_bf16 a[112:127], v[76:79], v[16:19], a[112:127]
v_exp_f32 v51, v51
v_add_f32 v108, v108, v34
v_add_f32 v109, v109, v50
v_mul_f32 v237, s68, v237
s_waitcnt lgkmcnt(6)
v_mfma_f32_32x32x16_bf16 a[0:15], v[80:83], v[8:11], a[0:15]
buffer_load_dwordx4 v123, s[44:47], s66 offen offset:3072 nt lds
ds_read_b128 v[64:67], v96 offset:0
v_add_u32 v97, 0x8000, v97
v_exp_f32 v36, v36
v_cvt_pk_bf16_f32 v230, v236, v237
v_mfma_f32_32x32x16_bf16 a[16:31], v[80:83], v[24:27], a[16:31]
v_exp_f32 v52, v52
v_add_f32 v108, v108, v35
v_add_f32 v109, v109, v51
v_cvt_pk_bf16_f32 v33, v34, v35
s_waitcnt lgkmcnt(5)
v_mfma_f32_32x32x16_bf16 a[32:47], v[84:87], v[8:11], a[32:47]
ds_read_b128 v[68:71], v97 offset:0
v_add_u32 v98, 0x8000, v98
v_cvt_pk_bf16_f32 v49, v50, v51
v_exp_f32 v37, v37
v_mul_f32 v238, s68, v238
v_mfma_f32_32x32x16_bf16 a[48:63], v[84:87], v[24:27], a[48:63]
s_add_u32 m0, s61, 16384
s_nop 0
buffer_load_dwordx4 v124, s[48:51], s66 offen offset:0 nt lds
v_exp_f32 v53, v53
s_waitcnt lgkmcnt(4)
v_mfma_f32_32x32x16_bf16 a[64:79], v[88:91], v[8:11], a[64:79]
ds_read_b128 v[72:75], v98 offset:0
v_add_u32 v99, 0x8000, v99
v_add_f32 v108, v108, v36
v_add_f32 v109, v109, v52
v_exp_f32 v38, v38
v_mfma_f32_32x32x16_bf16 a[80:95], v[88:91], v[24:27], a[80:95]
buffer_load_dwordx4 v125, s[48:51], s66 offen offset:1024 nt lds
v_exp_f32 v54, v54
v_add_f32 v108, v108, v37
v_add_f32 v109, v109, v53
s_waitcnt lgkmcnt(3)
v_mfma_f32_32x32x16_bf16 a[96:111], v[92:95], v[8:11], a[96:111]
ds_read_b128 v[76:79], v99 offset:0
v_add_u32 v100, 0x8000, v100
v_cvt_pk_bf16_f32 v34, v36, v37
v_cvt_pk_bf16_f32 v50, v52, v53
v_exp_f32 v39, v39
v_mfma_f32_32x32x16_bf16 a[112:127], v[92:95], v[24:27], a[112:127]
v_exp_f32 v55, v55
v_add_f32 v108, v108, v38
v_add_f32 v109, v109, v54
v_mul_f32 v239, s68, v239
s_waitcnt lgkmcnt(3)
v_mfma_f32_32x32x16_bf16 v[0:15], v[64:67], a[128:131], v[128:143]
buffer_load_dwordx4 v124, s[48:51], s66 offen offset:2048 nt lds
ds_read_b128 v[80:83], v100 offset:0
v_add_u32 v101, 0x8000, v101
v_exp_f32 v40, v40
v_cvt_pk_bf16_f32 v231, v238, v239
v_mfma_f32_32x32x16_bf16 v[16:31], v[64:67], a[160:163], v[144:159]
v_exp_f32 v56, v56
v_add_f32 v108, v108, v39
v_add_f32 v109, v109, v55
v_cvt_pk_bf16_f32 v35, v38, v39
s_waitcnt lgkmcnt(3)
v_mfma_f32_32x32x16_bf16 v[0:15], v[68:71], a[132:135], v[0:15]
buffer_load_dwordx4 v125, s[48:51], s66 offen offset:3072 nt lds
ds_read_b128 v[84:87], v101 offset:0
v_add_u32 v102, 0x8000, v102
v_cvt_pk_bf16_f32 v51, v54, v55
v_exp_f32 v41, v41
v_mfma_f32_32x32x16_bf16 v[16:31], v[68:71], a[164:167], v[16:31]
v_exp_f32 v57, v57
v_add_f32 v108, v108, v40
v_add_f32 v109, v109, v56
v_mul_f32 v240, s68, v240
s_waitcnt lgkmcnt(3)
v_mfma_f32_32x32x16_bf16 v[0:15], v[72:75], a[136:139], v[0:15]
ds_read_b128 v[88:91], v102 offset:0
v_add_u32 v103, 0x8000, v103
v_exp_f32 v42, v42
v_exp_f32 v58, v58
v_mfma_f32_32x32x16_bf16 v[16:31], v[72:75], a[168:171], v[16:31]
v_add_f32 v108, v108, v41
v_add_f32 v109, v109, v57
v_cvt_pk_bf16_f32 v40, v40, v41
v_cvt_pk_bf16_f32 v56, v56, v57
v_mul_f32 v241, s68, v241
s_waitcnt lgkmcnt(3)
v_mfma_f32_32x32x16_bf16 v[0:15], v[76:79], a[140:143], v[0:15]
ds_read_b128 v[92:95], v103 offset:0
v_exp_f32 v43, v43
v_exp_f32 v59, v59
v_add_f32 v108, v108, v42
v_mfma_f32_32x32x16_bf16 v[16:31], v[76:79], a[172:175], v[16:31]
v_add_f32 v109, v109, v58
v_exp_f32 v44, v44
v_exp_f32 v60, v60
s_waitcnt lgkmcnt(3)
v_mfma_f32_32x32x16_bf16 v[0:15], v[80:83], a[144:147], v[0:15]
ds_read_b64_tr_b16 v[64:65], v104 offset:8192
ds_read_b64_tr_b16 v[66:67], v104 offset:10240
v_add_f32 v108, v108, v43
v_add_f32 v109, v109, v59
v_cvt_pk_bf16_f32 v41, v42, v43
v_cvt_pk_bf16_f32 v57, v58, v59
v_mfma_f32_32x32x16_bf16 v[16:31], v[80:83], a[176:179], v[16:31]
v_exp_f32 v45, v45
v_exp_f32 v61, v61
v_add_f32 v108, v108, v44
s_waitcnt lgkmcnt(4)
v_mfma_f32_32x32x16_bf16 v[0:15], v[84:87], a[148:151], v[0:15]
ds_read_b64_tr_b16 v[68:69], v104 offset:8704
ds_read_b64_tr_b16 v[70:71], v104 offset:10752
v_add_f32 v109, v109, v60
v_exp_f32 v46, v46
v_cvt_pk_bf16_f32 v240, v240, v241
v_mfma_f32_32x32x16_bf16 v[16:31], v[84:87], a[180:183], v[16:31]
v_exp_f32 v62, v62
v_add_f32 v108, v108, v45
v_add_f32 v109, v109, v61
v_cvt_pk_bf16_f32 v42, v44, v45
s_waitcnt lgkmcnt(5)
v_mfma_f32_32x32x16_bf16 v[0:15], v[88:91], a[152:155], v[0:15]
ds_read_b64_tr_b16 v[72:73], v104 offset:9216
ds_read_b64_tr_b16 v[74:75], v104 offset:11264
v_cvt_pk_bf16_f32 v58, v60, v61
v_exp_f32 v47, v47
v_mul_f32 v242, s68, v242
v_mfma_f32_32x32x16_bf16 v[16:31], v[88:91], a[184:187], v[16:31]
v_exp_f32 v63, v63
v_add_f32 v108, v108, v46
v_add_f32 v109, v109, v62
v_add_f32 v108, v108, v47
s_waitcnt lgkmcnt(6)
v_mfma_f32_32x32x16_bf16 v[0:15], v[92:95], a[156:159], v[0:15]
ds_read_b64_tr_b16 v[76:77], v104 offset:9728
ds_read_b64_tr_b16 v[78:79], v104 offset:11776
v_add_f32 v109, v109, v63
v_cvt_pk_bf16_f32 v43, v46, v47
v_cvt_pk_bf16_f32 v59, v62, v63
v_max_f32 v160, v108, v109
v_mfma_f32_32x32x16_bf16 v[16:31], v[92:95], a[188:191], v[16:31]
v_cmp_nge_f32 vcc, 0x45800000, v160
s_cbranch_vccnz L_tr_3
L_ret_3:
v_add_f32 v106, v106, v108
v_add_f32 v107, v107, v109
v_mul_f32 v243, s68, v243
s_waitcnt lgkmcnt(6)
v_mfma_f32_32x32x16_bf16 a[0:15], v[64:67], v[32:35], a[0:15]
ds_read_b64_tr_b16 v[80:81], v104 offset:12288
ds_read_b64_tr_b16 v[82:83], v104 offset:14336
v_cvt_pk_bf16_f32 v241, v242, v243
v_mul_f32 v244, s68, v244
v_mul_f32 v245, s68, v245
v_cvt_pk_bf16_f32 v242, v244, v245
v_mfma_f32_32x32x16_bf16 a[16:31], v[64:67], v[48:51], a[16:31]
v_mul_f32 v246, s68, v246
v_mul_f32 v247, s68, v247
v_cvt_pk_bf16_f32 v243, v246, v247
v_mul_f32 v248, s68, v248
v_mul_f32 v249, s68, v249
s_waitcnt lgkmcnt(6)
v_mfma_f32_32x32x16_bf16 a[32:47], v[68:71], v[32:35], a[32:47]
ds_read_b64_tr_b16 v[84:85], v104 offset:12800
ds_read_b64_tr_b16 v[86:87], v104 offset:14848
v_exp_f32 v0, v0
v_exp_f32 v16, v16
v_mfma_f32_32x32x16_bf16 a[48:63], v[68:71], v[48:51], a[48:63]
v_exp_f32 v1, v1
v_exp_f32 v17, v17
v_cvt_pk_bf16_f32 v244, v248, v249
s_waitcnt lgkmcnt(6)
v_mfma_f32_32x32x16_bf16 a[64:79], v[72:75], v[32:35], a[64:79]
ds_read_b64_tr_b16 v[88:89], v104 offset:13312
ds_read_b64_tr_b16 v[90:91], v104 offset:15360
v_exp_f32 v2, v2
v_exp_f32 v18, v18
v_mfma_f32_32x32x16_bf16 a[80:95], v[72:75], v[48:51], a[80:95]
v_add_f32 v108, v0, v1
v_add_f32 v109, v16, v17
v_cvt_pk_bf16_f32 v0, v0, v1
v_cvt_pk_bf16_f32 v16, v16, v17
v_mul_f32 v250, s68, v250
s_waitcnt lgkmcnt(6)
v_mfma_f32_32x32x16_bf16 a[96:111], v[76:79], v[32:35], a[96:111]
ds_read_b64_tr_b16 v[92:93], v104 offset:13824
ds_read_b64_tr_b16 v[94:95], v104 offset:15872
v_exp_f32 v3, v3
v_exp_f32 v19, v19
v_mfma_f32_32x32x16_bf16 a[112:127], v[76:79], v[48:51], a[112:127]
v_add_f32 v108, v108, v2
v_add_f32 v109, v109, v18
v_exp_f32 v4, v4
v_mul_f32 v251, s68, v251
s_waitcnt lgkmcnt(6)
v_mfma_f32_32x32x16_bf16 a[0:15], v[80:83], v[40:43], a[0:15]
ds_read_b128 v[64:67], v96 offset:8192
v_exp_f32 v20, v20
v_add_f32 v108, v108, v3
v_add_f32 v109, v109, v19
v_cvt_pk_bf16_f32 v1, v2, v3
v_mfma_f32_32x32x16_bf16 a[16:31], v[80:83], v[56:59], a[16:31]
v_cvt_pk_bf16_f32 v17, v18, v19
v_exp_f32 v5, v5
v_exp_f32 v21, v21
s_waitcnt lgkmcnt(5)
v_mfma_f32_32x32x16_bf16 a[32:47], v[84:87], v[40:43], a[32:47]
ds_read_b128 v[68:71], v97 offset:8192
v_add_f32 v108, v108, v4
v_add_f32 v109, v109, v20
v_exp_f32 v6, v6
v_cvt_pk_bf16_f32 v245, v250, v251
v_mfma_f32_32x32x16_bf16 a[48:63], v[84:87], v[56:59], a[48:63]
v_exp_f32 v22, v22
v_add_f32 v108, v108, v5
v_add_f32 v109, v109, v21
v_cvt_pk_bf16_f32 v2, v4, v5
s_waitcnt lgkmcnt(4)
v_mfma_f32_32x32x16_bf16 a[64:79], v[88:91], v[40:43], a[64:79]
ds_read_b128 v[72:75], v98 offset:8192
v_cvt_pk_bf16_f32 v18, v20, v21
v_exp_f32 v7, v7
v_exp_f32 v23, v23
v_mfma_f32_32x32x16_bf16 a[80:95], v[88:91], v[56:59], a[80:95]
v_add_f32 v108, v108, v6
v_add_f32 v109, v109, v22
v_exp_f32 v8, v8
v_mul_f32 v252, s68, v252
s_waitcnt lgkmcnt(3)
v_mfma_f32_32x32x16_bf16 a[96:111], v[92:95], v[40:43], a[96:111]
ds_read_b128 v[76:79], v99 offset:8192
v_exp_f32 v24, v24
v_add_f32 v108, v108, v7
v_add_f32 v109, v109, v23
v_cvt_pk_bf16_f32 v3, v6, v7
v_mfma_f32_32x32x16_bf16 a[112:127], v[92:95], v[56:59], a[112:127]
v_cvt_pk_bf16_f32 v19, v22, v23
v_exp_f32 v9, v9
v_exp_f32 v25, v25
s_waitcnt lgkmcnt(3)
v_mfma_f32_32x32x16_bf16 v[32:47], v[64:67], a[128:131], v[128:143]
ds_read_b128 v[80:83], v100 offset:8192
v_add_f32 v108, v108, v8
v_add_f32 v109, v109, v24
v_exp_f32 v10, v10
v_mul_f32 v253, s68, v253
v_mfma_f32_32x32x16_bf16 v[48:63], v[64:67], a[160:163], v[144:159]
v_exp_f32 v26, v26
v_add_f32 v108, v108, v9
v_add_f32 v109, v109, v25
v_cvt_pk_bf16_f32 v8, v8, v9
s_waitcnt lgkmcnt(3)
v_mfma_f32_32x32x16_bf16 v[32:47], v[68:71], a[132:135], v[32:47]
ds_read_b128 v[84:87], v101 offset:8192
v_cvt_pk_bf16_f32 v24, v24, v25
v_exp_f32 v11, v11
v_exp_f32 v27, v27
v_mfma_f32_32x32x16_bf16 v[48:63], v[68:71], a[164:167], v[48:63]
v_add_f32 v108, v108, v10
v_add_f32 v109, v109, v26
v_exp_f32 v12, v12
v_cvt_pk_bf16_f32 v246, v252, v253
s_waitcnt lgkmcnt(3)
v_mfma_f32_32x32x16_bf16 v[32:47], v[72:75], a[136:139], v[32:47]
ds_read_b128 v[88:91], v102 offset:8192
v_exp_f32 v28, v28
v_add_f32 v108, v108, v11
v_add_f32 v109, v109, v27
v_cvt_pk_bf16_f32 v9, v10, v11
v_mfma_f32_32x32x16_bf16 v[48:63], v[72:75], a[168:171], v[48:63]
v_cvt_pk_bf16_f32 v25, v26, v27
v_exp_f32 v13, v13
v_exp_f32 v29, v29
s_waitcnt lgkmcnt(3)
v_mfma_f32_32x32x16_bf16 v[32:47], v[76:79], a[140:143], v[32:47]
ds_read_b128 v[92:95], v103 offset:8192
v_add_u32 v104, 0x8000, v104
v_add_f32 v108, v108, v12
v_add_f32 v109, v109, v28
v_exp_f32 v14, v14
v_mfma_f32_32x32x16_bf16 v[48:63], v[76:79], a[172:175], v[48:63]
v_exp_f32 v30, v30
v_add_f32 v108, v108, v13
v_add_f32 v109, v109, v29
v_cvt_pk_bf16_f32 v10, v12, v13
s_waitcnt lgkmcnt(3)
v_mfma_f32_32x32x16_bf16 v[32:47], v[80:83], a[144:147], v[32:47]
ds_read_b64_tr_b16 v[64:65], v104 offset:0
ds_read_b64_tr_b16 v[66:67], v104 offset:2048
v_cvt_pk_bf16_f32 v26, v28, v29
v_exp_f32 v15, v15
v_mul_f32 v254, s68, v254
v_mfma_f32_32x32x16_bf16 v[48:63], v[80:83], a[176:179], v[48:63]
v_exp_f32 v31, v31
v_add_f32 v108, v108, v14
v_add_f32 v109, v109, v30
v_add_f32 v108, v108, v15
s_waitcnt lgkmcnt(4)
v_mfma_f32_32x32x16_bf16 v[32:47], v[84:87], a[148:151], v[32:47]
ds_read_b64_tr_b16 v[68:69], v104 offset:512
ds_read_b64_tr_b16 v[70:71], v104 offset:2560
v_add_f32 v109, v109, v31
v_cvt_pk_bf16_f32 v11, v14, v15
v_cvt_pk_bf16_f32 v27, v30, v31
v_max_f32 v160, v108, v109
v_mfma_f32_32x32x16_bf16 v[48:63], v[84:87], a[180:183], v[48:63]
v_cmp_nge_f32 vcc, 0x45800000, v160
v_mul_f32 v255, s68, v255
v_cvt_pk_bf16_f32 v247, v254, v255
v_accvgpr_read_b32 v116, a192
v_accvgpr_read_b32 v117, a193
s_waitcnt lgkmcnt(5)
v_mfma_f32_32x32x16_bf16 v[32:47], v[88:91], a[152:155], v[32:47]
s_waitcnt vmcnt(16)
s_barrier
ds_read_b64_tr_b16 v[72:73], v104 offset:1024
ds_read_b64_tr_b16 v[74:75], v104 offset:3072
v_mul_f32 v116, s68, v116
v_mul_f32 v117, s68, v117
v_mfma_f32_32x32x16_bf16 v[48:63], v[88:91], a[184:187], v[48:63]
v_cvt_pk_bf16_f32 v200, v116, v117
v_accvgpr_read_b32 v118, a194
v_accvgpr_read_b32 v119, a195
v_mul_f32 v118, s68, v118
v_mul_f32 v119, s68, v119
s_waitcnt lgkmcnt(6)
v_mfma_f32_32x32x16_bf16 v[32:47], v[92:95], a[156:159], v[32:47]
ds_read_b64_tr_b16 v[76:77], v104 offset:1536
ds_read_b64_tr_b16 v[78:79], v104 offset:3584
v_cvt_pk_bf16_f32 v201, v118, v119
v_accvgpr_read_b32 v116, a196
v_accvgpr_read_b32 v117, a197
v_mul_f32 v116, s68, v116
v_mfma_f32_32x32x16_bf16 v[48:63], v[92:95], a[188:191], v[48:63]
s_cbranch_vccnz L_tr_4
L_ret_4:
v_add_f32 v106, v106, v108
v_add_f32 v107, v107, v109
v_mul_f32 v117, s68, v117
v_cvt_pk_bf16_f32 v202, v116, v117
s_waitcnt lgkmcnt(6)
v_mfma_f32_32x32x16_bf16 a[0:15], v[64:67], v[0:3], a[0:15]
ds_read_b64_tr_b16 v[80:81], v104 offset:4096
ds_read_b64_tr_b16 v[82:83], v104 offset:6144
v_accvgpr_read_b32 v118, a198
v_accvgpr_read_b32 v119, a199
v_mul_f32 v118, s68, v118
v_mul_f32 v119, s68, v119
v_mfma_f32_32x32x16_bf16 a[16:31], v[64:67], v[16:19], a[16:31]
s_add_u32 m0, s61, 32768
s_add_u32 s66, s61, 98304
buffer_load_dwordx4 v120, s[44:47], s66 offen offset:0 nt lds
v_cvt_pk_bf16_f32 v203, v118, v119
v_accvgpr_read_b32 v116, a200
s_waitcnt lgkmcnt(6)
v_mfma_f32_32x32x16_bf16 a[32:47], v[68:71], v[0:3], a[32:47]
ds_read_b64_tr_b16 v[84:85], v104 offset:4608
ds_read_b64_tr_b16 v[86:87], v104 offset:6656
v_exp_f32 v32, v32
v_exp_f32 v48, v48
v_mfma_f32_32x32x16_bf16 a[48:63], v[68:71], v[16:19], a[48:63]
buffer_load_dwordx4 v121, s[44:47], s66 offen offset:1024 nt lds
v_exp_f32 v33, v33
v_exp_f32 v49, v49
s_waitcnt lgkmcnt(6)
v_mfma_f32_32x32x16_bf16 a[64:79], v[72:75], v[0:3], a[64:79]
ds_read_b64_tr_b16 v[88:89], v104 offset:5120
ds_read_b64_tr_b16 v[90:91], v104 offset:7168
v_exp_f32 v34, v34
v_exp_f32 v50, v50
v_mfma_f32_32x32x16_bf16 a[80:95], v[72:75], v[16:19], a[80:95]
v_add_f32 v108, v32, v33
v_add_f32 v109, v48, v49
v_cvt_pk_bf16_f32 v32, v32, v33
v_cvt_pk_bf16_f32 v48, v48, v49
v_accvgpr_read_b32 v117, a201
s_waitcnt lgkmcnt(6)
v_mfma_f32_32x32x16_bf16 a[96:111], v[76:79], v[0:3], a[96:111]
buffer_load_dwordx4 v122, s[44:47], s66 offen offset:2048 nt lds
ds_read_b64_tr_b16 v[92:93], v104 offset:5632
ds_read_b64_tr_b16 v[94:95], v104 offset:7680
v_add_u32 v96, 0x8000, v96
v_exp_f32 v35, v35
v_mfma_f32_32x32x16_bf16 a[112:127], v[76:79], v[16:19], a[112:127]
v_exp_f32 v51, v51
v_add_f32 v108, v108, v34
v_add_f32 v109, v109, v50
v_mul_f32 v116, s68, v116
s_waitcnt lgkmcnt(6)
v_mfma_f32_32x32x16_bf16 a[0:15], v[80:83], v[8:11], a[0:15]
buffer_load_dwordx4 v123, s[44:47], s66 offen offset:3072 nt lds
ds_read_b128 v[64:67], v96 offset:0
v_add_u32 v97, 0x8000, v97
v_exp_f32 v36, v36
v_mul_f32 v117, s68, v117
v_mfma_f32_32x32x16_bf16 a[16:31], v[80:83], v[24:27], a[16:31]
v_exp_f32 v52, v52
v_add_f32 v108, v108, v35
v_add_f32 v109, v109, v51
v_cvt_pk_bf16_f32 v33, v34, v35
s_waitcnt lgkmcnt(5)
v_mfma_f32_32x32x16_bf16 a[32:47], v[84:87], v[8:11], a[32:47]
ds_read_b128 v[68:71], v97 offset:0
v_add_u32 v98, 0x8000, v98
v_cvt_pk_bf16_f32 v49, v50, v51
v_exp_f32 v37, v37
v_cvt_pk_bf16_f32 v204, v116, v117
v_mfma_f32_32x32x16_bf16 a[48:63], v[84:87], v[24:27], a[48:63]
s_add_u32 m0, s61, 49152
s_nop 0
buffer_load_dwordx4 v124, s[48:51], s66 offen offset:0 nt lds
v_exp_f32 v53, v53
s_waitcnt lgkmcnt(4)
v_mfma_f32_32x32x16_bf16 a[64:79], v[88:91], v[8:11], a[64:79]
ds_read_b128 v[72:75], v98 offset:0
v_add_u32 v99, 0x8000, v99
v_add_f32 v108, v108, v36
v_add_f32 v109, v109, v52
v_exp_f32 v38, v38
v_mfma_f32_32x32x16_bf16 a[80:95], v[88:91], v[24:27], a[80:95]
buffer_load_dwordx4 v125, s[48:51], s66 offen offset:1024 nt lds
v_exp_f32 v54, v54
v_add_f32 v108, v108, v37
v_add_f32 v109, v109, v53
s_waitcnt lgkmcnt(3)
v_mfma_f32_32x32x16_bf16 a[96:111], v[92:95], v[8:11], a[96:111]
ds_read_b128 v[76:79], v99 offset:0
v_add_u32 v100, 0x8000, v100
v_cvt_pk_bf16_f32 v34, v36, v37
v_cvt_pk_bf16_f32 v50, v52, v53
v_exp_f32 v39, v39
v_mfma_f32_32x32x16_bf16 a[112:127], v[92:95], v[24:27], a[112:127]
v_exp_f32 v55, v55
v_add_f32 v108, v108, v38
v_add_f32 v109, v109, v54
v_accvgpr_read_b32 v118, a202
s_waitcnt lgkmcnt(3)
v_mfma_f32_32x32x16_bf16 v[0:15], v[64:67], a[128:131], v[128:143]
buffer_load_dwordx4 v124, s[48:51], s66 offen offset:2048 nt lds
ds_read_b128 v[80:83], v100 offset:0
v_add_u32 v101, 0x8000, v101
v_exp_f32 v40, v40
v_accvgpr_read_b32 v119, a203
v_mfma_f32_32x32x16_bf16 v[16:31], v[64:67], a[160:163], v[144:159]
v_exp_f32 v56, v56
v_add_f32 v108, v108, v39
v_add_f32 v109, v109, v55
v_cvt_pk_bf16_f32 v35, v38, v39
s_waitcnt lgkmcnt(3)
v_mfma_f32_32x32x16_bf16 v[0:15], v[68:71], a[132:135], v[0:15]
buffer_load_dwordx4 v125, s[48:51], s66 offen offset:3072 nt lds
ds_read_b128 v[84:87], v101 offset:0
v_add_u32 v102, 0x8000, v102
v_cvt_pk_bf16_f32 v51, v54, v55
v_exp_f32 v41, v41
v_mfma_f32_32x32x16_bf16 v[16:31], v[68:71], a[164:167], v[16:31]
v_exp_f32 v57, v57
v_add_f32 v108, v108, v40
v_add_f32 v109, v109, v56
v_mul_f32 v118, s68, v118
s_waitcnt lgkmcnt(3)
v_mfma_f32_32x32x16_bf16 v[0:15], v[72:75], a[136:139], v[0:15]
ds_read_b128 v[88:91], v102 offset:0
v_add_u32 v103, 0x8000, v103
v_exp_f32 v42, v42
v_exp_f32 v58, v58
v_mfma_f32_32x32x16_bf16 v[16:31], v[72:75], a[168:171], v[16:31]
v_add_f32 v108, v108, v41
v_add_f32 v109, v109, v57
v_cvt_pk_bf16_f32 v40, v40, v41
v_cvt_pk_bf16_f32 v56, v56, v57
v_mul_f32 v119, s68, v119
s_waitcnt lgkmcnt(3)
v_mfma_f32_32x32x16_bf16 v[0:15], v[76:79], a[140:143], v[0:15]
ds_read_b128 v[92:95], v103 offset:0
v_exp_f32 v43, v43
v_exp_f32 v59, v59
v_add_f32 v108, v108, v42
v_mfma_f32_32x32x16_bf16 v[16:31], v[76:79], a[172:175], v[16:31]
v_add_f32 v109, v109, v58
v_exp_f32 v44, v44
v_exp_f32 v60, v60
s_waitcnt lgkmcnt(3)
v_mfma_f32_32x32x16_bf16 v[0:15], v[80:83], a[144:147], v[0:15]
ds_read_b64_tr_b16 v[64:65], v104 offset:8192
ds_read_b64_tr_b16 v[66:67], v104 offset:10240
v_add_f32 v108, v108, v43
v_add_f32 v109, v109, v59
v_cvt_pk_bf16_f32 v41, v42, v43
v_cvt_pk_bf16_f32 v57, v58, v59
v_mfma_f32_32x32x16_bf16 v[16:31], v[80:83], a[176:179], v[16:31]
v_exp_f32 v45, v45
v_exp_f32 v61, v61
v_add_f32 v108, v108, v44
s_waitcnt lgkmcnt(4)
v_mfma_f32_32x32x16_bf16 v[0:15], v[84:87], a[148:151], v[0:15]
ds_read_b64_tr_b16 v[68:69], v104 offset:8704
ds_read_b64_tr_b16 v[70:71], v104 offset:10752
v_add_f32 v109, v109, v60
v_exp_f32 v46, v46
v_cvt_pk_bf16_f32 v205, v118, v119
v_mfma_f32_32x32x16_bf16 v[16:31], v[84:87], a[180:183], v[16:31]
v_exp_f32 v62, v62
v_add_f32 v108, v108, v45
v_add_f32 v109, v109, v61
v_cvt_pk_bf16_f32 v42, v44, v45
s_waitcnt lgkmcnt(5)
v_mfma_f32_32x32x16_bf16 v[0:15], v[88:91], a[152:155], v[0:15]
ds_read_b64_tr_b16 v[72:73], v104 offset:9216
ds_read_b64_tr_b16 v[74:75], v104 offset:11264
v_cvt_pk_bf16_f32 v58, v60, v61
v_exp_f32 v47, v47
v_accvgpr_read_b32 v116, a204
v_mfma_f32_32x32x16_bf16 v[16:31], v[88:91], a[184:187], v[16:31]
v_exp_f32 v63, v63
v_add_f32 v108, v108, v46
v_add_f32 v109, v109, v62
v_add_f32 v108, v108, v47
s_waitcnt lgkmcnt(6)
v_mfma_f32_32x32x16_bf16 v[0:15], v[92:95], a[156:159], v[0:15]
ds_read_b64_tr_b16 v[76:77], v104 offset:9728
ds_read_b64_tr_b16 v[78:79], v104 offset:11776
v_add_f32 v109, v109, v63
v_cvt_pk_bf16_f32 v43, v46, v47
v_cvt_pk_bf16_f32 v59, v62, v63
v_max_f32 v160, v108, v109
v_mfma_f32_32x32x16_bf16 v[16:31], v[92:95], a[188:191], v[16:31]
v_cmp_nge_f32 vcc, 0x45800000, v160
s_cbranch_vccnz L_tr_5
L_ret_5:
v_add_f32 v106, v106, v108
v_add_f32 v107, v107, v109
v_accvgpr_read_b32 v117, a205
s_waitcnt lgkmcnt(6)
v_mfma_f32_32x32x16_bf16 a[0:15], v[64:67], v[32:35], a[0:15]
ds_read_b64_tr_b16 v[80:81], v104 offset:12288
ds_read_b64_tr_b16 v[82:83], v104 offset:14336
v_mul_f32 v116, s68, v116
v_mul_f32 v117, s68, v117
v_cvt_pk_bf16_f32 v206, v116, v117
v_accvgpr_read_b32 v118, a206
v_mfma_f32_32x32x16_bf16 a[16:31], v[64:67], v[48:51], a[16:31]
v_accvgpr_read_b32 v119, a207
v_mul_f32 v118, s68, v118
v_mul_f32 v119, s68, v119
v_cvt_pk_bf16_f32 v207, v118, v119
v_accvgpr_read_b32 v116, a208
s_waitcnt lgkmcnt(6)
v_mfma_f32_32x32x16_bf16 a[32:47], v[68:71], v[32:35], a[32:47]
ds_read_b64_tr_b16 v[84:85], v104 offset:12800
ds_read_b64_tr_b16 v[86:87], v104 offset:14848
v_exp_f32 v0, v0
v_exp_f32 v16, v16
v_mfma_f32_32x32x16_bf16 a[48:63], v[68:71], v[48:51], a[48:63]
v_exp_f32 v1, v1
v_exp_f32 v17, v17
v_accvgpr_read_b32 v117, a209
s_waitcnt lgkmcnt(6)
v_mfma_f32_32x32x16_bf16 a[64:79], v[72:75], v[32:35], a[64:79]
ds_read_b64_tr_b16 v[88:89], v104 offset:13312
ds_read_b64_tr_b16 v[90:91], v104 offset:15360
v_exp_f32 v2, v2
v_exp_f32 v18, v18
v_mfma_f32_32x32x16_bf16 a[80:95], v[72:75], v[48:51], a[80:95]
v_add_f32 v108, v0, v1
v_add_f32 v109, v16, v17
v_cvt_pk_bf16_f32 v0, v0, v1
v_cvt_pk_bf16_f32 v16, v16, v17
v_mul_f32 v116, s68, v116
s_waitcnt lgkmcnt(6)
v_mfma_f32_32x32x16_bf16 a[96:111], v[76:79], v[32:35], a[96:111]
ds_read_b64_tr_b16 v[92:93], v104 offset:13824
ds_read_b64_tr_b16 v[94:95], v104 offset:15872
v_exp_f32 v3, v3
v_exp_f32 v19, v19
v_mfma_f32_32x32x16_bf16 a[112:127], v[76:79], v[48:51], a[112:127]
v_add_f32 v108, v108, v2
v_add_f32 v109, v109, v18
v_exp_f32 v4, v4
v_mul_f32 v117, s68, v117
s_waitcnt lgkmcnt(6)
v_mfma_f32_32x32x16_bf16 a[0:15], v[80:83], v[40:43], a[0:15]
ds_read_b128 v[64:67], v96 offset:8192
v_exp_f32 v20, v20
v_add_f32 v108, v108, v3
v_add_f32 v109, v109, v19
v_cvt_pk_bf16_f32 v1, v2, v3
v_mfma_f32_32x32x16_bf16 a[16:31], v[80:83], v[56:59], a[16:31]
v_cvt_pk_bf16_f32 v17, v18, v19
v_exp_f32 v5, v5
v_exp_f32 v21, v21
s_waitcnt lgkmcnt(5)
v_mfma_f32_32x32x16_bf16 a[32:47], v[84:87], v[40:43], a[32:47]
ds_read_b128 v[68:71], v97 offset:8192
v_add_f32 v108, v108, v4
v_add_f32 v109, v109, v20
v_exp_f32 v6, v6
v_cvt_pk_bf16_f32 v216, v116, v117
v_mfma_f32_32x32x16_bf16 a[48:63], v[84:87], v[56:59], a[48:63]
v_exp_f32 v22, v22
v_add_f32 v108, v108, v5
v_add_f32 v109, v109, v21
v_cvt_pk_bf16_f32 v2, v4, v5
s_waitcnt lgkmcnt(4)
v_mfma_f32_32x32x16_bf16 a[64:79], v[88:91], v[40:43], a[64:79]
ds_read_b128 v[72:75], v98 offset:8192
v_cvt_pk_bf16_f32 v18, v20, v21
v_exp_f32 v7, v7
v_exp_f32 v23, v23
v_mfma_f32_32x32x16_bf16 a[80:95], v[88:91], v[56:59], a[80:95]
v_add_f32 v108, v108, v6
v_add_f32 v109, v109, v22
v_exp_f32 v8, v8
v_accvgpr_read_b32 v118, a210
s_waitcnt lgkmcnt(3)
v_mfma_f32_32x32x16_bf16 a[96:111], v[92:95], v[40:43], a[96:111]
ds_read_b128 v[76:79], v99 offset:8192
v_exp_f32 v24, v24
v_add_f32 v108, v108, v7
v_add_f32 v109, v109, v23
v_cvt_pk_bf16_f32 v3, v6, v7
v_mfma_f32_32x32x16_bf16 a[112:127], v[92:95], v[56:59], a[112:127]
v_cvt_pk_bf16_f32 v19, v22, v23
v_exp_f32 v9, v9
v_exp_f32 v25, v25
s_waitcnt lgkmcnt(3)
v_mfma_f32_32x32x16_bf16 v[32:47], v[64:67], a[128:131], v[128:143]
ds_read_b128 v[80:83], v100 offset:8192
v_add_f32 v108, v108, v8
v_add_f32 v109, v109, v24
v_exp_f32 v10, v10
v_accvgpr_read_b32 v119, a211
v_mfma_f32_32x32x16_bf16 v[48:63], v[64:67], a[160:163], v[144:159]
v_exp_f32 v26, v26
v_add_f32 v108, v108, v9
v_add_f32 v109, v109, v25
v_cvt_pk_bf16_f32 v8, v8, v9
s_waitcnt lgkmcnt(3)
v_mfma_f32_32x32x16_bf16 v[32:47], v[68:71], a[132:135], v[32:47]
ds_read_b128 v[84:87], v101 offset:8192
v_cvt_pk_bf16_f32 v24, v24, v25
v_exp_f32 v11, v11
v_exp_f32 v27, v27
v_mfma_f32_32x32x16_bf16 v[48:63], v[68:71], a[164:167], v[48:63]
v_add_f32 v108, v108, v10
v_add_f32 v109, v109, v26
v_exp_f32 v12, v12
v_mul_f32 v118, s68, v118
s_waitcnt lgkmcnt(3)
v_mfma_f32_32x32x16_bf16 v[32:47], v[72:75], a[136:139], v[32:47]
ds_read_b128 v[88:91], v102 offset:8192
v_exp_f32 v28, v28
v_add_f32 v108, v108, v11
v_add_f32 v109, v109, v27
v_cvt_pk_bf16_f32 v9, v10, v11
v_mfma_f32_32x32x16_bf16 v[48:63], v[72:75], a[168:171], v[48:63]
v_cvt_pk_bf16_f32 v25, v26, v27
v_exp_f32 v13, v13
v_exp_f32 v29, v29
s_waitcnt lgkmcnt(3)
v_mfma_f32_32x32x16_bf16 v[32:47], v[76:79], a[140:143], v[32:47]
ds_read_b128 v[92:95], v103 offset:8192
v_add_u32 v104, 0x8000, v104
v_add_f32 v108, v108, v12
v_add_f32 v109, v109, v28
v_exp_f32 v14, v14
v_mfma_f32_32x32x16_bf16 v[48:63], v[76:79], a[172:175], v[48:63]
v_exp_f32 v30, v30
v_add_f32 v108, v108, v13
v_add_f32 v109, v109, v29
v_cvt_pk_bf16_f32 v10, v12, v13
s_waitcnt lgkmcnt(3)
v_mfma_f32_32x32x16_bf16 v[32:47], v[80:83], a[144:147], v[32:47]
ds_read_b64_tr_b16 v[64:65], v104 offset:0
ds_read_b64_tr_b16 v[66:67], v104 offset:2048
v_cvt_pk_bf16_f32 v26, v28, v29
v_exp_f32 v15, v15
v_mul_f32 v119, s68, v119
v_mfma_f32_32x32x16_bf16 v[48:63], v[80:83], a[176:179], v[48:63]
v_exp_f32 v31, v31
v_add_f32 v108, v108, v14
v_add_f32 v109, v109, v30
v_add_f32 v108, v108, v15
s_waitcnt lgkmcnt(4)
v_mfma_f32_32x32x16_bf16 v[32:47], v[84:87], a[148:151], v[32:47]
ds_read_b64_tr_b16 v[68:69], v104 offset:512
ds_read_b64_tr_b16 v[70:71], v104 offset:2560
v_add_f32 v109, v109, v31
v_cvt_pk_bf16_f32 v11, v14, v15
v_cvt_pk_bf16_f32 v27, v30, v31
v_max_f32 v160, v108, v109
v_mfma_f32_32x32x16_bf16 v[48:63], v[84:87], a[180:183], v[48:63]
v_cmp_nge_f32 vcc, 0x45800000, v160
v_cvt_pk_bf16_f32 v217, v118, v119
v_accvgpr_read_b32 v116, a212
v_accvgpr_read_b32 v117, a213
v_mul_f32 v116, s68, v116
s_waitcnt lgkmcnt(5)
v_mfma_f32_32x32x16_bf16 v[32:47], v[88:91], a[152:155], v[32:47]
s_waitcnt vmcnt(16)
s_barrier
ds_read_b64_tr_b16 v[72:73], v104 offset:1024
ds_read_b64_tr_b16 v[74:75], v104 offset:3072
v_mul_f32 v117, s68, v117
v_cvt_pk_bf16_f32 v218, v116, v117
v_mfma_f32_32x32x16_bf16 v[48:63], v[88:91], a[184:187], v[48:63]
v_accvgpr_read_b32 v118, a214
v_accvgpr_read_b32 v119, a215
v_mul_f32 v118, s68, v118
v_mul_f32 v119, s68, v119
v_cvt_pk_bf16_f32 v219, v118, v119
s_waitcnt lgkmcnt(6)
v_mfma_f32_32x32x16_bf16 v[32:47], v[92:95], a[156:159], v[32:47]
ds_read_b64_tr_b16 v[76:77], v104 offset:1536
ds_read_b64_tr_b16 v[78:79], v104 offset:3584
v_accvgpr_read_b32 v116, a216
v_accvgpr_read_b32 v117, a217
v_mul_f32 v116, s68, v116
v_mul_f32 v117, s68, v117
v_mfma_f32_32x32x16_bf16 v[48:63], v[92:95], a[188:191], v[48:63]
s_cbranch_vccnz L_tr_6
L_ret_6:
v_add_f32 v106, v106, v108
v_add_f32 v107, v107, v109
v_cvt_pk_bf16_f32 v220, v116, v117
v_accvgpr_read_b32 v118, a218
s_waitcnt lgkmcnt(6)
v_mfma_f32_32x32x16_bf16 a[0:15], v[64:67], v[0:3], a[0:15]
ds_read_b64_tr_b16 v[80:81], v104 offset:4096
ds_read_b64_tr_b16 v[82:83], v104 offset:6144
v_accvgpr_read_b32 v119, a219
v_mul_f32 v118, s68, v118
v_mul_f32 v119, s68, v119
v_cvt_pk_bf16_f32 v221, v118, v119
v_mfma_f32_32x32x16_bf16 a[16:31], v[64:67], v[16:19], a[16:31]
s_add_u32 m0, s61, 65536
s_add_u32 s66, s61, 114688
buffer_load_dwordx4 v120, s[44:47], s66 offen offset:0 nt lds
v_accvgpr_read_b32 v116, a220
v_accvgpr_read_b32 v117, a221
s_waitcnt lgkmcnt(6)
v_mfma_f32_32x32x16_bf16 a[32:47], v[68:71], v[0:3], a[32:47]
ds_read_b64_tr_b16 v[84:85], v104 offset:4608
ds_read_b64_tr_b16 v[86:87], v104 offset:6656
v_exp_f32 v32, v32
v_exp_f32 v48, v48
v_mfma_f32_32x32x16_bf16 a[48:63], v[68:71], v[16:19], a[48:63]
buffer_load_dwordx4 v121, s[44:47], s66 offen offset:1024 nt lds
v_exp_f32 v33, v33
v_exp_f32 v49, v49
s_waitcnt lgkmcnt(6)
v_mfma_f32_32x32x16_bf16 a[64:79], v[72:75], v[0:3], a[64:79]
ds_read_b64_tr_b16 v[88:89], v104 offset:5120
ds_read_b64_tr_b16 v[90:91], v104 offset:7168
v_exp_f32 v34, v34
v_exp_f32 v50, v50
v_mfma_f32_32x32x16_bf16 a[80:95], v[72:75], v[16:19], a[80:95]
v_add_f32 v108, v32, v33
v_add_f32 v109, v48, v49
v_cvt_pk_bf16_f32 v32, v32, v33
v_cvt_pk_bf16_f32 v48, v48, v49
v_mul_f32 v116, s68, v116
s_waitcnt lgkmcnt(6)
v_mfma_f32_32x32x16_bf16 a[96:111], v[76:79], v[0:3], a[96:111]
buffer_load_dwordx4 v122, s[44:47], s66 offen offset:2048 nt lds
ds_read_b64_tr_b16 v[92:93], v104 offset:5632
ds_read_b64_tr_b16 v[94:95], v104 offset:7680
v_add_u32 v96, 0x8000, v96
v_exp_f32 v35, v35
v_mfma_f32_32x32x16_bf16 a[112:127], v[76:79], v[16:19], a[112:127]
v_exp_f32 v51, v51
v_add_f32 v108, v108, v34
v_add_f32 v109, v109, v50
v_mul_f32 v117, s68, v117
s_waitcnt lgkmcnt(6)
v_mfma_f32_32x32x16_bf16 a[0:15], v[80:83], v[8:11], a[0:15]
buffer_load_dwordx4 v123, s[44:47], s66 offen offset:3072 nt lds
ds_read_b128 v[64:67], v96 offset:0
v_add_u32 v97, 0x8000, v97
v_exp_f32 v36, v36
v_cvt_pk_bf16_f32 v222, v116, v117
v_mfma_f32_32x32x16_bf16 a[16:31], v[80:83], v[24:27], a[16:31]
v_exp_f32 v52, v52
v_add_f32 v108, v108, v35
v_add_f32 v109, v109, v51
v_cvt_pk_bf16_f32 v33, v34, v35
s_waitcnt lgkmcnt(5)
v_mfma_f32_32x32x16_bf16 a[32:47], v[84:87], v[8:11], a[32:47]
ds_read_b128 v[68:71], v97 offset:0
v_add_u32 v98, 0x8000, v98
v_cvt_pk_bf16_f32 v49, v50, v51
v_exp_f32 v37, v37
v_accvgpr_read_b32 v118, a222
v_mfma_f32_32x32x16_bf16 a[48:63], v[84:87], v[24:27], a[48:63]
s_add_u32 m0, s61, 81920
s_nop 0
buffer_load_dwordx4 v124, s[48:51], s66 offen offset:0 nt lds
v_exp_f32 v53, v53
s_waitcnt lgkmcnt(4)
v_mfma_f32_32x32x16_bf16 a[64:79], v[88:91], v[8:11], a[64:79]
ds_read_b128 v[72:75], v98 offset:0
v_add_u32 v99, 0x8000, v99
v_add_f32 v108, v108, v36
v_add_f32 v109, v109, v52
v_exp_f32 v38, v38
v_mfma_f32_32x32x16_bf16 a[80:95], v[88:91], v[24:27], a[80:95]
buffer_load_dwordx4 v125, s[48:51], s66 offen offset:1024 nt lds
v_exp_f32 v54, v54
v_add_f32 v108, v108, v37
v_add_f32 v109, v109, v53
s_waitcnt lgkmcnt(3)
v_mfma_f32_32x32x16_bf16 a[96:111], v[92:95], v[8:11], a[96:111]
ds_read_b128 v[76:79], v99 offset:0
v_add_u32 v100, 0x8000, v100
v_cvt_pk_bf16_f32 v34, v36, v37
v_cvt_pk_bf16_f32 v50, v52, v53
v_exp_f32 v39, v39
v_mfma_f32_32x32x16_bf16 a[112:127], v[92:95], v[24:27], a[112:127]
v_exp_f32 v55, v55
v_add_f32 v108, v108, v38
v_add_f32 v109, v109, v54
v_accvgpr_read_b32 v119, a223
s_waitcnt lgkmcnt(3)
v_mfma_f32_32x32x16_bf16 v[0:15], v[64:67], a[128:131], v[128:143]
buffer_load_dwordx4 v124, s[48:51], s66 offen offset:2048 nt lds
ds_read_b128 v[80:83], v100 offset:0
v_add_u32 v101, 0x8000, v101
v_exp_f32 v40, v40
v_mul_f32 v118, s68, v118
v_mfma_f32_32x32x16_bf16 v[16:31], v[64:67], a[160:163], v[144:159]
v_exp_f32 v56, v56
v_add_f32 v108, v108, v39
v_add_f32 v109, v109, v55
v_cvt_pk_bf16_f32 v35, v38, v39
s_waitcnt lgkmcnt(3)
v_mfma_f32_32x32x16_bf16 v[0:15], v[68:71], a[132:135], v[0:15]
buffer_load_dwordx4 v125, s[48:51], s66 offen offset:3072 nt lds
ds_read_b128 v[84:87], v101 offset:0
v_add_u32 v102, 0x8000, v102
v_cvt_pk_bf16_f32 v51, v54, v55
v_exp_f32 v41, v41
v_mfma_f32_32x32x16_bf16 v[16:31], v[68:71], a[164:167], v[16:31]
v_exp_f32 v57, v57
v_add_f32 v108, v108, v40
v_add_f32 v109, v109, v56
v_mul_f32 v119, s68, v119
s_waitcnt lgkmcnt(3)
v_mfma_f32_32x32x16_bf16 v[0:15], v[72:75], a[136:139], v[0:15]
ds_read_b128 v[88:91], v102 offset:0
v_add_u32 v103, 0x8000, v103
v_exp_f32 v42, v42
v_exp_f32 v58, v58
v_mfma_f32_32x32x16_bf16 v[16:31], v[72:75], a[168:171], v[16:31]
v_add_f32 v108, v108, v41
v_add_f32 v109, v109, v57
v_cvt_pk_bf16_f32 v40, v40, v41
v_cvt_pk_bf16_f32 v56, v56, v57
v_cvt_pk_bf16_f32 v223, v118, v119
s_waitcnt lgkmcnt(3)
v_mfma_f32_32x32x16_bf16 v[0:15], v[76:79], a[140:143], v[0:15]
ds_read_b128 v[92:95], v103 offset:0
v_exp_f32 v43, v43
v_exp_f32 v59, v59
v_add_f32 v108, v108, v42
v_mfma_f32_32x32x16_bf16 v[16:31], v[76:79], a[172:175], v[16:31]
v_add_f32 v109, v109, v58
v_exp_f32 v44, v44
v_exp_f32 v60, v60
s_waitcnt lgkmcnt(3)
v_mfma_f32_32x32x16_bf16 v[0:15], v[80:83], a[144:147], v[0:15]
ds_read_b64_tr_b16 v[64:65], v104 offset:8192
ds_read_b64_tr_b16 v[66:67], v104 offset:10240
v_add_f32 v108, v108, v43
v_add_f32 v109, v109, v59
v_cvt_pk_bf16_f32 v41, v42, v43
v_cvt_pk_bf16_f32 v57, v58, v59
v_mfma_f32_32x32x16_bf16 v[16:31], v[80:83], a[176:179], v[16:31]
v_exp_f32 v45, v45
v_exp_f32 v61, v61
v_add_f32 v108, v108, v44
s_waitcnt lgkmcnt(4)
v_mfma_f32_32x32x16_bf16 v[0:15], v[84:87], a[148:151], v[0:15]
ds_read_b64_tr_b16 v[68:69], v104 offset:8704
ds_read_b64_tr_b16 v[70:71], v104 offset:10752
v_add_f32 v109, v109, v60
v_exp_f32 v46, v46
v_accvgpr_read_b32 v116, a224
v_mfma_f32_32x32x16_bf16 v[16:31], v[84:87], a[180:183], v[16:31]
v_exp_f32 v62, v62
v_add_f32 v108, v108, v45
v_add_f32 v109, v109, v61
v_cvt_pk_bf16_f32 v42, v44, v45
s_waitcnt lgkmcnt(5)
v_mfma_f32_32x32x16_bf16 v[0:15], v[88:91], a[152:155], v[0:15]
ds_read_b64_tr_b16 v[72:73], v104 offset:9216
ds_read_b64_tr_b16 v[74:75], v104 offset:11264
v_cvt_pk_bf16_f32 v58, v60, v61
v_exp_f32 v47, v47
v_accvgpr_read_b32 v117, a225
v_mfma_f32_32x32x16_bf16 v[16:31], v[88:91], a[184:187], v[16:31]
v_exp_f32 v63, v63
v_add_f32 v108, v108, v46
v_add_f32 v109, v109, v62
v_add_f32 v108, v108, v47
s_waitcnt lgkmcnt(6)
v_mfma_f32_32x32x16_bf16 v[0:15], v[92:95], a[156:159], v[0:15]
ds_read_b64_tr_b16 v[76:77], v104 offset:9728
ds_read_b64_tr_b16 v[78:79], v104 offset:11776
v_add_f32 v109, v109, v63
v_cvt_pk_bf16_f32 v43, v46, v47
v_cvt_pk_bf16_f32 v59, v62, v63
v_max_f32 v160, v108, v109
v_mfma_f32_32x32x16_bf16 v[16:31], v[92:95], a[188:191], v[16:31]
v_cmp_nge_f32 vcc, 0x45800000, v160
s_cbranch_vccnz L_tr_7
L_ret_7:
v_add_f32 v106, v106, v108
v_add_f32 v107, v107, v109
v_mul_f32 v116, s68, v116
s_waitcnt lgkmcnt(6)
v_mfma_f32_32x32x16_bf16 a[0:15], v[64:67], v[32:35], a[0:15]
ds_read_b64_tr_b16 v[80:81], v104 offset:12288
ds_read_b64_tr_b16 v[82:83], v104 offset:14336
v_mul_f32 v117, s68, v117
v_cvt_pk_bf16_f32 v232, v116, v117
v_accvgpr_read_b32 v118, a226
v_accvgpr_read_b32 v119, a227
v_mfma_f32_32x32x16_bf16 a[16:31], v[64:67], v[48:51], a[16:31]
v_mul_f32 v118, s68, v118
v_mul_f32 v119, s68, v119
v_cvt_pk_bf16_f32 v233, v118, v119
v_accvgpr_read_b32 v116, a228
v_accvgpr_read_b32 v117, a229
s_waitcnt lgkmcnt(6)
v_mfma_f32_32x32x16_bf16 a[32:47], v[68:71], v[32:35], a[32:47]
ds_read_b64_tr_b16 v[84:85], v104 offset:12800
ds_read_b64_tr_b16 v[86:87], v104 offset:14848
v_exp_f32 v0, v0
v_exp_f32 v16, v16
v_mfma_f32_32x32x16_bf16 a[48:63], v[68:71], v[48:51], a[48:63]
v_exp_f32 v1, v1
v_exp_f32 v17, v17
v_mul_f32 v116, s68, v116
s_waitcnt lgkmcnt(6)
v_mfma_f32_32x32x16_bf16 a[64:79], v[72:75], v[32:35], a[64:79]
ds_read_b64_tr_b16 v[88:89], v104 offset:13312
ds_read_b64_tr_b16 v[90:91], v104 offset:15360
v_exp_f32 v2, v2
v_exp_f32 v18, v18
v_mfma_f32_32x32x16_bf16 a[80:95], v[72:75], v[48:51], a[80:95]
v_add_f32 v108, v0, v1
v_add_f32 v109, v16, v17
v_cvt_pk_bf16_f32 v0, v0, v1
v_cvt_pk_bf16_f32 v16, v16, v17
v_mul_f32 v117, s68, v117
s_waitcnt lgkmcnt(6)
v_mfma_f32_32x32x16_bf16 a[96:111], v[76:79], v[32:35], a[96:111]
ds_read_b64_tr_b16 v[92:93], v104 offset:13824
ds_read_b64_tr_b16 v[94:95], v104 offset:15872
v_exp_f32 v3, v3
v_exp_f32 v19, v19
v_mfma_f32_32x32x16_bf16 a[112:127], v[76:79], v[48:51], a[112:127]
v_add_f32 v108, v108, v2
v_add_f32 v109, v109, v18
v_exp_f32 v4, v4
v_cvt_pk_bf16_f32 v234, v116, v117
s_waitcnt lgkmcnt(6)
v_mfma_f32_32x32x16_bf16 a[0:15], v[80:83], v[40:43], a[0:15]
ds_read_b128 v[64:67], v96 offset:8192
v_exp_f32 v20, v20
v_add_f32 v108, v108, v3
v_add_f32 v109, v109, v19
v_cvt_pk_bf16_f32 v1, v2, v3
v_mfma_f32_32x32x16_bf16 a[16:31], v[80:83], v[56:59], a[16:31]
v_cvt_pk_bf16_f32 v17, v18, v19
v_exp_f32 v5, v5
v_exp_f32 v21, v21
s_waitcnt lgkmcnt(5)
v_mfma_f32_32x32x16_bf16 a[32:47], v[84:87], v[40:43], a[32:47]
ds_read_b128 v[68:71], v97 offset:8192
v_add_f32 v108, v108, v4
v_add_f32 v109, v109, v20
v_exp_f32 v6, v6
v_accvgpr_read_b32 v118, a230
v_mfma_f32_32x32x16_bf16 a[48:63], v[84:87], v[56:59], a[48:63]
v_exp_f32 v22, v22
v_add_f32 v108, v108, v5
v_add_f32 v109, v109, v21
v_cvt_pk_bf16_f32 v2, v4, v5
s_waitcnt lgkmcnt(4)
v_mfma_f32_32x32x16_bf16 a[64:79], v[88:91], v[40:43], a[64:79]
ds_read_b128 v[72:75], v98 offset:8192
v_cvt_pk_bf16_f32 v18, v20, v21
v_exp_f32 v7, v7
v_exp_f32 v23, v23
v_mfma_f32_32x32x16_bf16 a[80:95], v[88:91], v[56:59], a[80:95]
v_add_f32 v108, v108, v6
v_add_f32 v109, v109, v22
v_exp_f32 v8, v8
v_accvgpr_read_b32 v119, a231
s_waitcnt lgkmcnt(3)
v_mfma_f32_32x32x16_bf16 a[96:111], v[92:95], v[40:43], a[96:111]
ds_read_b128 v[76:79], v99 offset:8192
v_exp_f32 v24, v24
v_add_f32 v108, v108, v7
v_add_f32 v109, v109, v23
v_cvt_pk_bf16_f32 v3, v6, v7
v_mfma_f32_32x32x16_bf16 a[112:127], v[92:95], v[56:59], a[112:127]
v_cvt_pk_bf16_f32 v19, v22, v23
v_exp_f32 v9, v9
v_exp_f32 v25, v25
s_waitcnt lgkmcnt(3)
v_mfma_f32_32x32x16_bf16 v[32:47], v[64:67], a[128:131], v[128:143]
ds_read_b128 v[80:83], v100 offset:8192
v_add_f32 v108, v108, v8
v_add_f32 v109, v109, v24
v_exp_f32 v10, v10
v_mul_f32 v118, s68, v118
v_mfma_f32_32x32x16_bf16 v[48:63], v[64:67], a[160:163], v[144:159]
v_exp_f32 v26, v26
v_add_f32 v108, v108, v9
v_add_f32 v109, v109, v25
v_cvt_pk_bf16_f32 v8, v8, v9
s_waitcnt lgkmcnt(3)
v_mfma_f32_32x32x16_bf16 v[32:47], v[68:71], a[132:135], v[32:47]
ds_read_b128 v[84:87], v101 offset:8192
v_cvt_pk_bf16_f32 v24, v24, v25
v_exp_f32 v11, v11
v_exp_f32 v27, v27
v_mfma_f32_32x32x16_bf16 v[48:63], v[68:71], a[164:167], v[48:63]
v_add_f32 v108, v108, v10
v_add_f32 v109, v109, v26
v_exp_f32 v12, v12
v_mul_f32 v119, s68, v119
s_waitcnt lgkmcnt(3)
v_mfma_f32_32x32x16_bf16 v[32:47], v[72:75], a[136:139], v[32:47]
ds_read_b128 v[88:91], v102 offset:8192
v_exp_f32 v28, v28
v_add_f32 v108, v108, v11
v_add_f32 v109, v109, v27
v_cvt_pk_bf16_f32 v9, v10, v11
v_mfma_f32_32x32x16_bf16 v[48:63], v[72:75], a[168:171], v[48:63]
v_cvt_pk_bf16_f32 v25, v26, v27
v_exp_f32 v13, v13
v_exp_f32 v29, v29
s_waitcnt lgkmcnt(3)
v_mfma_f32_32x32x16_bf16 v[32:47], v[76:79], a[140:143], v[32:47]
ds_read_b128 v[92:95], v103 offset:8192
v_add_u32 v104, 0x8000, v104
v_add_f32 v108, v108, v12
v_add_f32 v109, v109, v28
v_exp_f32 v14, v14
v_mfma_f32_32x32x16_bf16 v[48:63], v[76:79], a[172:175], v[48:63]
v_exp_f32 v30, v30
v_add_f32 v108, v108, v13
v_add_f32 v109, v109, v29
v_cvt_pk_bf16_f32 v10, v12, v13
s_waitcnt lgkmcnt(3)
v_mfma_f32_32x32x16_bf16 v[32:47], v[80:83], a[144:147], v[32:47]
ds_read_b64_tr_b16 v[64:65], v104 offset:0
ds_read_b64_tr_b16 v[66:67], v104 offset:2048
v_cvt_pk_bf16_f32 v26, v28, v29
v_exp_f32 v15, v15
v_cvt_pk_bf16_f32 v235, v118, v119
v_mfma_f32_32x32x16_bf16 v[48:63], v[80:83], a[176:179], v[48:63]
v_exp_f32 v31, v31
v_add_f32 v108, v108, v14
v_add_f32 v109, v109, v30
v_add_f32 v108, v108, v15
s_waitcnt lgkmcnt(4)
v_mfma_f32_32x32x16_bf16 v[32:47], v[84:87], a[148:151], v[32:47]
ds_read_b64_tr_b16 v[68:69], v104 offset:512
ds_read_b64_tr_b16 v[70:71], v104 offset:2560
v_add_f32 v109, v109, v31
v_cvt_pk_bf16_f32 v11, v14, v15
v_cvt_pk_bf16_f32 v27, v30, v31
v_max_f32 v160, v108, v109
v_mfma_f32_32x32x16_bf16 v[48:63], v[84:87], a[180:183], v[48:63]
v_cmp_nge_f32 vcc, 0x45800000, v160
v_accvgpr_read_b32 v116, a232
v_accvgpr_read_b32 v117, a233
v_mul_f32 v116, s68, v116
v_mul_f32 v117, s68, v117
s_waitcnt lgkmcnt(5)
v_mfma_f32_32x32x16_bf16 v[32:47], v[88:91], a[152:155], v[32:47]
s_waitcnt vmcnt(16)
s_barrier
ds_read_b64_tr_b16 v[72:73], v104 offset:1024
ds_read_b64_tr_b16 v[74:75], v104 offset:3072
v_cvt_pk_bf16_f32 v236, v116, v117
v_accvgpr_read_b32 v118, a234
v_mfma_f32_32x32x16_bf16 v[48:63], v[88:91], a[184:187], v[48:63]
v_accvgpr_read_b32 v119, a235
v_mul_f32 v118, s68, v118
v_mul_f32 v119, s68, v119
v_cvt_pk_bf16_f32 v237, v118, v119
v_accvgpr_read_b32 v116, a236
s_waitcnt lgkmcnt(6)
v_mfma_f32_32x32x16_bf16 v[32:47], v[92:95], a[156:159], v[32:47]
ds_read_b64_tr_b16 v[76:77], v104 offset:1536
ds_read_b64_tr_b16 v[78:79], v104 offset:3584
v_accvgpr_read_b32 v117, a237
v_mul_f32 v116, s68, v116
v_mul_f32 v117, s68, v117
v_cvt_pk_bf16_f32 v238, v116, v117
v_mfma_f32_32x32x16_bf16 v[48:63], v[92:95], a[188:191], v[48:63]
s_cbranch_vccnz L_tr_8
L_ret_8:
v_add_f32 v106, v106, v108
v_add_f32 v107, v107, v109
v_accvgpr_read_b32 v118, a238
v_accvgpr_read_b32 v119, a239
s_waitcnt lgkmcnt(6)
v_mfma_f32_32x32x16_bf16 a[0:15], v[64:67], v[0:3], a[0:15]
ds_read_b64_tr_b16 v[80:81], v104 offset:4096
ds_read_b64_tr_b16 v[82:83], v104 offset:6144
v_mul_f32 v118, s68, v118
v_mul_f32 v119, s68, v119
v_cvt_pk_bf16_f32 v239, v118, v119
v_accvgpr_read_b32 v116, a240
v_mfma_f32_32x32x16_bf16 a[16:31], v[64:67], v[16:19], a[16:31]
s_add_u32 m0, s61, 98304
s_add_u32 s66, s61, 131072
buffer_load_dwordx4 v120, s[44:47], s66 offen offset:0 nt lds
v_accvgpr_read_b32 v117, a241
v_mul_f32 v116, s68, v116
s_waitcnt lgkmcnt(6)
v_mfma_f32_32x32x16_bf16 a[32:47], v[68:71], v[0:3], a[32:47]
ds_read_b64_tr_b16 v[84:85], v104 offset:4608
ds_read_b64_tr_b16 v[86:87], v104 offset:6656
v_exp_f32 v32, v32
v_exp_f32 v48, v48
v_mfma_f32_32x32x16_bf16 a[48:63], v[68:71], v[16:19], a[48:63]
buffer_load_dwordx4 v121, s[44:47], s66 offen offset:1024 nt lds
v_exp_f32 v33, v33
v_exp_f32 v49, v49
s_waitcnt lgkmcnt(6)
v_mfma_f32_32x32x16_bf16 a[64:79], v[72:75], v[0:3], a[64:79]
ds_read_b64_tr_b16 v[88:89], v104 offset:5120
ds_read_b64_tr_b16 v[90:91], v104 offset:7168
v_exp_f32 v34, v34
v_exp_f32 v50, v50
v_mfma_f32_32x32x16_bf16 a[80:95], v[72:75], v[16:19], a[80:95]
v_add_f32 v108, v32, v33
v_add_f32 v109, v48, v49
v_cvt_pk_bf16_f32 v32, v32, v33
v_cvt_pk_bf16_f32 v48, v48, v49
v_mul_f32 v117, s68, v117
s_waitcnt lgkmcnt(6)
v_mfma_f32_32x32x16_bf16 a[96:111], v[76:79], v[0:3], a[96:111]
buffer_load_dwordx4 v122, s[44:47], s66 offen offset:2048 nt lds
ds_read_b64_tr_b16 v[92:93], v104 offset:5632
ds_read_b64_tr_b16 v[94:95], v104 offset:7680
v_add_u32 v96, 0xfffe0000, v96
v_exp_f32 v35, v35
v_mfma_f32_32x32x16_bf16 a[112:127], v[76:79], v[16:19], a[112:127]
v_exp_f32 v51, v51
v_add_f32 v108, v108, v34
v_add_f32 v109, v109, v50
v_cvt_pk_bf16_f32 v248, v116, v117
s_waitcnt lgkmcnt(6)
v_mfma_f32_32x32x16_bf16 a[0:15], v[80:83], v[8:11], a[0:15]
buffer_load_dwordx4 v123, s[44:47], s66 offen offset:3072 nt lds
ds_read_b128 v[64:67], v96 offset:0
v_add_u32 v97, 0xfffe0000, v97
v_exp_f32 v36, v36
v_accvgpr_read_b32 v118, a242
v_mfma_f32_32x32x16_bf16 a[16:31], v[80:83], v[24:27], a[16:31]
v_exp_f32 v52, v52
v_add_f32 v108, v108, v35
v_add_f32 v109, v109, v51
v_cvt_pk_bf16_f32 v33, v34, v35
s_waitcnt lgkmcnt(5)
v_mfma_f32_32x32x16_bf16 a[32:47], v[84:87], v[8:11], a[32:47]
ds_read_b128 v[68:71], v97 offset:0
v_add_u32 v98, 0xfffe0000, v98
v_cvt_pk_bf16_f32 v49, v50, v51
v_exp_f32 v37, v37
v_accvgpr_read_b32 v119, a243
v_mfma_f32_32x32x16_bf16 a[48:63], v[84:87], v[24:27], a[48:63]
s_add_u32 m0, s61, 114688
s_nop 0
buffer_load_dwordx4 v124, s[48:51], s66 offen offset:0 nt lds
v_exp_f32 v53, v53
s_waitcnt lgkmcnt(4)
v_mfma_f32_32x32x16_bf16 a[64:79], v[88:91], v[8:11], a[64:79]
ds_read_b128 v[72:75], v98 offset:0
v_add_u32 v99, 0xfffe0000, v99
v_add_f32 v108, v108, v36
v_add_f32 v109, v109, v52
v_exp_f32 v38, v38
v_mfma_f32_32x32x16_bf16 a[80:95], v[88:91], v[24:27], a[80:95]
buffer_load_dwordx4 v125, s[48:51], s66 offen offset:1024 nt lds
v_exp_f32 v54, v54
v_add_f32 v108, v108, v37
v_add_f32 v109, v109, v53
s_waitcnt lgkmcnt(3)
v_mfma_f32_32x32x16_bf16 a[96:111], v[92:95], v[8:11], a[96:111]
ds_read_b128 v[76:79], v99 offset:0
v_add_u32 v100, 0xfffe0000, v100
v_cvt_pk_bf16_f32 v34, v36, v37
v_cvt_pk_bf16_f32 v50, v52, v53
v_exp_f32 v39, v39
v_mfma_f32_32x32x16_bf16 a[112:127], v[92:95], v[24:27], a[112:127]
v_exp_f32 v55, v55
v_add_f32 v108, v108, v38
v_add_f32 v109, v109, v54
v_mul_f32 v118, s68, v118
s_waitcnt lgkmcnt(3)
v_mfma_f32_32x32x16_bf16 v[0:15], v[64:67], a[128:131], v[128:143]
buffer_load_dwordx4 v124, s[48:51], s66 offen offset:2048 nt lds
ds_read_b128 v[80:83], v100 offset:0
v_add_u32 v101, 0xfffe0000, v101
v_exp_f32 v40, v40
v_mul_f32 v119, s68, v119
v_mfma_f32_32x32x16_bf16 v[16:31], v[64:67], a[160:163], v[144:159]
v_exp_f32 v56, v56
v_add_f32 v108, v108, v39
v_add_f32 v109, v109, v55
v_cvt_pk_bf16_f32 v35, v38, v39
s_waitcnt lgkmcnt(3)
v_mfma_f32_32x32x16_bf16 v[0:15], v[68:71], a[132:135], v[0:15]
buffer_load_dwordx4 v125, s[48:51], s66 offen offset:3072 nt lds
ds_read_b128 v[84:87], v101 offset:0
v_add_u32 v102, 0xfffe0000, v102
v_cvt_pk_bf16_f32 v51, v54, v55
v_exp_f32 v41, v41
v_mfma_f32_32x32x16_bf16 v[16:31], v[68:71], a[164:167], v[16:31]
v_exp_f32 v57, v57
v_add_f32 v108, v108, v40
v_add_f32 v109, v109, v56
v_cvt_pk_bf16_f32 v249, v118, v119
s_waitcnt lgkmcnt(3)
v_mfma_f32_32x32x16_bf16 v[0:15], v[72:75], a[136:139], v[0:15]
ds_read_b128 v[88:91], v102 offset:0
v_add_u32 v103, 0xfffe0000, v103
v_exp_f32 v42, v42
v_exp_f32 v58, v58
v_mfma_f32_32x32x16_bf16 v[16:31], v[72:75], a[168:171], v[16:31]
v_add_f32 v108, v108, v41
v_add_f32 v109, v109, v57
v_cvt_pk_bf16_f32 v40, v40, v41
v_cvt_pk_bf16_f32 v56, v56, v57
v_accvgpr_read_b32 v116, a244
s_waitcnt lgkmcnt(3)
v_mfma_f32_32x32x16_bf16 v[0:15], v[76:79], a[140:143], v[0:15]
ds_read_b128 v[92:95], v103 offset:0
v_exp_f32 v43, v43
v_exp_f32 v59, v59
v_add_f32 v108, v108, v42
v_mfma_f32_32x32x16_bf16 v[16:31], v[76:79], a[172:175], v[16:31]
v_add_f32 v109, v109, v58
v_exp_f32 v44, v44
v_exp_f32 v60, v60
s_waitcnt lgkmcnt(3)
v_mfma_f32_32x32x16_bf16 v[0:15], v[80:83], a[144:147], v[0:15]
ds_read_b64_tr_b16 v[64:65], v104 offset:8192
ds_read_b64_tr_b16 v[66:67], v104 offset:10240
v_add_f32 v108, v108, v43
v_add_f32 v109, v109, v59
v_cvt_pk_bf16_f32 v41, v42, v43
v_cvt_pk_bf16_f32 v57, v58, v59
v_mfma_f32_32x32x16_bf16 v[16:31], v[80:83], a[176:179], v[16:31]
v_exp_f32 v45, v45
v_exp_f32 v61, v61
v_add_f32 v108, v108, v44
s_waitcnt lgkmcnt(4)
v_mfma_f32_32x32x16_bf16 v[0:15], v[84:87], a[148:151], v[0:15]
ds_read_b64_tr_b16 v[68:69], v104 offset:8704
ds_read_b64_tr_b16 v[70:71], v104 offset:10752
v_add_f32 v109, v109, v60
v_exp_f32 v46, v46
v_accvgpr_read_b32 v117, a245
v_mfma_f32_32x32x16_bf16 v[16:31], v[84:87], a[180:183], v[16:31]
v_exp_f32 v62, v62
v_add_f32 v108, v108, v45
v_add_f32 v109, v109, v61
v_cvt_pk_bf16_f32 v42, v44, v45
s_waitcnt lgkmcnt(5)
v_mfma_f32_32x32x16_bf16 v[0:15], v[88:91], a[152:155], v[0:15]
ds_read_b64_tr_b16 v[72:73], v104 offset:9216
ds_read_b64_tr_b16 v[74:75], v104 offset:11264
v_cvt_pk_bf16_f32 v58, v60, v61
v_exp_f32 v47, v47
v_mul_f32 v116, s68, v116
v_mfma_f32_32x32x16_bf16 v[16:31], v[88:91], a[184:187], v[16:31]
v_exp_f32 v63, v63
v_add_f32 v108, v108, v46
v_add_f32 v109, v109, v62
v_add_f32 v108, v108, v47
s_waitcnt lgkmcnt(6)
v_mfma_f32_32x32x16_bf16 v[0:15], v[92:95], a[156:159], v[0:15]
ds_read_b64_tr_b16 v[76:77], v104 offset:9728
ds_read_b64_tr_b16 v[78:79], v104 offset:11776
v_add_f32 v109, v109, v63
v_cvt_pk_bf16_f32 v43, v46, v47
v_cvt_pk_bf16_f32 v59, v62, v63
v_max_f32 v160, v108, v109
v_mfma_f32_32x32x16_bf16 v[16:31], v[92:95], a[188:191], v[16:31]
v_cmp_nge_f32 vcc, 0x45800000, v160
s_cbranch_vccnz L_tr_9
L_ret_9:
v_add_f32 v106, v106, v108
v_add_f32 v107, v107, v109
v_mul_f32 v117, s68, v117
s_waitcnt lgkmcnt(6)
v_mfma_f32_32x32x16_bf16 a[0:15], v[64:67], v[32:35], a[0:15]
ds_read_b64_tr_b16 v[80:81], v104 offset:12288
ds_read_b64_tr_b16 v[82:83], v104 offset:14336
v_cvt_pk_bf16_f32 v250, v116, v117
v_accvgpr_read_b32 v118, a246
v_accvgpr_read_b32 v119, a247
v_mul_f32 v118, s68, v118
v_mfma_f32_32x32x16_bf16 a[16:31], v[64:67], v[48:51], a[16:31]
v_mul_f32 v119, s68, v119
v_cvt_pk_bf16_f32 v251, v118, v119
v_accvgpr_read_b32 v116, a248
v_accvgpr_read_b32 v117, a249
v_mul_f32 v116, s68, v116
s_waitcnt lgkmcnt(6)
v_mfma_f32_32x32x16_bf16 a[32:47], v[68:71], v[32:35], a[32:47]
ds_read_b64_tr_b16 v[84:85], v104 offset:12800
ds_read_b64_tr_b16 v[86:87], v104 offset:14848
v_exp_f32 v0, v0
v_exp_f32 v16, v16
v_mfma_f32_32x32x16_bf16 a[48:63], v[68:71], v[48:51], a[48:63]
v_exp_f32 v1, v1
v_exp_f32 v17, v17
v_mul_f32 v117, s68, v117
s_waitcnt lgkmcnt(6)
v_mfma_f32_32x32x16_bf16 a[64:79], v[72:75], v[32:35], a[64:79]
ds_read_b64_tr_b16 v[88:89], v104 offset:13312
ds_read_b64_tr_b16 v[90:91], v104 offset:15360
v_exp_f32 v2, v2
v_exp_f32 v18, v18
v_mfma_f32_32x32x16_bf16 a[80:95], v[72:75], v[48:51], a[80:95]
v_add_f32 v108, v0, v1
v_add_f32 v109, v16, v17
v_cvt_pk_bf16_f32 v0, v0, v1
v_cvt_pk_bf16_f32 v16, v16, v17
v_cvt_pk_bf16_f32 v252, v116, v117
s_waitcnt lgkmcnt(6)
v_mfma_f32_32x32x16_bf16 a[96:111], v[76:79], v[32:35], a[96:111]
ds_read_b64_tr_b16 v[92:93], v104 offset:13824
ds_read_b64_tr_b16 v[94:95], v104 offset:15872
v_exp_f32 v3, v3
v_exp_f32 v19, v19
v_mfma_f32_32x32x16_bf16 a[112:127], v[76:79], v[48:51], a[112:127]
v_add_f32 v108, v108, v2
v_add_f32 v109, v109, v18
v_exp_f32 v4, v4
v_accvgpr_read_b32 v118, a250
s_waitcnt lgkmcnt(6)
v_mfma_f32_32x32x16_bf16 a[0:15], v[80:83], v[40:43], a[0:15]
ds_read_b128 v[64:67], v96 offset:8192
v_exp_f32 v20, v20
v_add_f32 v108, v108, v3
v_add_f32 v109, v109, v19
v_cvt_pk_bf16_f32 v1, v2, v3
v_mfma_f32_32x32x16_bf16 a[16:31], v[80:83], v[56:59], a[16:31]
v_cvt_pk_bf16_f32 v17, v18, v19
v_exp_f32 v5, v5
v_exp_f32 v21, v21
s_waitcnt lgkmcnt(5)
v_mfma_f32_32x32x16_bf16 a[32:47], v[84:87], v[40:43], a[32:47]
ds_read_b128 v[68:71], v97 offset:8192
v_add_f32 v108, v108, v4
v_add_f32 v109, v109, v20
v_exp_f32 v6, v6
v_accvgpr_read_b32 v119, a251
v_mfma_f32_32x32x16_bf16 a[48:63], v[84:87], v[56:59], a[48:63]
v_exp_f32 v22, v22
v_add_f32 v108, v108, v5
v_add_f32 v109, v109, v21
v_cvt_pk_bf16_f32 v2, v4, v5
s_waitcnt lgkmcnt(4)
v_mfma_f32_32x32x16_bf16 a[64:79], v[88:91], v[40:43], a[64:79]
ds_read_b128 v[72:75], v98 offset:8192
v_cvt_pk_bf16_f32 v18, v20, v21
v_exp_f32 v7, v7
v_exp_f32 v23, v23
v_mfma_f32_32x32x16_bf16 a[80:95], v[88:91], v[56:59], a[80:95]
v_add_f32 v108, v108, v6
v_add_f32 v109, v109, v22
v_exp_f32 v8, v8
v_mul_f32 v118, s68, v118
s_waitcnt lgkmcnt(3)
v_mfma_f32_32x32x16_bf16 a[96:111], v[92:95], v[40:43], a[96:111]
ds_read_b128 v[76:79], v99 offset:8192
v_exp_f32 v24, v24
v_add_f32 v108, v108, v7
v_add_f32 v109, v109, v23
v_cvt_pk_bf16_f32 v3, v6, v7
v_mfma_f32_32x32x16_bf16 a[112:127], v[92:95], v[56:59], a[112:127]
v_cvt_pk_bf16_f32 v19, v22, v23
v_exp_f32 v9, v9
v_exp_f32 v25, v25
s_waitcnt lgkmcnt(3)
v_mfma_f32_32x32x16_bf16 v[32:47], v[64:67], a[128:131], v[128:143]
ds_read_b128 v[80:83], v100 offset:8192
v_add_f32 v108, v108, v8
v_add_f32 v109, v109, v24
v_exp_f32 v10, v10
v_mul_f32 v119, s68, v119
v_mfma_f32_32x32x16_bf16 v[48:63], v[64:67], a[160:163], v[144:159]
v_exp_f32 v26, v26
v_add_f32 v108, v108, v9
v_add_f32 v109, v109, v25
v_cvt_pk_bf16_f32 v8, v8, v9
s_waitcnt lgkmcnt(3)
v_mfma_f32_32x32x16_bf16 v[32:47], v[68:71], a[132:135], v[32:47]
ds_read_b128 v[84:87], v101 offset:8192
v_cvt_pk_bf16_f32 v24, v24, v25
v_exp_f32 v11, v11
v_exp_f32 v27, v27
v_mfma_f32_32x32x16_bf16 v[48:63], v[68:71], a[164:167], v[48:63]
v_add_f32 v108, v108, v10
v_add_f32 v109, v109, v26
v_exp_f32 v12, v12
v_cvt_pk_bf16_f32 v253, v118, v119
s_waitcnt lgkmcnt(3)
v_mfma_f32_32x32x16_bf16 v[32:47], v[72:75], a[136:139], v[32:47]
ds_read_b128 v[88:91], v102 offset:8192
v_exp_f32 v28, v28
v_add_f32 v108, v108, v11
v_add_f32 v109, v109, v27
v_cvt_pk_bf16_f32 v9, v10, v11
v_mfma_f32_32x32x16_bf16 v[48:63], v[72:75], a[168:171], v[48:63]
v_cvt_pk_bf16_f32 v25, v26, v27
v_exp_f32 v13, v13
v_exp_f32 v29, v29
s_waitcnt lgkmcnt(3)
v_mfma_f32_32x32x16_bf16 v[32:47], v[76:79], a[140:143], v[32:47]
ds_read_b128 v[92:95], v103 offset:8192
v_add_u32 v104, 0xfffe0000, v104
v_add_f32 v108, v108, v12
v_add_f32 v109, v109, v28
v_exp_f32 v14, v14
v_mfma_f32_32x32x16_bf16 v[48:63], v[76:79], a[172:175], v[48:63]
v_exp_f32 v30, v30
v_add_f32 v108, v108, v13
v_add_f32 v109, v109, v29
v_cvt_pk_bf16_f32 v10, v12, v13
s_waitcnt lgkmcnt(3)
v_mfma_f32_32x32x16_bf16 v[32:47], v[80:83], a[144:147], v[32:47]
ds_read_b64_tr_b16 v[64:65], v104 offset:0
ds_read_b64_tr_b16 v[66:67], v104 offset:2048
v_cvt_pk_bf16_f32 v26, v28, v29
v_exp_f32 v15, v15
v_accvgpr_read_b32 v116, a252
v_mfma_f32_32x32x16_bf16 v[48:63], v[80:83], a[176:179], v[48:63]
v_exp_f32 v31, v31
v_add_f32 v108, v108, v14
v_add_f32 v109, v109, v30
v_add_f32 v108, v108, v15
s_waitcnt lgkmcnt(4)
v_mfma_f32_32x32x16_bf16 v[32:47], v[84:87], a[148:151], v[32:47]
ds_read_b64_tr_b16 v[68:69], v104 offset:512
ds_read_b64_tr_b16 v[70:71], v104 offset:2560
v_add_f32 v109, v109, v31
v_cvt_pk_bf16_f32 v11, v14, v15
v_cvt_pk_bf16_f32 v27, v30, v31
v_max_f32 v160, v108, v109
v_mfma_f32_32x32x16_bf16 v[48:63], v[84:87], a[180:183], v[48:63]
v_cmp_nge_f32 vcc, 0x45800000, v160
v_accvgpr_read_b32 v117, a253
v_mul_f32 v116, s68, v116
v_mul_f32 v117, s68, v117
v_cvt_pk_bf16_f32 v254, v116, v117
s_waitcnt lgkmcnt(5)
v_mfma_f32_32x32x16_bf16 v[32:47], v[88:91], a[152:155], v[32:47]
s_waitcnt vmcnt(16)
s_barrier
ds_read_b64_tr_b16 v[72:73], v104 offset:1024
ds_read_b64_tr_b16 v[74:75], v104 offset:3072
v_accvgpr_read_b32 v118, a254
v_accvgpr_read_b32 v119, a255
v_mfma_f32_32x32x16_bf16 v[48:63], v[88:91], a[184:187], v[48:63]
v_mul_f32 v118, s68, v118
v_mul_f32 v119, s68, v119
v_cvt_pk_bf16_f32 v255, v118, v119
v_mov_b32 v112, 0
v_mov_b32 v113, 0
s_waitcnt lgkmcnt(6)
v_mfma_f32_32x32x16_bf16 v[32:47], v[92:95], a[156:159], v[32:47]
ds_read_b64_tr_b16 v[76:77], v104 offset:1536
ds_read_b64_tr_b16 v[78:79], v104 offset:3584
v_mfma_f32_32x32x16_bf16 v[48:63], v[92:95], a[188:191], v[48:63]
s_cbranch_vccnz L_tr_10
L_ret_10:
v_add_f32 v106, v106, v108
v_add_f32 v107, v107, v109
s_waitcnt lgkmcnt(6)
v_mfma_f32_32x32x16_bf16 a[0:15], v[64:67], v[0:3], a[0:15]
ds_read_b64_tr_b16 v[80:81], v104 offset:4096
ds_read_b64_tr_b16 v[82:83], v104 offset:6144
v_mfma_f32_32x32x16_bf16 a[16:31], v[64:67], v[16:19], a[16:31]
s_add_u32 m0, s61, 131072
s_add_u32 s66, s61, 147456
buffer_load_dwordx4 v120, s[44:47], s66 offen offset:0 nt lds
s_waitcnt lgkmcnt(6)
v_mfma_f32_32x32x16_bf16 a[32:47], v[68:71], v[0:3], a[32:47]
ds_read_b64_tr_b16 v[84:85], v104 offset:4608
ds_read_b64_tr_b16 v[86:87], v104 offset:6656
v_exp_f32 v32, v32
v_exp_f32 v48, v48
v_mfma_f32_32x32x16_bf16 a[48:63], v[68:71], v[16:19], a[48:63]
buffer_load_dwordx4 v121, s[44:47], s66 offen offset:1024 nt lds
v_exp_f32 v33, v33
v_exp_f32 v49, v49
s_waitcnt lgkmcnt(6)
v_mfma_f32_32x32x16_bf16 a[64:79], v[72:75], v[0:3], a[64:79]
ds_read_b64_tr_b16 v[88:89], v104 offset:5120
ds_read_b64_tr_b16 v[90:91], v104 offset:7168
v_exp_f32 v34, v34
v_exp_f32 v50, v50
v_mfma_f32_32x32x16_bf16 a[80:95], v[72:75], v[16:19], a[80:95]
v_add_f32 v108, v32, v33
v_add_f32 v109, v48, v49
v_cvt_pk_bf16_f32 v32, v32, v33
v_cvt_pk_bf16_f32 v48, v48, v49
s_waitcnt lgkmcnt(6)
v_mfma_f32_32x32x16_bf16 a[96:111], v[76:79], v[0:3], a[96:111]
buffer_load_dwordx4 v122, s[44:47], s66 offen offset:2048 nt lds
ds_read_b64_tr_b16 v[92:93], v104 offset:5632
ds_read_b64_tr_b16 v[94:95], v104 offset:7680
v_add_u32 v96, 0x8000, v96
v_exp_f32 v35, v35
v_mfma_f32_32x32x16_bf16 a[112:127], v[76:79], v[16:19], a[112:127]
v_exp_f32 v51, v51
v_add_f32 v108, v108, v34
v_add_f32 v109, v109, v50
s_waitcnt lgkmcnt(6)
v_mfma_f32_32x32x16_bf16 a[0:15], v[80:83], v[8:11], a[0:15]
buffer_load_dwordx4 v123, s[44:47], s66 offen offset:3072 nt lds
ds_read_b128 v[64:67], v96 offset:0
v_add_u32 v97, 0x8000, v97
v_exp_f32 v36, v36
v_mfma_f32_32x32x16_bf16 a[16:31], v[80:83], v[24:27], a[16:31]
v_exp_f32 v52, v52
v_add_f32 v108, v108, v35
v_add_f32 v109, v109, v51
v_cvt_pk_bf16_f32 v33, v34, v35
s_waitcnt lgkmcnt(5)
v_mfma_f32_32x32x16_bf16 a[32:47], v[84:87], v[8:11], a[32:47]
ds_read_b128 v[68:71], v97 offset:0
v_add_u32 v98, 0x8000, v98
v_cvt_pk_bf16_f32 v49, v50, v51
v_exp_f32 v37, v37
v_mfma_f32_32x32x16_bf16 a[48:63], v[84:87], v[24:27], a[48:63]
s_add_u32 m0, s61, 147456
s_nop 0
buffer_load_dwordx4 v124, s[48:51], s66 offen offset:0 nt lds
v_exp_f32 v53, v53
s_waitcnt lgkmcnt(4)
v_mfma_f32_32x32x16_bf16 a[64:79], v[88:91], v[8:11], a[64:79]
ds_read_b128 v[72:75], v98 offset:0
v_add_u32 v99, 0x8000, v99
v_add_f32 v108, v108, v36
v_add_f32 v109, v109, v52
v_exp_f32 v38, v38
v_mfma_f32_32x32x16_bf16 a[80:95], v[88:91], v[24:27], a[80:95]
buffer_load_dwordx4 v125, s[48:51], s66 offen offset:1024 nt lds
v_exp_f32 v54, v54
v_add_f32 v108, v108, v37
v_add_f32 v109, v109, v53
s_waitcnt lgkmcnt(3)
v_mfma_f32_32x32x16_bf16 a[96:111], v[92:95], v[8:11], a[96:111]
ds_read_b128 v[76:79], v99 offset:0
v_add_u32 v100, 0x8000, v100
v_cvt_pk_bf16_f32 v34, v36, v37
v_cvt_pk_bf16_f32 v50, v52, v53
v_exp_f32 v39, v39
v_mfma_f32_32x32x16_bf16 a[112:127], v[92:95], v[24:27], a[112:127]
v_exp_f32 v55, v55
v_add_f32 v108, v108, v38
v_add_f32 v109, v109, v54
s_waitcnt lgkmcnt(3)
v_mfma_f32_32x32x16_bf16 v[0:15], v[64:67], a[128:131], v[128:143]
buffer_load_dwordx4 v124, s[48:51], s66 offen offset:2048 nt lds
ds_read_b128 v[80:83], v100 offset:0
v_add_u32 v101, 0x8000, v101
v_exp_f32 v40, v40
v_mfma_f32_32x32x16_bf16 v[16:31], v[64:67], a[160:163], v[144:159]
v_exp_f32 v56, v56
v_add_f32 v108, v108, v39
v_add_f32 v109, v109, v55
v_cvt_pk_bf16_f32 v35, v38, v39
s_waitcnt lgkmcnt(3)
v_mfma_f32_32x32x16_bf16 v[0:15], v[68:71], a[132:135], v[0:15]
buffer_load_dwordx4 v125, s[48:51], s66 offen offset:3072 nt lds
ds_read_b128 v[84:87], v101 offset:0
v_add_u32 v102, 0x8000, v102
v_cvt_pk_bf16_f32 v51, v54, v55
v_exp_f32 v41, v41
v_mfma_f32_32x32x16_bf16 v[16:31], v[68:71], a[164:167], v[16:31]
v_exp_f32 v57, v57
v_add_f32 v108, v108, v40
v_add_f32 v109, v109, v56
s_waitcnt lgkmcnt(3)
v_mfma_f32_32x32x16_bf16 v[0:15], v[72:75], a[136:139], v[0:15]
ds_read_b128 v[88:91], v102 offset:0
v_add_u32 v103, 0x8000, v103
v_exp_f32 v42, v42
v_exp_f32 v58, v58
v_mfma_f32_32x32x16_bf16 v[16:31], v[72:75], a[168:171], v[16:31]
v_add_f32 v108, v108, v41
v_add_f32 v109, v109, v57
v_cvt_pk_bf16_f32 v40, v40, v41
v_cvt_pk_bf16_f32 v56, v56, v57
s_waitcnt lgkmcnt(3)
v_mfma_f32_32x32x16_bf16 v[0:15], v[76:79], a[140:143], v[0:15]
ds_read_b128 v[92:95], v103 offset:0
v_exp_f32 v43, v43
v_exp_f32 v59, v59
v_add_f32 v108, v108, v42
v_mfma_f32_32x32x16_bf16 v[16:31], v[76:79], a[172:175], v[16:31]
v_add_f32 v109, v109, v58
v_exp_f32 v44, v44
v_exp_f32 v60, v60
s_waitcnt lgkmcnt(3)
v_mfma_f32_32x32x16_bf16 v[0:15], v[80:83], a[144:147], v[0:15]
ds_read_b64_tr_b16 v[64:65], v104 offset:8192
ds_read_b64_tr_b16 v[66:67], v104 offset:10240
v_add_f32 v108, v108, v43
v_add_f32 v109, v109, v59
v_cvt_pk_bf16_f32 v41, v42, v43
v_cvt_pk_bf16_f32 v57, v58, v59
v_mfma_f32_32x32x16_bf16 v[16:31], v[80:83], a[176:179], v[16:31]
v_exp_f32 v45, v45
v_exp_f32 v61, v61
v_add_f32 v108, v108, v44
s_waitcnt lgkmcnt(4)
v_mfma_f32_32x32x16_bf16 v[0:15], v[84:87], a[148:151], v[0:15]
ds_read_b64_tr_b16 v[68:69], v104 offset:8704
ds_read_b64_tr_b16 v[70:71], v104 offset:10752
v_add_f32 v109, v109, v60
v_exp_f32 v46, v46
v_mfma_f32_32x32x16_bf16 v[16:31], v[84:87], a[180:183], v[16:31]
v_exp_f32 v62, v62
v_add_f32 v108, v108, v45
v_add_f32 v109, v109, v61
v_cvt_pk_bf16_f32 v42, v44, v45
s_waitcnt lgkmcnt(5)
v_mfma_f32_32x32x16_bf16 v[0:15], v[88:91], a[152:155], v[0:15]
ds_read_b64_tr_b16 v[72:73], v104 offset:9216
ds_read_b64_tr_b16 v[74:75], v104 offset:11264
v_cvt_pk_bf16_f32 v58, v60, v61
v_exp_f32 v47, v47
v_mfma_f32_32x32x16_bf16 v[16:31], v[88:91], a[184:187], v[16:31]
v_exp_f32 v63, v63
v_add_f32 v108, v108, v46
v_add_f32 v109, v109, v62
v_add_f32 v108, v108, v47
s_waitcnt lgkmcnt(6)
v_mfma_f32_32x32x16_bf16 v[0:15], v[92:95], a[156:159], v[0:15]
ds_read_b64_tr_b16 v[76:77], v104 offset:9728
ds_read_b64_tr_b16 v[78:79], v104 offset:11776
v_add_f32 v109, v109, v63
v_cvt_pk_bf16_f32 v43, v46, v47
v_cvt_pk_bf16_f32 v59, v62, v63
v_max_f32 v160, v108, v109
v_mfma_f32_32x32x16_bf16 v[16:31], v[92:95], a[188:191], v[16:31]
v_cmp_nge_f32 vcc, 0x45800000, v160
s_cbranch_vccnz L_tr_11
L_ret_11:
v_add_f32 v106, v106, v108
v_add_f32 v107, v107, v109
s_waitcnt lgkmcnt(6)
v_mfma_f32_32x32x16_bf16 a[0:15], v[64:67], v[32:35], a[0:15]
ds_read_b64_tr_b16 v[80:81], v104 offset:12288
ds_read_b64_tr_b16 v[82:83], v104 offset:14336
v_mfma_f32_32x32x16_bf16 a[16:31], v[64:67], v[48:51], a[16:31]
s_waitcnt lgkmcnt(6)
v_mfma_f32_32x32x16_bf16 a[32:47], v[68:71], v[32:35], a[32:47]
ds_read_b64_tr_b16 v[84:85], v104 offset:12800
ds_read_b64_tr_b16 v[86:87], v104 offset:14848
v_exp_f32 v0, v0
v_exp_f32 v16, v16
v_mfma_f32_32x32x16_bf16 a[48:63], v[68:71], v[48:51], a[48:63]
v_exp_f32 v1, v1
v_exp_f32 v17, v17
s_waitcnt lgkmcnt(6)
v_mfma_f32_32x32x16_bf16 a[64:79], v[72:75], v[32:35], a[64:79]
ds_read_b64_tr_b16 v[88:89], v104 offset:13312
ds_read_b64_tr_b16 v[90:91], v104 offset:15360
v_exp_f32 v2, v2
v_exp_f32 v18, v18
v_mfma_f32_32x32x16_bf16 a[80:95], v[72:75], v[48:51], a[80:95]
v_add_f32 v108, v0, v1
v_add_f32 v109, v16, v17
v_cvt_pk_bf16_f32 v0, v0, v1
v_cvt_pk_bf16_f32 v16, v16, v17
s_waitcnt lgkmcnt(6)
v_mfma_f32_32x32x16_bf16 a[96:111], v[76:79], v[32:35], a[96:111]
ds_read_b64_tr_b16 v[92:93], v104 offset:13824
ds_read_b64_tr_b16 v[94:95], v104 offset:15872
v_exp_f32 v3, v3
v_exp_f32 v19, v19
v_mfma_f32_32x32x16_bf16 a[112:127], v[76:79], v[48:51], a[112:127]
v_add_f32 v108, v108, v2
v_add_f32 v109, v109, v18
v_exp_f32 v4, v4
s_waitcnt lgkmcnt(6)
v_mfma_f32_32x32x16_bf16 a[0:15], v[80:83], v[40:43], a[0:15]
ds_read_b128 v[64:67], v96 offset:8192
v_exp_f32 v20, v20
v_add_f32 v108, v108, v3
v_add_f32 v109, v109, v19
v_cvt_pk_bf16_f32 v1, v2, v3
v_mfma_f32_32x32x16_bf16 a[16:31], v[80:83], v[56:59], a[16:31]
v_cvt_pk_bf16_f32 v17, v18, v19
v_exp_f32 v5, v5
v_exp_f32 v21, v21
s_waitcnt lgkmcnt(5)
v_mfma_f32_32x32x16_bf16 a[32:47], v[84:87], v[40:43], a[32:47]
ds_read_b128 v[68:71], v97 offset:8192
v_add_f32 v108, v108, v4
v_add_f32 v109, v109, v20
v_exp_f32 v6, v6
v_mfma_f32_32x32x16_bf16 a[48:63], v[84:87], v[56:59], a[48:63]
v_exp_f32 v22, v22
v_add_f32 v108, v108, v5
v_add_f32 v109, v109, v21
v_cvt_pk_bf16_f32 v2, v4, v5
s_waitcnt lgkmcnt(4)
v_mfma_f32_32x32x16_bf16 a[64:79], v[88:91], v[40:43], a[64:79]
ds_read_b128 v[72:75], v98 offset:8192
v_cvt_pk_bf16_f32 v18, v20, v21
v_exp_f32 v7, v7
v_exp_f32 v23, v23
v_mfma_f32_32x32x16_bf16 a[80:95], v[88:91], v[56:59], a[80:95]
v_add_f32 v108, v108, v6
v_add_f32 v109, v109, v22
v_exp_f32 v8, v8
s_waitcnt lgkmcnt(3)
v_mfma_f32_32x32x16_bf16 a[96:111], v[92:95], v[40:43], a[96:111]
ds_read_b128 v[76:79], v99 offset:8192
v_exp_f32 v24, v24
v_add_f32 v108, v108, v7
v_add_f32 v109, v109, v23
v_cvt_pk_bf16_f32 v3, v6, v7
v_mfma_f32_32x32x16_bf16 a[112:127], v[92:95], v[56:59], a[112:127]
v_cvt_pk_bf16_f32 v19, v22, v23
v_exp_f32 v9, v9
v_exp_f32 v25, v25
s_waitcnt lgkmcnt(3)
v_mfma_f32_32x32x16_bf16 v[32:47], v[64:67], a[128:131], v[128:143]
ds_read_b128 v[80:83], v100 offset:8192
v_add_f32 v108, v108, v8
v_add_f32 v109, v109, v24
v_exp_f32 v10, v10
v_mfma_f32_32x32x16_bf16 v[48:63], v[64:67], a[160:163], v[144:159]
v_exp_f32 v26, v26
v_add_f32 v108, v108, v9
v_add_f32 v109, v109, v25
v_cvt_pk_bf16_f32 v8, v8, v9
s_waitcnt lgkmcnt(3)
v_mfma_f32_32x32x16_bf16 v[32:47], v[68:71], a[132:135], v[32:47]
ds_read_b128 v[84:87], v101 offset:8192
v_cvt_pk_bf16_f32 v24, v24, v25
v_exp_f32 v11, v11
v_exp_f32 v27, v27
v_mfma_f32_32x32x16_bf16 v[48:63], v[68:71], a[164:167], v[48:63]
v_add_f32 v108, v108, v10
v_add_f32 v109, v109, v26
v_exp_f32 v12, v12
s_waitcnt lgkmcnt(3)
v_mfma_f32_32x32x16_bf16 v[32:47], v[72:75], a[136:139], v[32:47]
ds_read_b128 v[88:91], v102 offset:8192
v_exp_f32 v28, v28
v_add_f32 v108, v108, v11
v_add_f32 v109, v109, v27
v_cvt_pk_bf16_f32 v9, v10, v11
v_mfma_f32_32x32x16_bf16 v[48:63], v[72:75], a[168:171], v[48:63]
v_cvt_pk_bf16_f32 v25, v26, v27
v_exp_f32 v13, v13
v_exp_f32 v29, v29
s_waitcnt lgkmcnt(3)
v_mfma_f32_32x32x16_bf16 v[32:47], v[76:79], a[140:143], v[32:47]
ds_read_b128 v[92:95], v103 offset:8192
v_add_u32 v104, 0x8000, v104
v_add_f32 v108, v108, v12
v_add_f32 v109, v109, v28
v_exp_f32 v14, v14
v_mfma_f32_32x32x16_bf16 v[48:63], v[76:79], a[172:175], v[48:63]
v_exp_f32 v30, v30
v_add_f32 v108, v108, v13
v_add_f32 v109, v109, v29
v_cvt_pk_bf16_f32 v10, v12, v13
s_waitcnt lgkmcnt(3)
v_mfma_f32_32x32x16_bf16 v[32:47], v[80:83], a[144:147], v[32:47]
ds_read_b64_tr_b16 v[64:65], v104 offset:0
ds_read_b64_tr_b16 v[66:67], v104 offset:2048
v_cvt_pk_bf16_f32 v26, v28, v29
v_exp_f32 v15, v15
v_mfma_f32_32x32x16_bf16 v[48:63], v[80:83], a[176:179], v[48:63]
v_exp_f32 v31, v31
v_add_f32 v108, v108, v14
v_add_f32 v109, v109, v30
v_add_f32 v108, v108, v15
s_waitcnt lgkmcnt(4)
v_mfma_f32_32x32x16_bf16 v[32:47], v[84:87], a[148:151], v[32:47]
ds_read_b64_tr_b16 v[68:69], v104 offset:512
ds_read_b64_tr_b16 v[70:71], v104 offset:2560
v_add_f32 v109, v109, v31
v_cvt_pk_bf16_f32 v11, v14, v15
v_cvt_pk_bf16_f32 v27, v30, v31
v_max_f32 v160, v108, v109
v_mfma_f32_32x32x16_bf16 v[48:63], v[84:87], a[180:183], v[48:63]
v_cmp_nge_f32 vcc, 0x45800000, v160
s_waitcnt lgkmcnt(5)
v_mfma_f32_32x32x16_bf16 v[32:47], v[88:91], a[152:155], v[32:47]
s_waitcnt vmcnt(16)
s_barrier
ds_read_b64_tr_b16 v[72:73], v104 offset:1024
ds_read_b64_tr_b16 v[74:75], v104 offset:3072
v_mfma_f32_32x32x16_bf16 v[48:63], v[88:91], a[184:187], v[48:63]
s_waitcnt lgkmcnt(6)
v_mfma_f32_32x32x16_bf16 v[32:47], v[92:95], a[156:159], v[32:47]
ds_read_b64_tr_b16 v[76:77], v104 offset:1536
ds_read_b64_tr_b16 v[78:79], v104 offset:3584
v_mfma_f32_32x32x16_bf16 v[48:63], v[92:95], a[188:191], v[48:63]
s_cbranch_vccnz L_tr_12
L_ret_12:
v_add_f32 v106, v106, v108
v_add_f32 v107, v107, v109
s_waitcnt lgkmcnt(6)
v_mfma_f32_32x32x16_bf16 a[0:15], v[64:67], v[0:3], a[0:15]
ds_read_b64_tr_b16 v[80:81], v104 offset:4096
ds_read_b64_tr_b16 v[82:83], v104 offset:6144
v_mfma_f32_32x32x16_bf16 a[16:31], v[64:67], v[16:19], a[16:31]
s_add_u32 m0, s61, 0
s_add_u32 s66, s61, 163840
buffer_load_dwordx4 v120, s[44:47], s66 offen offset:0 nt lds
s_waitcnt lgkmcnt(6)
v_mfma_f32_32x32x16_bf16 a[32:47], v[68:71], v[0:3], a[32:47]
ds_read_b64_tr_b16 v[84:85], v104 offset:4608
ds_read_b64_tr_b16 v[86:87], v104 offset:6656
v_exp_f32 v32, v32
v_exp_f32 v48, v48
v_mfma_f32_32x32x16_bf16 a[48:63], v[68:71], v[16:19], a[48:63]
buffer_load_dwordx4 v121, s[44:47], s66 offen offset:1024 nt lds
v_exp_f32 v33, v33
v_exp_f32 v49, v49
s_waitcnt lgkmcnt(6)
v_mfma_f32_32x32x16_bf16 a[64:79], v[72:75], v[0:3], a[64:79]
ds_read_b64_tr_b16 v[88:89], v104 offset:5120
ds_read_b64_tr_b16 v[90:91], v104 offset:7168
v_exp_f32 v34, v34
v_exp_f32 v50, v50
v_mfma_f32_32x32x16_bf16 a[80:95], v[72:75], v[16:19], a[80:95]
v_add_f32 v108, v32, v33
v_add_f32 v109, v48, v49
v_cvt_pk_bf16_f32 v32, v32, v33
v_cvt_pk_bf16_f32 v48, v48, v49
s_waitcnt lgkmcnt(6)
v_mfma_f32_32x32x16_bf16 a[96:111], v[76:79], v[0:3], a[96:111]
buffer_load_dwordx4 v122, s[44:47], s66 offen offset:2048 nt lds
ds_read_b64_tr_b16 v[92:93], v104 offset:5632
ds_read_b64_tr_b16 v[94:95], v104 offset:7680
v_add_u32 v96, 0x8000, v96
v_exp_f32 v35, v35
v_mfma_f32_32x32x16_bf16 a[112:127], v[76:79], v[16:19], a[112:127]
v_exp_f32 v51, v51
v_add_f32 v108, v108, v34
v_add_f32 v109, v109, v50
s_waitcnt lgkmcnt(6)
v_mfma_f32_32x32x16_bf16 a[0:15], v[80:83], v[8:11], a[0:15]
buffer_load_dwordx4 v123, s[44:47], s66 offen offset:3072 nt lds
ds_read_b128 v[64:67], v96 offset:0
v_add_u32 v97, 0x8000, v97
v_exp_f32 v36, v36
v_mfma_f32_32x32x16_bf16 a[16:31], v[80:83], v[24:27], a[16:31]
v_exp_f32 v52, v52
v_add_f32 v108, v108, v35
v_add_f32 v109, v109, v51
v_cvt_pk_bf16_f32 v33, v34, v35
s_waitcnt lgkmcnt(5)
v_mfma_f32_32x32x16_bf16 a[32:47], v[84:87], v[8:11], a[32:47]
ds_read_b128 v[68:71], v97 offset:0
v_add_u32 v98, 0x8000, v98
v_cvt_pk_bf16_f32 v49, v50, v51
v_exp_f32 v37, v37
v_mfma_f32_32x32x16_bf16 a[48:63], v[84:87], v[24:27], a[48:63]
s_add_u32 m0, s61, 16384
s_nop 0
buffer_load_dwordx4 v124, s[48:51], s66 offen offset:0 nt lds
v_exp_f32 v53, v53
s_waitcnt lgkmcnt(4)
v_mfma_f32_32x32x16_bf16 a[64:79], v[88:91], v[8:11], a[64:79]
ds_read_b128 v[72:75], v98 offset:0
v_add_u32 v99, 0x8000, v99
v_add_f32 v108, v108, v36
v_add_f32 v109, v109, v52
v_exp_f32 v38, v38
v_mfma_f32_32x32x16_bf16 a[80:95], v[88:91], v[24:27], a[80:95]
buffer_load_dwordx4 v125, s[48:51], s66 offen offset:1024 nt lds
v_exp_f32 v54, v54
v_add_f32 v108, v108, v37
v_add_f32 v109, v109, v53
s_waitcnt lgkmcnt(3)
v_mfma_f32_32x32x16_bf16 a[96:111], v[92:95], v[8:11], a[96:111]
ds_read_b128 v[76:79], v99 offset:0
v_add_u32 v100, 0x8000, v100
v_cvt_pk_bf16_f32 v34, v36, v37
v_cvt_pk_bf16_f32 v50, v52, v53
v_exp_f32 v39, v39
v_mfma_f32_32x32x16_bf16 a[112:127], v[92:95], v[24:27], a[112:127]
v_exp_f32 v55, v55
v_add_f32 v108, v108, v38
v_add_f32 v109, v109, v54
s_waitcnt lgkmcnt(3)
v_mfma_f32_32x32x16_bf16 v[0:15], v[64:67], a[128:131], v[128:143]
buffer_load_dwordx4 v124, s[48:51], s66 offen offset:2048 nt lds
ds_read_b128 v[80:83], v100 offset:0
v_add_u32 v101, 0x8000, v101
v_exp_f32 v40, v40
v_mfma_f32_32x32x16_bf16 v[16:31], v[64:67], a[160:163], v[144:159]
v_exp_f32 v56, v56
v_add_f32 v108, v108, v39
v_add_f32 v109, v109, v55
v_cvt_pk_bf16_f32 v35, v38, v39
s_waitcnt lgkmcnt(3)
v_mfma_f32_32x32x16_bf16 v[0:15], v[68:71], a[132:135], v[0:15]
buffer_load_dwordx4 v125, s[48:51], s66 offen offset:3072 nt lds
ds_read_b128 v[84:87], v101 offset:0
v_add_u32 v102, 0x8000, v102
v_cvt_pk_bf16_f32 v51, v54, v55
v_exp_f32 v41, v41
v_mfma_f32_32x32x16_bf16 v[16:31], v[68:71], a[164:167], v[16:31]
v_exp_f32 v57, v57
v_add_f32 v108, v108, v40
v_add_f32 v109, v109, v56
s_waitcnt lgkmcnt(3)
v_mfma_f32_32x32x16_bf16 v[0:15], v[72:75], a[136:139], v[0:15]
ds_read_b128 v[88:91], v102 offset:0
v_add_u32 v103, 0x8000, v103
v_exp_f32 v42, v42
v_exp_f32 v58, v58
v_mfma_f32_32x32x16_bf16 v[16:31], v[72:75], a[168:171], v[16:31]
v_add_f32 v108, v108, v41
v_add_f32 v109, v109, v57
v_cvt_pk_bf16_f32 v40, v40, v41
v_cvt_pk_bf16_f32 v56, v56, v57
s_waitcnt lgkmcnt(3)
v_mfma_f32_32x32x16_bf16 v[0:15], v[76:79], a[140:143], v[0:15]
ds_read_b128 v[92:95], v103 offset:0
v_exp_f32 v43, v43
v_exp_f32 v59, v59
v_add_f32 v108, v108, v42
v_mfma_f32_32x32x16_bf16 v[16:31], v[76:79], a[172:175], v[16:31]
v_add_f32 v109, v109, v58
v_exp_f32 v44, v44
v_exp_f32 v60, v60
s_waitcnt lgkmcnt(3)
v_mfma_f32_32x32x16_bf16 v[0:15], v[80:83], a[144:147], v[0:15]
ds_read_b64_tr_b16 v[64:65], v104 offset:8192
ds_read_b64_tr_b16 v[66:67], v104 offset:10240
v_add_f32 v108, v108, v43
v_add_f32 v109, v109, v59
v_cvt_pk_bf16_f32 v41, v42, v43
v_cvt_pk_bf16_f32 v57, v58, v59
v_mfma_f32_32x32x16_bf16 v[16:31], v[80:83], a[176:179], v[16:31]
v_exp_f32 v45, v45
v_exp_f32 v61, v61
v_add_f32 v108, v108, v44
s_waitcnt lgkmcnt(4)
v_mfma_f32_32x32x16_bf16 v[0:15], v[84:87], a[148:151], v[0:15]
ds_read_b64_tr_b16 v[68:69], v104 offset:8704
ds_read_b64_tr_b16 v[70:71], v104 offset:10752
v_add_f32 v109, v109, v60
v_exp_f32 v46, v46
v_mfma_f32_32x32x16_bf16 v[16:31], v[84:87], a[180:183], v[16:31]
v_exp_f32 v62, v62
v_add_f32 v108, v108, v45
v_add_f32 v109, v109, v61
v_cvt_pk_bf16_f32 v42, v44, v45
s_waitcnt lgkmcnt(5)
v_mfma_f32_32x32x16_bf16 v[0:15], v[88:91], a[152:155], v[0:15]
ds_read_b64_tr_b16 v[72:73], v104 offset:9216
ds_read_b64_tr_b16 v[74:75], v104 offset:11264
v_cvt_pk_bf16_f32 v58, v60, v61
v_exp_f32 v47, v47
v_mfma_f32_32x32x16_bf16 v[16:31], v[88:91], a[184:187], v[16:31]
v_exp_f32 v63, v63
v_add_f32 v108, v108, v46
v_add_f32 v109, v109, v62
v_add_f32 v108, v108, v47
s_waitcnt lgkmcnt(6)
v_mfma_f32_32x32x16_bf16 v[0:15], v[92:95], a[156:159], v[0:15]
ds_read_b64_tr_b16 v[76:77], v104 offset:9728
ds_read_b64_tr_b16 v[78:79], v104 offset:11776
v_add_f32 v109, v109, v63
v_cvt_pk_bf16_f32 v43, v46, v47
v_cvt_pk_bf16_f32 v59, v62, v63
v_max_f32 v160, v108, v109
v_mfma_f32_32x32x16_bf16 v[16:31], v[92:95], a[188:191], v[16:31]
v_cmp_nge_f32 vcc, 0x45800000, v160
s_cbranch_vccnz L_tr_13
L_ret_13:
v_add_f32 v106, v106, v108
v_add_f32 v107, v107, v109
s_waitcnt lgkmcnt(6)
v_mfma_f32_32x32x16_bf16 a[0:15], v[64:67], v[32:35], a[0:15]
ds_read_b64_tr_b16 v[80:81], v104 offset:12288
ds_read_b64_tr_b16 v[82:83], v104 offset:14336
v_mfma_f32_32x32x16_bf16 a[16:31], v[64:67], v[48:51], a[16:31]
s_waitcnt lgkmcnt(6)
v_mfma_f32_32x32x16_bf16 a[32:47], v[68:71], v[32:35], a[32:47]
ds_read_b64_tr_b16 v[84:85], v104 offset:12800
ds_read_b64_tr_b16 v[86:87], v104 offset:14848
v_exp_f32 v0, v0
v_exp_f32 v16, v16
v_mfma_f32_32x32x16_bf16 a[48:63], v[68:71], v[48:51], a[48:63]
v_exp_f32 v1, v1
v_exp_f32 v17, v17
s_waitcnt lgkmcnt(6)
v_mfma_f32_32x32x16_bf16 a[64:79], v[72:75], v[32:35], a[64:79]
ds_read_b64_tr_b16 v[88:89], v104 offset:13312
ds_read_b64_tr_b16 v[90:91], v104 offset:15360
v_exp_f32 v2, v2
v_exp_f32 v18, v18
v_mfma_f32_32x32x16_bf16 a[80:95], v[72:75], v[48:51], a[80:95]
v_add_f32 v108, v0, v1
v_add_f32 v109, v16, v17
v_cvt_pk_bf16_f32 v0, v0, v1
v_cvt_pk_bf16_f32 v16, v16, v17
s_waitcnt lgkmcnt(6)
v_mfma_f32_32x32x16_bf16 a[96:111], v[76:79], v[32:35], a[96:111]
ds_read_b64_tr_b16 v[92:93], v104 offset:13824
ds_read_b64_tr_b16 v[94:95], v104 offset:15872
v_exp_f32 v3, v3
v_exp_f32 v19, v19
v_mfma_f32_32x32x16_bf16 a[112:127], v[76:79], v[48:51], a[112:127]
v_add_f32 v108, v108, v2
v_add_f32 v109, v109, v18
v_exp_f32 v4, v4
s_waitcnt lgkmcnt(6)
v_mfma_f32_32x32x16_bf16 a[0:15], v[80:83], v[40:43], a[0:15]
ds_read_b128 v[64:67], v96 offset:8192
v_exp_f32 v20, v20
v_add_f32 v108, v108, v3
v_add_f32 v109, v109, v19
v_cvt_pk_bf16_f32 v1, v2, v3
v_mfma_f32_32x32x16_bf16 a[16:31], v[80:83], v[56:59], a[16:31]
v_cvt_pk_bf16_f32 v17, v18, v19
v_exp_f32 v5, v5
v_exp_f32 v21, v21
s_waitcnt lgkmcnt(5)
v_mfma_f32_32x32x16_bf16 a[32:47], v[84:87], v[40:43], a[32:47]
ds_read_b128 v[68:71], v97 offset:8192
v_add_f32 v108, v108, v4
v_add_f32 v109, v109, v20
v_exp_f32 v6, v6
v_mfma_f32_32x32x16_bf16 a[48:63], v[84:87], v[56:59], a[48:63]
v_exp_f32 v22, v22
v_add_f32 v108, v108, v5
v_add_f32 v109, v109, v21
v_cvt_pk_bf16_f32 v2, v4, v5
s_waitcnt lgkmcnt(4)
v_mfma_f32_32x32x16_bf16 a[64:79], v[88:91], v[40:43], a[64:79]
ds_read_b128 v[72:75], v98 offset:8192
v_cvt_pk_bf16_f32 v18, v20, v21
v_exp_f32 v7, v7
v_exp_f32 v23, v23
v_mfma_f32_32x32x16_bf16 a[80:95], v[88:91], v[56:59], a[80:95]
v_add_f32 v108, v108, v6
v_add_f32 v109, v109, v22
v_exp_f32 v8, v8
s_waitcnt lgkmcnt(3)
v_mfma_f32_32x32x16_bf16 a[96:111], v[92:95], v[40:43], a[96:111]
ds_read_b128 v[76:79], v99 offset:8192
v_exp_f32 v24, v24
v_add_f32 v108, v108, v7
v_add_f32 v109, v109, v23
v_cvt_pk_bf16_f32 v3, v6, v7
v_mfma_f32_32x32x16_bf16 a[112:127], v[92:95], v[56:59], a[112:127]
v_cvt_pk_bf16_f32 v19, v22, v23
v_exp_f32 v9, v9
v_exp_f32 v25, v25
s_waitcnt lgkmcnt(3)
v_mfma_f32_32x32x16_bf16 v[32:47], v[64:67], a[128:131], v[128:143]
ds_read_b128 v[80:83], v100 offset:8192
v_add_f32 v108, v108, v8
v_add_f32 v109, v109, v24
v_exp_f32 v10, v10
v_mfma_f32_32x32x16_bf16 v[48:63], v[64:67], a[160:163], v[144:159]
v_exp_f32 v26, v26
v_add_f32 v108, v108, v9
v_add_f32 v109, v109, v25
v_cvt_pk_bf16_f32 v8, v8, v9
s_waitcnt lgkmcnt(3)
v_mfma_f32_32x32x16_bf16 v[32:47], v[68:71], a[132:135], v[32:47]
ds_read_b128 v[84:87], v101 offset:8192
v_cvt_pk_bf16_f32 v24, v24, v25
v_exp_f32 v11, v11
v_exp_f32 v27, v27
v_mfma_f32_32x32x16_bf16 v[48:63], v[68:71], a[164:167], v[48:63]
v_add_f32 v108, v108, v10
v_add_f32 v109, v109, v26
v_exp_f32 v12, v12
s_waitcnt lgkmcnt(3)
v_mfma_f32_32x32x16_bf16 v[32:47], v[72:75], a[136:139], v[32:47]
ds_read_b128 v[88:91], v102 offset:8192
v_exp_f32 v28, v28
v_add_f32 v108, v108, v11
v_add_f32 v109, v109, v27
v_cvt_pk_bf16_f32 v9, v10, v11
v_mfma_f32_32x32x16_bf16 v[48:63], v[72:75], a[168:171], v[48:63]
v_cvt_pk_bf16_f32 v25, v26, v27
v_exp_f32 v13, v13
v_exp_f32 v29, v29
s_waitcnt lgkmcnt(3)
v_mfma_f32_32x32x16_bf16 v[32:47], v[76:79], a[140:143], v[32:47]
ds_read_b128 v[92:95], v103 offset:8192
v_add_u32 v104, 0x8000, v104
v_add_f32 v108, v108, v12
v_add_f32 v109, v109, v28
v_exp_f32 v14, v14
v_mfma_f32_32x32x16_bf16 v[48:63], v[76:79], a[172:175], v[48:63]
v_exp_f32 v30, v30
v_add_f32 v108, v108, v13
v_add_f32 v109, v109, v29
v_cvt_pk_bf16_f32 v10, v12, v13
s_waitcnt lgkmcnt(3)
v_mfma_f32_32x32x16_bf16 v[32:47], v[80:83], a[144:147], v[32:47]
ds_read_b64_tr_b16 v[64:65], v104 offset:0
ds_read_b64_tr_b16 v[66:67], v104 offset:2048
v_cvt_pk_bf16_f32 v26, v28, v29
v_exp_f32 v15, v15
v_mfma_f32_32x32x16_bf16 v[48:63], v[80:83], a[176:179], v[48:63]
v_exp_f32 v31, v31
v_add_f32 v108, v108, v14
v_add_f32 v109, v109, v30
v_add_f32 v108, v108, v15
s_waitcnt lgkmcnt(4)
v_mfma_f32_32x32x16_bf16 v[32:47], v[84:87], a[148:151], v[32:47]
ds_read_b64_tr_b16 v[68:69], v104 offset:512
ds_read_b64_tr_b16 v[70:71], v104 offset:2560
v_add_f32 v109, v109, v31
v_cvt_pk_bf16_f32 v11, v14, v15
v_cvt_pk_bf16_f32 v27, v30, v31
v_max_f32 v160, v108, v109
v_mfma_f32_32x32x16_bf16 v[48:63], v[84:87], a[180:183], v[48:63]
v_cmp_nge_f32 vcc, 0x45800000, v160
s_waitcnt lgkmcnt(5)
v_mfma_f32_32x32x16_bf16 v[32:47], v[88:91], a[152:155], v[32:47]
s_waitcnt vmcnt(16)
s_barrier
ds_read_b64_tr_b16 v[72:73], v104 offset:1024
ds_read_b64_tr_b16 v[74:75], v104 offset:3072
v_mfma_f32_32x32x16_bf16 v[48:63], v[88:91], a[184:187], v[48:63]
s_waitcnt lgkmcnt(6)
v_mfma_f32_32x32x16_bf16 v[32:47], v[92:95], a[156:159], v[32:47]
ds_read_b64_tr_b16 v[76:77], v104 offset:1536
ds_read_b64_tr_b16 v[78:79], v104 offset:3584
v_mfma_f32_32x32x16_bf16 v[48:63], v[92:95], a[188:191], v[48:63]
s_cbranch_vccnz L_tr_14
L_ret_14:
v_add_f32 v106, v106, v108
v_add_f32 v107, v107, v109
s_waitcnt lgkmcnt(6)
v_mfma_f32_32x32x16_bf16 a[0:15], v[64:67], v[0:3], a[0:15]
ds_read_b64_tr_b16 v[80:81], v104 offset:4096
ds_read_b64_tr_b16 v[82:83], v104 offset:6144
v_mfma_f32_32x32x16_bf16 a[16:31], v[64:67], v[16:19], a[16:31]
s_add_u32 m0, s61, 32768
s_add_u32 s66, s61, 180224
buffer_load_dwordx4 v120, s[44:47], s66 offen offset:0 nt lds
s_waitcnt lgkmcnt(6)
v_mfma_f32_32x32x16_bf16 a[32:47], v[68:71], v[0:3], a[32:47]
ds_read_b64_tr_b16 v[84:85], v104 offset:4608
ds_read_b64_tr_b16 v[86:87], v104 offset:6656
v_exp_f32 v32, v32
v_exp_f32 v48, v48
v_mfma_f32_32x32x16_bf16 a[48:63], v[68:71], v[16:19], a[48:63]
buffer_load_dwordx4 v121, s[44:47], s66 offen offset:1024 nt lds
v_exp_f32 v33, v33
v_exp_f32 v49, v49
s_waitcnt lgkmcnt(6)
v_mfma_f32_32x32x16_bf16 a[64:79], v[72:75], v[0:3], a[64:79]
ds_read_b64_tr_b16 v[88:89], v104 offset:5120
ds_read_b64_tr_b16 v[90:91], v104 offset:7168
v_exp_f32 v34, v34
v_exp_f32 v50, v50
v_mfma_f32_32x32x16_bf16 a[80:95], v[72:75], v[16:19], a[80:95]
v_add_f32 v108, v32, v33
v_add_f32 v109, v48, v49
v_cvt_pk_bf16_f32 v32, v32, v33
v_cvt_pk_bf16_f32 v48, v48, v49
s_waitcnt lgkmcnt(6)
v_mfma_f32_32x32x16_bf16 a[96:111], v[76:79], v[0:3], a[96:111]
buffer_load_dwordx4 v122, s[44:47], s66 offen offset:2048 nt lds
ds_read_b64_tr_b16 v[92:93], v104 offset:5632
ds_read_b64_tr_b16 v[94:95], v104 offset:7680
v_add_u32 v96, 0x8000, v96
v_exp_f32 v35, v35
v_mfma_f32_32x32x16_bf16 a[112:127], v[76:79], v[16:19], a[112:127]
v_exp_f32 v51, v51
v_add_f32 v108, v108, v34
v_add_f32 v109, v109, v50
s_waitcnt lgkmcnt(6)
v_mfma_f32_32x32x16_bf16 a[0:15], v[80:83], v[8:11], a[0:15]
buffer_load_dwordx4 v123, s[44:47], s66 offen offset:3072 nt lds
ds_read_b128 v[64:67], v96 offset:0
v_add_u32 v97, 0x8000, v97
v_exp_f32 v36, v36
v_mfma_f32_32x32x16_bf16 a[16:31], v[80:83], v[24:27], a[16:31]
v_exp_f32 v52, v52
v_add_f32 v108, v108, v35
v_add_f32 v109, v109, v51
v_cvt_pk_bf16_f32 v33, v34, v35
s_waitcnt lgkmcnt(5)
v_mfma_f32_32x32x16_bf16 a[32:47], v[84:87], v[8:11], a[32:47]
ds_read_b128 v[68:71], v97 offset:0
v_add_u32 v98, 0x8000, v98
v_cvt_pk_bf16_f32 v49, v50, v51
v_exp_f32 v37, v37
v_mfma_f32_32x32x16_bf16 a[48:63], v[84:87], v[24:27], a[48:63]
s_add_u32 m0, s61, 49152
s_nop 0
buffer_load_dwordx4 v124, s[48:51], s66 offen offset:0 nt lds
v_exp_f32 v53, v53
s_waitcnt lgkmcnt(4)
v_mfma_f32_32x32x16_bf16 a[64:79], v[88:91], v[8:11], a[64:79]
ds_read_b128 v[72:75], v98 offset:0
v_add_u32 v99, 0x8000, v99
v_add_f32 v108, v108, v36
v_add_f32 v109, v109, v52
v_exp_f32 v38, v38
v_mfma_f32_32x32x16_bf16 a[80:95], v[88:91], v[24:27], a[80:95]
buffer_load_dwordx4 v125, s[48:51], s66 offen offset:1024 nt lds
v_exp_f32 v54, v54
v_add_f32 v108, v108, v37
v_add_f32 v109, v109, v53
s_waitcnt lgkmcnt(3)
v_mfma_f32_32x32x16_bf16 a[96:111], v[92:95], v[8:11], a[96:111]
ds_read_b128 v[76:79], v99 offset:0
v_add_u32 v100, 0x8000, v100
v_cvt_pk_bf16_f32 v34, v36, v37
v_cvt_pk_bf16_f32 v50, v52, v53
v_exp_f32 v39, v39
v_mfma_f32_32x32x16_bf16 a[112:127], v[92:95], v[24:27], a[112:127]
v_exp_f32 v55, v55
v_add_f32 v108, v108, v38
v_add_f32 v109, v109, v54
s_waitcnt lgkmcnt(3)
v_mfma_f32_32x32x16_bf16 v[0:15], v[64:67], v[192:195], 0
buffer_load_dwordx4 v124, s[48:51], s66 offen offset:2048 nt lds
ds_read_b128 v[80:83], v100 offset:0
v_add_u32 v101, 0x8000, v101
v_exp_f32 v40, v40
v_mfma_f32_32x32x16_bf16 v[16:31], v[64:67], v[208:211], 0
v_exp_f32 v56, v56
v_add_f32 v108, v108, v39
v_add_f32 v109, v109, v55
v_cvt_pk_bf16_f32 v35, v38, v39
s_waitcnt lgkmcnt(3)
v_mfma_f32_32x32x16_bf16 v[0:15], v[68:71], v[196:199], v[0:15]
buffer_load_dwordx4 v125, s[48:51], s66 offen offset:3072 nt lds
ds_read_b128 v[84:87], v101 offset:0
v_add_u32 v102, 0x8000, v102
v_cvt_pk_bf16_f32 v51, v54, v55
v_exp_f32 v41, v41
v_mfma_f32_32x32x16_bf16 v[16:31], v[68:71], v[212:215], v[16:31]
v_exp_f32 v57, v57
v_add_f32 v108, v108, v40
v_add_f32 v109, v109, v56
s_waitcnt lgkmcnt(3)
v_mfma_f32_32x32x16_bf16 v[0:15], v[72:75], v[224:227], v[0:15]
ds_read_b128 v[88:91], v102 offset:0
v_add_u32 v103, 0x8000, v103
v_exp_f32 v42, v42
v_exp_f32 v58, v58
v_mfma_f32_32x32x16_bf16 v[16:31], v[72:75], v[240:243], v[16:31]
v_add_f32 v108, v108, v41
v_add_f32 v109, v109, v57
v_cvt_pk_bf16_f32 v40, v40, v41
v_cvt_pk_bf16_f32 v56, v56, v57
s_waitcnt lgkmcnt(3)
v_mfma_f32_32x32x16_bf16 v[0:15], v[76:79], v[228:231], v[0:15]
ds_read_b128 v[92:95], v103 offset:0
v_exp_f32 v43, v43
v_exp_f32 v59, v59
v_add_f32 v108, v108, v42
v_mfma_f32_32x32x16_bf16 v[16:31], v[76:79], v[244:247], v[16:31]
v_add_f32 v109, v109, v58
v_exp_f32 v44, v44
v_exp_f32 v60, v60
s_waitcnt lgkmcnt(3)
v_mfma_f32_32x32x16_bf16 v[0:15], v[80:83], v[200:203], v[0:15]
ds_read_b64_tr_b16 v[64:65], v104 offset:8192
ds_read_b64_tr_b16 v[66:67], v104 offset:10240
v_add_f32 v108, v108, v43
v_add_f32 v109, v109, v59
v_cvt_pk_bf16_f32 v41, v42, v43
v_cvt_pk_bf16_f32 v57, v58, v59
v_mfma_f32_32x32x16_bf16 v[16:31], v[80:83], v[216:219], v[16:31]
v_exp_f32 v45, v45
v_exp_f32 v61, v61
v_add_f32 v108, v108, v44
s_waitcnt lgkmcnt(4)
v_mfma_f32_32x32x16_bf16 v[0:15], v[84:87], v[204:207], v[0:15]
ds_read_b64_tr_b16 v[68:69], v104 offset:8704
ds_read_b64_tr_b16 v[70:71], v104 offset:10752
v_add_f32 v109, v109, v60
v_exp_f32 v46, v46
v_mfma_f32_32x32x16_bf16 v[16:31], v[84:87], v[220:223], v[16:31]
v_exp_f32 v62, v62
v_add_f32 v108, v108, v45
v_add_f32 v109, v109, v61
v_cvt_pk_bf16_f32 v42, v44, v45
s_waitcnt lgkmcnt(5)
v_mfma_f32_32x32x16_bf16 v[0:15], v[88:91], v[232:235], v[0:15]
ds_read_b64_tr_b16 v[72:73], v104 offset:9216
ds_read_b64_tr_b16 v[74:75], v104 offset:11264
v_cvt_pk_bf16_f32 v58, v60, v61
v_exp_f32 v47, v47
v_mfma_f32_32x32x16_bf16 v[16:31], v[88:91], v[248:251], v[16:31]
v_exp_f32 v63, v63
v_add_f32 v108, v108, v46
v_add_f32 v109, v109, v62
v_add_f32 v108, v108, v47
s_waitcnt lgkmcnt(6)
v_mfma_f32_32x32x16_bf16 v[0:15], v[92:95], v[236:239], v[0:15]
ds_read_b64_tr_b16 v[76:77], v104 offset:9728
ds_read_b64_tr_b16 v[78:79], v104 offset:11776
v_add_f32 v109, v109, v63
v_cvt_pk_bf16_f32 v43, v46, v47
v_cvt_pk_bf16_f32 v59, v62, v63
v_max_f32 v160, v108, v109
v_mfma_f32_32x32x16_bf16 v[16:31], v[92:95], v[252:255], v[16:31]
v_cmp_nge_f32 vcc, 0x45800000, v160
s_cbranch_vccnz L_tr_15
L_ret_15:
v_add_f32 v106, v106, v108
v_add_f32 v107, v107, v109
s_waitcnt lgkmcnt(6)
v_mfma_f32_32x32x16_bf16 a[0:15], v[64:67], v[32:35], a[0:15]
ds_read_b64_tr_b16 v[80:81], v104 offset:12288
ds_read_b64_tr_b16 v[82:83], v104 offset:14336
v_mfma_f32_32x32x16_bf16 a[16:31], v[64:67], v[48:51], a[16:31]
v_mov_b32 v128, 0
v_mov_b32 v129, 0
v_mov_b32 v130, 0
v_mov_b32 v131, 0
v_mov_b32 v132, 0
s_waitcnt lgkmcnt(6)
v_mfma_f32_32x32x16_bf16 a[32:47], v[68:71], v[32:35], a[32:47]
ds_read_b64_tr_b16 v[84:85], v104 offset:12800
ds_read_b64_tr_b16 v[86:87], v104 offset:14848
v_exp_f32 v0, v0
v_exp_f32 v16, v16
v_mfma_f32_32x32x16_bf16 a[48:63], v[68:71], v[48:51], a[48:63]
v_exp_f32 v1, v1
v_exp_f32 v17, v17
v_mov_b32 v133, 0
s_waitcnt lgkmcnt(6)
v_mfma_f32_32x32x16_bf16 a[64:79], v[72:75], v[32:35], a[64:79]
ds_read_b64_tr_b16 v[88:89], v104 offset:13312
ds_read_b64_tr_b16 v[90:91], v104 offset:15360
v_exp_f32 v2, v2
v_exp_f32 v18, v18
v_mfma_f32_32x32x16_bf16 a[80:95], v[72:75], v[48:51], a[80:95]
v_add_f32 v108, v0, v1
v_add_f32 v109, v16, v17
v_cvt_pk_bf16_f32 v0, v0, v1
v_cvt_pk_bf16_f32 v16, v16, v17
v_mov_b32 v134, 0
s_waitcnt lgkmcnt(6)
v_mfma_f32_32x32x16_bf16 a[96:111], v[76:79], v[32:35], a[96:111]
ds_read_b64_tr_b16 v[92:93], v104 offset:13824
ds_read_b64_tr_b16 v[94:95], v104 offset:15872
v_exp_f32 v3, v3
v_exp_f32 v19, v19
v_mfma_f32_32x32x16_bf16 a[112:127], v[76:79], v[48:51], a[112:127]
v_add_f32 v108, v108, v2
v_add_f32 v109, v109, v18
v_exp_f32 v4, v4
v_mov_b32 v135, 0
s_waitcnt lgkmcnt(6)
v_mfma_f32_32x32x16_bf16 a[0:15], v[80:83], v[40:43], a[0:15]
ds_read_b128 v[64:67], v96 offset:8192
v_exp_f32 v20, v20
v_add_f32 v108, v108, v3
v_add_f32 v109, v109, v19
v_cvt_pk_bf16_f32 v1, v2, v3
v_mfma_f32_32x32x16_bf16 a[16:31], v[80:83], v[56:59], a[16:31]
v_cvt_pk_bf16_f32 v17, v18, v19
v_exp_f32 v5, v5
v_exp_f32 v21, v21
s_waitcnt lgkmcnt(5)
v_mfma_f32_32x32x16_bf16 a[32:47], v[84:87], v[40:43], a[32:47]
ds_read_b128 v[68:71], v97 offset:8192
v_add_f32 v108, v108, v4
v_add_f32 v109, v109, v20
v_exp_f32 v6, v6
v_mov_b32 v136, 0
v_mfma_f32_32x32x16_bf16 a[48:63], v[84:87], v[56:59], a[48:63]
v_exp_f32 v22, v22
v_add_f32 v108, v108, v5
v_add_f32 v109, v109, v21
v_cvt_pk_bf16_f32 v2, v4, v5
s_waitcnt lgkmcnt(4)
v_mfma_f32_32x32x16_bf16 a[64:79], v[88:91], v[40:43], a[64:79]
ds_read_b128 v[72:75], v98 offset:8192
v_cvt_pk_bf16_f32 v18, v20, v21
v_exp_f32 v7, v7
v_exp_f32 v23, v23
v_mfma_f32_32x32x16_bf16 a[80:95], v[88:91], v[56:59], a[80:95]
v_add_f32 v108, v108, v6
v_add_f32 v109, v109, v22
v_exp_f32 v8, v8
v_mov_b32 v137, 0
s_waitcnt lgkmcnt(3)
v_mfma_f32_32x32x16_bf16 a[96:111], v[92:95], v[40:43], a[96:111]
ds_read_b128 v[76:79], v99 offset:8192
v_exp_f32 v24, v24
v_add_f32 v108, v108, v7
v_add_f32 v109, v109, v23
v_cvt_pk_bf16_f32 v3, v6, v7
v_mfma_f32_32x32x16_bf16 a[112:127], v[92:95], v[56:59], a[112:127]
v_cvt_pk_bf16_f32 v19, v22, v23
v_exp_f32 v9, v9
v_exp_f32 v25, v25
s_waitcnt lgkmcnt(3)
v_mfma_f32_32x32x16_bf16 v[32:47], v[64:67], v[192:195], 0
ds_read_b128 v[80:83], v100 offset:8192
v_add_f32 v108, v108, v8
v_add_f32 v109, v109, v24
v_exp_f32 v10, v10
v_mov_b32 v138, 0
v_mfma_f32_32x32x16_bf16 v[48:63], v[64:67], v[208:211], 0
v_exp_f32 v26, v26
v_add_f32 v108, v108, v9
v_add_f32 v109, v109, v25
v_cvt_pk_bf16_f32 v8, v8, v9
s_waitcnt lgkmcnt(3)
v_mfma_f32_32x32x16_bf16 v[32:47], v[68:71], v[196:199], v[32:47]
ds_read_b128 v[84:87], v101 offset:8192
v_cvt_pk_bf16_f32 v24, v24, v25
v_exp_f32 v11, v11
v_exp_f32 v27, v27
v_mfma_f32_32x32x16_bf16 v[48:63], v[68:71], v[212:215], v[48:63]
v_add_f32 v108, v108, v10
v_add_f32 v109, v109, v26
v_exp_f32 v12, v12
v_mov_b32 v139, 0
s_waitcnt lgkmcnt(3)
v_mfma_f32_32x32x16_bf16 v[32:47], v[72:75], v[224:227], v[32:47]
ds_read_b128 v[88:91], v102 offset:8192
v_exp_f32 v28, v28
v_add_f32 v108, v108, v11
v_add_f32 v109, v109, v27
v_cvt_pk_bf16_f32 v9, v10, v11
v_mfma_f32_32x32x16_bf16 v[48:63], v[72:75], v[240:243], v[48:63]
v_cvt_pk_bf16_f32 v25, v26, v27
v_exp_f32 v13, v13
v_exp_f32 v29, v29
s_waitcnt lgkmcnt(3)
v_mfma_f32_32x32x16_bf16 v[32:47], v[76:79], v[228:231], v[32:47]
ds_read_b128 v[92:95], v103 offset:8192
v_add_u32 v104, 0x8000, v104
v_add_f32 v108, v108, v12
v_add_f32 v109, v109, v28
v_exp_f32 v14, v14
v_mfma_f32_32x32x16_bf16 v[48:63], v[76:79], v[244:247], v[48:63]
v_exp_f32 v30, v30
v_add_f32 v108, v108, v13
v_add_f32 v109, v109, v29
v_cvt_pk_bf16_f32 v10, v12, v13
s_waitcnt lgkmcnt(3)
v_mfma_f32_32x32x16_bf16 v[32:47], v[80:83], v[200:203], v[32:47]
ds_read_b64_tr_b16 v[64:65], v104 offset:0
ds_read_b64_tr_b16 v[66:67], v104 offset:2048
v_cvt_pk_bf16_f32 v26, v28, v29
v_exp_f32 v15, v15
v_mov_b32 v140, 0
v_mfma_f32_32x32x16_bf16 v[48:63], v[80:83], v[216:219], v[48:63]
v_exp_f32 v31, v31
v_add_f32 v108, v108, v14
v_add_f32 v109, v109, v30
v_add_f32 v108, v108, v15
s_waitcnt lgkmcnt(4)
v_mfma_f32_32x32x16_bf16 v[32:47], v[84:87], v[204:207], v[32:47]
ds_read_b64_tr_b16 v[68:69], v104 offset:512
ds_read_b64_tr_b16 v[70:71], v104 offset:2560
v_add_f32 v109, v109, v31
v_cvt_pk_bf16_f32 v11, v14, v15
v_cvt_pk_bf16_f32 v27, v30, v31
v_max_f32 v160, v108, v109
v_mfma_f32_32x32x16_bf16 v[48:63], v[84:87], v[220:223], v[48:63]
v_mov_b32 v141, 0
v_mov_b32 v142, 0
v_mov_b32 v143, 0
v_mov_b32 v144, 0
v_mov_b32 v145, 0
v_mov_b32 v146, 0
v_mov_b32 v147, 0
v_mov_b32 v148, 0
v_mov_b32 v149, 0
v_mov_b32 v150, 0
v_mov_b32 v151, 0
v_mov_b32 v152, 0
v_mov_b32 v153, 0
v_mov_b32 v154, 0
v_mov_b32 v155, 0
v_mov_b32 v156, 0
v_mov_b32 v157, 0
v_mov_b32 v158, 0
v_mov_b32 v159, 0
v_mov_b32 v110, 0
v_mov_b32 v111, 0
s_waitcnt lgkmcnt(5)
v_mfma_f32_32x32x16_bf16 v[32:47], v[88:91], v[232:235], v[32:47]
s_waitcnt vmcnt(16)
s_barrier
ds_read_b64_tr_b16 v[72:73], v104 offset:1024
ds_read_b64_tr_b16 v[74:75], v104 offset:3072
v_min_f32 v161, v108, v109
v_cmp_nle_f32 vcc, 0x39800000, v161
v_mfma_f32_32x32x16_bf16 v[48:63], v[88:91], v[248:251], v[48:63]
s_mov_b64 s[64:65], vcc
v_cmp_nge_f32 vcc, 0x45800000, v160
s_or_b64 vcc, vcc, s[64:65]
v_mov_b32 v114, v106
v_mov_b32 v116, v106
s_waitcnt lgkmcnt(6)
v_mfma_f32_32x32x16_bf16 v[32:47], v[92:95], v[236:239], v[32:47]
ds_read_b64_tr_b16 v[76:77], v104 offset:1536
ds_read_b64_tr_b16 v[78:79], v104 offset:3584
v_mov_b32 v115, v107
v_mov_b32 v117, v107
v_permlane32_swap_b32 v114, v116
s_nop 0
v_permlane32_swap_b32 v115, v117
v_mfma_f32_32x32x16_bf16 v[48:63], v[92:95], v[252:255], v[48:63]
s_cbranch_vccnz L_tr_16
L_ret_16:
v_add_f32 v112, v112, v108
v_add_f32 v113, v113, v109
v_add_f32 v114, v114, v116
v_add_f32 v115, v115, v117
s_waitcnt lgkmcnt(6)
v_mfma_f32_32x32x16_bf16 a[128:143], v[64:67], v[0:3], 0
ds_read_b64_tr_b16 v[80:81], v104 offset:4096
ds_read_b64_tr_b16 v[82:83], v104 offset:6144
v_rcp_f32 v114, v114
v_rcp_f32 v115, v115
v_mfma_f32_32x32x16_bf16 a[144:159], v[64:67], v[16:19], 0
s_add_u32 m0, s61, 65536
s_add_u32 s66, s61, 196608
buffer_load_dwordx4 v120, s[44:47], s66 offen offset:0 nt lds
v_accvgpr_read_b32 v116, a0
v_accvgpr_read_b32 v117, a1
s_waitcnt lgkmcnt(6)
v_mfma_f32_32x32x16_bf16 a[160:175], v[68:71], v[0:3], 0
ds_read_b64_tr_b16 v[84:85], v104 offset:4608
ds_read_b64_tr_b16 v[86:87], v104 offset:6656
v_exp_f32 v32, v32
v_exp_f32 v48, v48
v_mfma_f32_32x32x16_bf16 a[176:191], v[68:71], v[16:19], 0
buffer_load_dwordx4 v121, s[44:47], s66 offen offset:1024 nt lds
v_exp_f32 v33, v33
v_exp_f32 v49, v49
s_waitcnt lgkmcnt(6)
v_mfma_f32_32x32x16_bf16 a[192:207], v[72:75], v[0:3], 0
ds_read_b64_tr_b16 v[88:89], v104 offset:5120
ds_read_b64_tr_b16 v[90:91], v104 offset:7168
v_exp_f32 v34, v34
v_exp_f32 v50, v50
v_mfma_f32_32x32x16_bf16 a[208:223], v[72:75], v[16:19], 0
v_add_f32 v108, v32, v33
v_add_f32 v109, v48, v49
v_cvt_pk_bf16_f32 v32, v32, v33
v_cvt_pk_bf16_f32 v48, v48, v49
v_mul_f32 v116, v116, v114
s_waitcnt lgkmcnt(6)
v_mfma_f32_32x32x16_bf16 a[224:239], v[76:79], v[0:3], 0
buffer_load_dwordx4 v122, s[44:47], s66 offen offset:2048 nt lds
ds_read_b64_tr_b16 v[92:93], v104 offset:5632
ds_read_b64_tr_b16 v[94:95], v104 offset:7680
v_add_u32 v96, 0x8000, v96
v_exp_f32 v35, v35
v_mfma_f32_32x32x16_bf16 a[240:255], v[76:79], v[16:19], 0
v_exp_f32 v51, v51
v_add_f32 v108, v108, v34
v_add_f32 v109, v109, v50
v_mul_f32 v117, v117, v114
s_waitcnt lgkmcnt(6)
v_mfma_f32_32x32x16_bf16 a[128:143], v[80:83], v[8:11], a[128:143]
buffer_load_dwordx4 v123, s[44:47], s66 offen offset:3072 nt lds
ds_read_b128 v[64:67], v96 offset:0
v_add_u32 v97, 0x8000, v97
v_exp_f32 v36, v36
v_cvt_pk_bf16_f32 v116, v116, v117
v_mfma_f32_32x32x16_bf16 a[144:159], v[80:83], v[24:27], a[144:159]
v_exp_f32 v52, v52
v_add_f32 v108, v108, v35
v_add_f32 v109, v109, v51
v_cvt_pk_bf16_f32 v33, v34, v35
s_waitcnt lgkmcnt(5)
v_mfma_f32_32x32x16_bf16 a[160:175], v[84:87], v[8:11], a[160:175]
ds_read_b128 v[68:71], v97 offset:0
v_add_u32 v98, 0x8000, v98
v_cvt_pk_bf16_f32 v49, v50, v51
v_exp_f32 v37, v37
v_accvgpr_write_b32 a0, v116
v_mfma_f32_32x32x16_bf16 a[176:191], v[84:87], v[24:27], a[176:191]
s_add_u32 m0, s61, 81920
s_nop 0
buffer_load_dwordx4 v124, s[48:51], s66 offen offset:0 nt lds
v_exp_f32 v53, v53
s_waitcnt lgkmcnt(4)
v_mfma_f32_32x32x16_bf16 a[192:207], v[88:91], v[8:11], a[192:207]
ds_read_b128 v[72:75], v98 offset:0
v_add_u32 v99, 0x8000, v99
v_add_f32 v108, v108, v36
v_add_f32 v109, v109, v52
v_exp_f32 v38, v38
v_mfma_f32_32x32x16_bf16 a[208:223], v[88:91], v[24:27], a[208:223]
buffer_load_dwordx4 v125, s[48:51], s66 offen offset:1024 nt lds
v_exp_f32 v54, v54
v_add_f32 v108, v108, v37
v_add_f32 v109, v109, v53
s_waitcnt lgkmcnt(3)
v_mfma_f32_32x32x16_bf16 a[224:239], v[92:95], v[8:11], a[224:239]
ds_read_b128 v[76:79], v99 offset:0
v_add_u32 v100, 0x8000, v100
v_cvt_pk_bf16_f32 v34, v36, v37
v_cvt_pk_bf16_f32 v50, v52, v53
v_exp_f32 v39, v39
v_mfma_f32_32x32x16_bf16 a[240:255], v[92:95], v[24:27], a[240:255]
v_exp_f32 v55, v55
v_add_f32 v108, v108, v38
v_add_f32 v109, v109, v54
v_accvgpr_read_b32 v118, a2
s_waitcnt lgkmcnt(3)
v_mfma_f32_32x32x16_bf16 v[0:15], v[64:67], v[192:195], v[128:143]
buffer_load_dwordx4 v124, s[48:51], s66 offen offset:2048 nt lds
ds_read_b128 v[80:83], v100 offset:0
v_add_u32 v101, 0x8000, v101
v_exp_f32 v40, v40
v_accvgpr_read_b32 v119, a3
v_mfma_f32_32x32x16_bf16 v[16:31], v[64:67], v[208:211], v[144:159]
v_exp_f32 v56, v56
v_add_f32 v108, v108, v39
v_add_f32 v109, v109, v55
v_cvt_pk_bf16_f32 v35, v38, v39
s_waitcnt lgkmcnt(3)
v_mfma_f32_32x32x16_bf16 v[0:15], v[68:71], v[196:199], v[0:15]
buffer_load_dwordx4 v125, s[48:51], s66 offen offset:3072 nt lds
ds_read_b128 v[84:87], v101 offset:0
v_add_u32 v102, 0x8000, v102
v_cvt_pk_bf16_f32 v51, v54, v55
v_exp_f32 v41, v41
v_mfma_f32_32x32x16_bf16 v[16:31], v[68:71], v[212:215], v[16:31]
v_exp_f32 v57, v57
v_add_f32 v108, v108, v40
v_add_f32 v109, v109, v56
v_mul_f32 v118, v118, v114
s_waitcnt lgkmcnt(3)
v_mfma_f32_32x32x16_bf16 v[0:15], v[72:75], v[224:227], v[0:15]
ds_read_b128 v[88:91], v102 offset:0
v_add_u32 v103, 0x8000, v103
v_exp_f32 v42, v42
v_exp_f32 v58, v58
v_mfma_f32_32x32x16_bf16 v[16:31], v[72:75], v[240:243], v[16:31]
v_add_f32 v108, v108, v41
v_add_f32 v109, v109, v57
v_cvt_pk_bf16_f32 v40, v40, v41
v_cvt_pk_bf16_f32 v56, v56, v57
v_mul_f32 v119, v119, v114
s_waitcnt lgkmcnt(3)
v_mfma_f32_32x32x16_bf16 v[0:15], v[76:79], v[228:231], v[0:15]
ds_read_b128 v[92:95], v103 offset:0
v_exp_f32 v43, v43
v_exp_f32 v59, v59
v_add_f32 v108, v108, v42
v_mfma_f32_32x32x16_bf16 v[16:31], v[76:79], v[244:247], v[16:31]
v_add_f32 v109, v109, v58
v_exp_f32 v44, v44
v_exp_f32 v60, v60
s_waitcnt lgkmcnt(3)
v_mfma_f32_32x32x16_bf16 v[0:15], v[80:83], v[200:203], v[0:15]
ds_read_b64_tr_b16 v[64:65], v104 offset:8192
ds_read_b64_tr_b16 v[66:67], v104 offset:10240
v_add_f32 v108, v108, v43
v_add_f32 v109, v109, v59
v_cvt_pk_bf16_f32 v41, v42, v43
v_cvt_pk_bf16_f32 v57, v58, v59
v_mfma_f32_32x32x16_bf16 v[16:31], v[80:83], v[216:219], v[16:31]
v_exp_f32 v45, v45
v_exp_f32 v61, v61
v_add_f32 v108, v108, v44
s_waitcnt lgkmcnt(4)
v_mfma_f32_32x32x16_bf16 v[0:15], v[84:87], v[204:207], v[0:15]
ds_read_b64_tr_b16 v[68:69], v104 offset:8704
ds_read_b64_tr_b16 v[70:71], v104 offset:10752
v_add_f32 v109, v109, v60
v_exp_f32 v46, v46
v_cvt_pk_bf16_f32 v118, v118, v119
v_mfma_f32_32x32x16_bf16 v[16:31], v[84:87], v[220:223], v[16:31]
v_exp_f32 v62, v62
v_add_f32 v108, v108, v45
v_add_f32 v109, v109, v61
v_cvt_pk_bf16_f32 v42, v44, v45
s_waitcnt lgkmcnt(5)
v_mfma_f32_32x32x16_bf16 v[0:15], v[88:91], v[232:235], v[0:15]
ds_read_b64_tr_b16 v[72:73], v104 offset:9216
ds_read_b64_tr_b16 v[74:75], v104 offset:11264
v_cvt_pk_bf16_f32 v58, v60, v61
v_exp_f32 v47, v47
v_accvgpr_write_b32 a1, v118
v_mfma_f32_32x32x16_bf16 v[16:31], v[88:91], v[248:251], v[16:31]
v_exp_f32 v63, v63
v_add_f32 v108, v108, v46
v_add_f32 v109, v109, v62
v_add_f32 v108, v108, v47
s_waitcnt lgkmcnt(6)
v_mfma_f32_32x32x16_bf16 v[0:15], v[92:95], v[236:239], v[0:15]
ds_read_b64_tr_b16 v[76:77], v104 offset:9728
ds_read_b64_tr_b16 v[78:79], v104 offset:11776
v_add_f32 v109, v109, v63
v_cvt_pk_bf16_f32 v43, v46, v47
v_cvt_pk_bf16_f32 v59, v62, v63
v_max_f32 v160, v108, v109
v_mfma_f32_32x32x16_bf16 v[16:31], v[92:95], v[252:255], v[16:31]
v_cmp_nge_f32 vcc, 0x45800000, v160
s_cbranch_vccnz L_tr_17
L_ret_17:
v_add_f32 v112, v112, v108
v_add_f32 v113, v113, v109
v_accvgpr_read_b32 v116, a4
s_waitcnt lgkmcnt(6)
v_mfma_f32_32x32x16_bf16 a[128:143], v[64:67], v[32:35], a[128:143]
ds_read_b64_tr_b16 v[80:81], v104 offset:12288
ds_read_b64_tr_b16 v[82:83], v104 offset:14336
v_accvgpr_read_b32 v117, a5
v_mul_f32 v116, v116, v114
v_mul_f32 v117, v117, v114
v_cvt_pk_bf16_f32 v116, v116, v117
v_mfma_f32_32x32x16_bf16 a[144:159], v[64:67], v[48:51], a[144:159]
v_accvgpr_write_b32 a2, v116
v_accvgpr_read_b32 v118, a6
v_accvgpr_read_b32 v119, a7
v_mul_f32 v118, v118, v114
v_mul_f32 v119, v119, v114
s_waitcnt lgkmcnt(6)
v_mfma_f32_32x32x16_bf16 a[160:175], v[68:71], v[32:35], a[160:175]
ds_read_b64_tr_b16 v[84:85], v104 offset:12800
ds_read_b64_tr_b16 v[86:87], v104 offset:14848
v_exp_f32 v0, v0
v_exp_f32 v16, v16
v_mfma_f32_32x32x16_bf16 a[176:191], v[68:71], v[48:51], a[176:191]
v_exp_f32 v1, v1
v_exp_f32 v17, v17
v_cvt_pk_bf16_f32 v118, v118, v119
s_waitcnt lgkmcnt(6)
v_mfma_f32_32x32x16_bf16 a[192:207], v[72:75], v[32:35], a[192:207]
ds_read_b64_tr_b16 v[88:89], v104 offset:13312
ds_read_b64_tr_b16 v[90:91], v104 offset:15360
v_exp_f32 v2, v2
v_exp_f32 v18, v18
v_mfma_f32_32x32x16_bf16 a[208:223], v[72:75], v[48:51], a[208:223]
v_add_f32 v108, v0, v1
v_add_f32 v109, v16, v17
v_cvt_pk_bf16_f32 v0, v0, v1
v_cvt_pk_bf16_f32 v16, v16, v17
v_accvgpr_write_b32 a3, v118
s_waitcnt lgkmcnt(6)
v_mfma_f32_32x32x16_bf16 a[224:239], v[76:79], v[32:35], a[224:239]
ds_read_b64_tr_b16 v[92:93], v104 offset:13824
ds_read_b64_tr_b16 v[94:95], v104 offset:15872
v_exp_f32 v3, v3
v_exp_f32 v19, v19
v_mfma_f32_32x32x16_bf16 a[240:255], v[76:79], v[48:51], a[240:255]
v_add_f32 v108, v108, v2
v_add_f32 v109, v109, v18
v_exp_f32 v4, v4
v_accvgpr_read_b32 v116, a8
s_waitcnt lgkmcnt(6)
v_mfma_f32_32x32x16_bf16 a[128:143], v[80:83], v[40:43], a[128:143]
ds_read_b128 v[64:67], v96 offset:8192
v_exp_f32 v20, v20
v_add_f32 v108, v108, v3
v_add_f32 v109, v109, v19
v_cvt_pk_bf16_f32 v1, v2, v3
v_mfma_f32_32x32x16_bf16 a[144:159], v[80:83], v[56:59], a[144:159]
v_cvt_pk_bf16_f32 v17, v18, v19
v_exp_f32 v5, v5
v_exp_f32 v21, v21
s_waitcnt lgkmcnt(5)
v_mfma_f32_32x32x16_bf16 a[160:175], v[84:87], v[40:43], a[160:175]
ds_read_b128 v[68:71], v97 offset:8192
v_add_f32 v108, v108, v4
v_add_f32 v109, v109, v20
v_exp_f32 v6, v6
v_accvgpr_read_b32 v117, a9
v_mfma_f32_32x32x16_bf16 a[176:191], v[84:87], v[56:59], a[176:191]
v_exp_f32 v22, v22
v_add_f32 v108, v108, v5
v_add_f32 v109, v109, v21
v_cvt_pk_bf16_f32 v2, v4, v5
s_waitcnt lgkmcnt(4)
v_mfma_f32_32x32x16_bf16 a[192:207], v[88:91], v[40:43], a[192:207]
ds_read_b128 v[72:75], v98 offset:8192
v_cvt_pk_bf16_f32 v18, v20, v21
v_exp_f32 v7, v7
v_exp_f32 v23, v23
v_mfma_f32_32x32x16_bf16 a[208:223], v[88:91], v[56:59], a[208:223]
v_add_f32 v108, v108, v6
v_add_f32 v109, v109, v22
v_exp_f32 v8, v8
v_mul_f32 v116, v116, v114
s_waitcnt lgkmcnt(3)
v_mfma_f32_32x32x16_bf16 a[224:239], v[92:95], v[40:43], a[224:239]
ds_read_b128 v[76:79], v99 offset:8192
v_exp_f32 v24, v24
v_add_f32 v108, v108, v7
v_add_f32 v109, v109, v23
v_cvt_pk_bf16_f32 v3, v6, v7
v_mfma_f32_32x32x16_bf16 a[240:255], v[92:95], v[56:59], a[240:255]
v_cvt_pk_bf16_f32 v19, v22, v23
v_exp_f32 v9, v9
v_exp_f32 v25, v25
s_waitcnt lgkmcnt(3)
v_mfma_f32_32x32x16_bf16 v[32:47], v[64:67], v[192:195], v[128:143]
ds_read_b128 v[80:83], v100 offset:8192
v_add_f32 v108, v108, v8
v_add_f32 v109, v109, v24
v_exp_f32 v10, v10
v_mul_f32 v117, v117, v114
v_mfma_f32_32x32x16_bf16 v[48:63], v[64:67], v[208:211], v[144:159]
v_exp_f32 v26, v26
v_add_f32 v108, v108, v9
v_add_f32 v109, v109, v25
v_cvt_pk_bf16_f32 v8, v8, v9
s_waitcnt lgkmcnt(3)
v_mfma_f32_32x32x16_bf16 v[32:47], v[68:71], v[196:199], v[32:47]
ds_read_b128 v[84:87], v101 offset:8192
v_cvt_pk_bf16_f32 v24, v24, v25
v_exp_f32 v11, v11
v_exp_f32 v27, v27
v_mfma_f32_32x32x16_bf16 v[48:63], v[68:71], v[212:215], v[48:63]
v_add_f32 v108, v108, v10
v_add_f32 v109, v109, v26
v_exp_f32 v12, v12
v_cvt_pk_bf16_f32 v116, v116, v117
s_waitcnt lgkmcnt(3)
v_mfma_f32_32x32x16_bf16 v[32:47], v[72:75], v[224:227], v[32:47]
ds_read_b128 v[88:91], v102 offset:8192
v_exp_f32 v28, v28
v_add_f32 v108, v108, v11
v_add_f32 v109, v109, v27
v_cvt_pk_bf16_f32 v9, v10, v11
v_mfma_f32_32x32x16_bf16 v[48:63], v[72:75], v[240:243], v[48:63]
v_cvt_pk_bf16_f32 v25, v26, v27
v_exp_f32 v13, v13
v_exp_f32 v29, v29
s_waitcnt lgkmcnt(3)
v_mfma_f32_32x32x16_bf16 v[32:47], v[76:79], v[228:231], v[32:47]
ds_read_b128 v[92:95], v103 offset:8192
v_add_u32 v104, 0x8000, v104
v_add_f32 v108, v108, v12
v_add_f32 v109, v109, v28
v_exp_f32 v14, v14
v_mfma_f32_32x32x16_bf16 v[48:63], v[76:79], v[244:247], v[48:63]
v_exp_f32 v30, v30
v_add_f32 v108, v108, v13
v_add_f32 v109, v109, v29
v_cvt_pk_bf16_f32 v10, v12, v13
s_waitcnt lgkmcnt(3)
v_mfma_f32_32x32x16_bf16 v[32:47], v[80:83], v[200:203], v[32:47]
ds_read_b64_tr_b16 v[64:65], v104 offset:0
ds_read_b64_tr_b16 v[66:67], v104 offset:2048
v_cvt_pk_bf16_f32 v26, v28, v29
v_exp_f32 v15, v15
v_accvgpr_write_b32 a4, v116
v_mfma_f32_32x32x16_bf16 v[48:63], v[80:83], v[216:219], v[48:63]
v_exp_f32 v31, v31
v_add_f32 v108, v108, v14
v_add_f32 v109, v109, v30
v_add_f32 v108, v108, v15
s_waitcnt lgkmcnt(4)
v_mfma_f32_32x32x16_bf16 v[32:47], v[84:87], v[204:207], v[32:47]
ds_read_b64_tr_b16 v[68:69], v104 offset:512
ds_read_b64_tr_b16 v[70:71], v104 offset:2560
v_add_f32 v109, v109, v31
v_cvt_pk_bf16_f32 v11, v14, v15
v_cvt_pk_bf16_f32 v27, v30, v31
v_max_f32 v160, v108, v109
v_mfma_f32_32x32x16_bf16 v[48:63], v[84:87], v[220:223], v[48:63]
v_cmp_nge_f32 vcc, 0x45800000, v160
v_accvgpr_read_b32 v118, a10
v_accvgpr_read_b32 v119, a11
v_mul_f32 v118, v118, v114
v_mul_f32 v119, v119, v114
s_waitcnt lgkmcnt(5)
v_mfma_f32_32x32x16_bf16 v[32:47], v[88:91], v[232:235], v[32:47]
s_waitcnt vmcnt(16)
s_barrier
ds_read_b64_tr_b16 v[72:73], v104 offset:1024
ds_read_b64_tr_b16 v[74:75], v104 offset:3072
v_cvt_pk_bf16_f32 v118, v118, v119
v_accvgpr_write_b32 a5, v118
v_mfma_f32_32x32x16_bf16 v[48:63], v[88:91], v[248:251], v[48:63]
v_accvgpr_read_b32 v116, a12
v_accvgpr_read_b32 v117, a13
v_mul_f32 v116, v116, v114
v_mul_f32 v117, v117, v114
v_cvt_pk_bf16_f32 v116, v116, v117
s_waitcnt lgkmcnt(6)
v_mfma_f32_32x32x16_bf16 v[32:47], v[92:95], v[236:239], v[32:47]
ds_read_b64_tr_b16 v[76:77], v104 offset:1536
ds_read_b64_tr_b16 v[78:79], v104 offset:3584
v_accvgpr_write_b32 a6, v116
v_accvgpr_read_b32 v118, a14
v_accvgpr_read_b32 v119, a15
v_mul_f32 v118, v118, v114
v_mfma_f32_32x32x16_bf16 v[48:63], v[92:95], v[252:255], v[48:63]
s_cbranch_vccnz L_tr_18
L_ret_18:
v_add_f32 v112, v112, v108
v_add_f32 v113, v113, v109
v_mul_f32 v119, v119, v114
v_cvt_pk_bf16_f32 v118, v118, v119
s_waitcnt lgkmcnt(6)
v_mfma_f32_32x32x16_bf16 a[128:143], v[64:67], v[0:3], a[128:143]
ds_read_b64_tr_b16 v[80:81], v104 offset:4096
ds_read_b64_tr_b16 v[82:83], v104 offset:6144
v_accvgpr_write_b32 a7, v118
v_accvgpr_read_b32 v116, a16
v_accvgpr_read_b32 v117, a17
v_mul_f32 v116, v116, v115
v_mfma_f32_32x32x16_bf16 a[144:159], v[64:67], v[16:19], a[144:159]
s_add_u32 m0, s61, 98304
s_add_u32 s66, s61, 212992
buffer_load_dwordx4 v120, s[44:47], s66 offen offset:0 nt lds
v_mul_f32 v117, v117, v115
v_cvt_pk_bf16_f32 v116, v116, v117
s_waitcnt lgkmcnt(6)
v_mfma_f32_32x32x16_bf16 a[160:175], v[68:71], v[0:3], a[160:175]
ds_read_b64_tr_b16 v[84:85], v104 offset:4608
ds_read_b64_tr_b16 v[86:87], v104 offset:6656
v_exp_f32 v32, v32
v_exp_f32 v48, v48
v_mfma_f32_32x32x16_bf16 a[176:191], v[68:71], v[16:19], a[176:191]
buffer_load_dwordx4 v121, s[44:47], s66 offen offset:1024 nt lds
v_exp_f32 v33, v33
v_exp_f32 v49, v49
s_waitcnt lgkmcnt(6)
v_mfma_f32_32x32x16_bf16 a[192:207], v[72:75], v[0:3], a[192:207]
ds_read_b64_tr_b16 v[88:89], v104 offset:5120
ds_read_b64_tr_b16 v[90:91], v104 offset:7168
v_exp_f32 v34, v34
v_exp_f32 v50, v50
v_mfma_f32_32x32x16_bf16 a[208:223], v[72:75], v[16:19], a[208:223]
v_add_f32 v108, v32, v33
v_add_f32 v109, v48, v49
v_cvt_pk_bf16_f32 v32, v32, v33
v_cvt_pk_bf16_f32 v48, v48, v49
v_accvgpr_write_b32 a16, v116
s_waitcnt lgkmcnt(6)
v_mfma_f32_32x32x16_bf16 a[224:239], v[76:79], v[0:3], a[224:239]
buffer_load_dwordx4 v122, s[44:47], s66 offen offset:2048 nt lds
ds_read_b64_tr_b16 v[92:93], v104 offset:5632
ds_read_b64_tr_b16 v[94:95], v104 offset:7680
v_add_u32 v96, 0xfffe0000, v96
v_exp_f32 v35, v35
v_mfma_f32_32x32x16_bf16 a[240:255], v[76:79], v[16:19], a[240:255]
v_exp_f32 v51, v51
v_add_f32 v108, v108, v34
v_add_f32 v109, v109, v50
v_accvgpr_read_b32 v118, a18
s_waitcnt lgkmcnt(6)
v_mfma_f32_32x32x16_bf16 a[128:143], v[80:83], v[8:11], a[128:143]
buffer_load_dwordx4 v123, s[44:47], s66 offen offset:3072 nt lds
ds_read_b128 v[64:67], v96 offset:0
v_add_u32 v97, 0xfffe0000, v97
v_exp_f32 v36, v36
v_accvgpr_read_b32 v119, a19
v_mfma_f32_32x32x16_bf16 a[144:159], v[80:83], v[24:27], a[144:159]
v_exp_f32 v52, v52
v_add_f32 v108, v108, v35
v_add_f32 v109, v109, v51
v_cvt_pk_bf16_f32 v33, v34, v35
s_waitcnt lgkmcnt(5)
v_mfma_f32_32x32x16_bf16 a[160:175], v[84:87], v[8:11], a[160:175]
ds_read_b128 v[68:71], v97 offset:0
v_add_u32 v98, 0xfffe0000, v98
v_cvt_pk_bf16_f32 v49, v50, v51
v_exp_f32 v37, v37
v_mul_f32 v118, v118, v115
v_mfma_f32_32x32x16_bf16 a[176:191], v[84:87], v[24:27], a[176:191]
s_add_u32 m0, s61, 114688
s_nop 0
buffer_load_dwordx4 v124, s[48:51], s66 offen offset:0 nt lds
v_exp_f32 v53, v53
s_waitcnt lgkmcnt(4)
v_mfma_f32_32x32x16_bf16 a[192:207], v[88:91], v[8:11], a[192:207]
ds_read_b128 v[72:75], v98 offset:0
v_add_u32 v99, 0xfffe0000, v99
v_add_f32 v108, v108, v36
v_add_f32 v109, v109, v52
v_exp_f32 v38, v38
v_mfma_f32_32x32x16_bf16 a[208:223], v[88:91], v[24:27], a[208:223]
buffer_load_dwordx4 v125, s[48:51], s66 offen offset:1024 nt lds
v_exp_f32 v54, v54
v_add_f32 v108, v108, v37
v_add_f32 v109, v109, v53
s_waitcnt lgkmcnt(3)
v_mfma_f32_32x32x16_bf16 a[224:239], v[92:95], v[8:11], a[224:239]
ds_read_b128 v[76:79], v99 offset:0
v_add_u32 v100, 0xfffe0000, v100
v_cvt_pk_bf16_f32 v34, v36, v37
v_cvt_pk_bf16_f32 v50, v52, v53
v_exp_f32 v39, v39
v_mfma_f32_32x32x16_bf16 a[240:255], v[92:95], v[24:27], a[240:255]
v_exp_f32 v55, v55
v_add_f32 v108, v108, v38
v_add_f32 v109, v109, v54
v_mul_f32 v119, v119, v115
s_waitcnt lgkmcnt(3)
v_mfma_f32_32x32x16_bf16 v[0:15], v[64:67], v[192:195], v[128:143]
buffer_load_dwordx4 v124, s[48:51], s66 offen offset:2048 nt lds
ds_read_b128 v[80:83], v100 offset:0
v_add_u32 v101, 0xfffe0000, v101
v_exp_f32 v40, v40
v_cvt_pk_bf16_f32 v118, v118, v119
v_mfma_f32_32x32x16_bf16 v[16:31], v[64:67], v[208:211], v[144:159]
v_exp_f32 v56, v56
v_add_f32 v108, v108, v39
v_add_f32 v109, v109, v55
v_cvt_pk_bf16_f32 v35, v38, v39
s_waitcnt lgkmcnt(3)
v_mfma_f32_32x32x16_bf16 v[0:15], v[68:71], v[196:199], v[0:15]
buffer_load_dwordx4 v125, s[48:51], s66 offen offset:3072 nt lds
ds_read_b128 v[84:87], v101 offset:0
v_add_u32 v102, 0xfffe0000, v102
v_cvt_pk_bf16_f32 v51, v54, v55
v_exp_f32 v41, v41
v_mfma_f32_32x32x16_bf16 v[16:31], v[68:71], v[212:215], v[16:31]
v_exp_f32 v57, v57
v_add_f32 v108, v108, v40
v_add_f32 v109, v109, v56
v_accvgpr_write_b32 a17, v118
s_waitcnt lgkmcnt(3)
v_mfma_f32_32x32x16_bf16 v[0:15], v[72:75], v[224:227], v[0:15]
ds_read_b128 v[88:91], v102 offset:0
v_add_u32 v103, 0xfffe0000, v103
v_exp_f32 v42, v42
v_exp_f32 v58, v58
v_mfma_f32_32x32x16_bf16 v[16:31], v[72:75], v[240:243], v[16:31]
v_add_f32 v108, v108, v41
v_add_f32 v109, v109, v57
v_cvt_pk_bf16_f32 v40, v40, v41
v_cvt_pk_bf16_f32 v56, v56, v57
v_accvgpr_read_b32 v116, a20
s_waitcnt lgkmcnt(3)
v_mfma_f32_32x32x16_bf16 v[0:15], v[76:79], v[228:231], v[0:15]
ds_read_b128 v[92:95], v103 offset:0
v_exp_f32 v43, v43
v_exp_f32 v59, v59
v_add_f32 v108, v108, v42
v_mfma_f32_32x32x16_bf16 v[16:31], v[76:79], v[244:247], v[16:31]
v_add_f32 v109, v109, v58
v_exp_f32 v44, v44
v_exp_f32 v60, v60
s_waitcnt lgkmcnt(3)
v_mfma_f32_32x32x16_bf16 v[0:15], v[80:83], v[200:203], v[0:15]
ds_read_b64_tr_b16 v[64:65], v104 offset:8192
ds_read_b64_tr_b16 v[66:67], v104 offset:10240
v_add_f32 v108, v108, v43
v_add_f32 v109, v109, v59
v_cvt_pk_bf16_f32 v41, v42, v43
v_cvt_pk_bf16_f32 v57, v58, v59
v_mfma_f32_32x32x16_bf16 v[16:31], v[80:83], v[216:219], v[16:31]
v_exp_f32 v45, v45
v_exp_f32 v61, v61
v_add_f32 v108, v108, v44
s_waitcnt lgkmcnt(4)
v_mfma_f32_32x32x16_bf16 v[0:15], v[84:87], v[204:207], v[0:15]
ds_read_b64_tr_b16 v[68:69], v104 offset:8704
ds_read_b64_tr_b16 v[70:71], v104 offset:10752
v_add_f32 v109, v109, v60
v_exp_f32 v46, v46
v_accvgpr_read_b32 v117, a21
v_mfma_f32_32x32x16_bf16 v[16:31], v[84:87], v[220:223], v[16:31]
v_exp_f32 v62, v62
v_add_f32 v108, v108, v45
v_add_f32 v109, v109, v61
v_cvt_pk_bf16_f32 v42, v44, v45
s_waitcnt lgkmcnt(5)
v_mfma_f32_32x32x16_bf16 v[0:15], v[88:91], v[232:235], v[0:15]
ds_read_b64_tr_b16 v[72:73], v104 offset:9216
ds_read_b64_tr_b16 v[74:75], v104 offset:11264
v_cvt_pk_bf16_f32 v58, v60, v61
v_exp_f32 v47, v47
v_mul_f32 v116, v116, v115
v_mfma_f32_32x32x16_bf16 v[16:31], v[88:91], v[248:251], v[16:31]
v_exp_f32 v63, v63
v_add_f32 v108, v108, v46
v_add_f32 v109, v109, v62
v_add_f32 v108, v108, v47
s_waitcnt lgkmcnt(6)
v_mfma_f32_32x32x16_bf16 v[0:15], v[92:95], v[236:239], v[0:15]
ds_read_b64_tr_b16 v[76:77], v104 offset:9728
ds_read_b64_tr_b16 v[78:79], v104 offset:11776
v_add_f32 v109, v109, v63
v_cvt_pk_bf16_f32 v43, v46, v47
v_cvt_pk_bf16_f32 v59, v62, v63
v_max_f32 v160, v108, v109
v_mfma_f32_32x32x16_bf16 v[16:31], v[92:95], v[252:255], v[16:31]
v_cmp_nge_f32 vcc, 0x45800000, v160
s_cbranch_vccnz L_tr_19
L_ret_19:
v_add_f32 v112, v112, v108
v_add_f32 v113, v113, v109
v_mul_f32 v117, v117, v115
s_waitcnt lgkmcnt(6)
v_mfma_f32_32x32x16_bf16 a[128:143], v[64:67], v[32:35], a[128:143]
ds_read_b64_tr_b16 v[80:81], v104 offset:12288
ds_read_b64_tr_b16 v[82:83], v104 offset:14336
v_cvt_pk_bf16_f32 v116, v116, v117
v_accvgpr_write_b32 a18, v116
v_accvgpr_read_b32 v118, a22
v_accvgpr_read_b32 v119, a23
v_mfma_f32_32x32x16_bf16 a[144:159], v[64:67], v[48:51], a[144:159]
v_mul_f32 v118, v118, v115
v_mul_f32 v119, v119, v115
v_cvt_pk_bf16_f32 v118, v118, v119
v_accvgpr_write_b32 a19, v118
v_accvgpr_read_b32 v116, a24
s_waitcnt lgkmcnt(6)
v_mfma_f32_32x32x16_bf16 a[160:175], v[68:71], v[32:35], a[160:175]
ds_read_b64_tr_b16 v[84:85], v104 offset:12800
ds_read_b64_tr_b16 v[86:87], v104 offset:14848
v_exp_f32 v0, v0
v_exp_f32 v16, v16
v_mfma_f32_32x32x16_bf16 a[176:191], v[68:71], v[48:51], a[176:191]
v_exp_f32 v1, v1
v_exp_f32 v17, v17
v_accvgpr_read_b32 v117, a25
s_waitcnt lgkmcnt(6)
v_mfma_f32_32x32x16_bf16 a[192:207], v[72:75], v[32:35], a[192:207]
ds_read_b64_tr_b16 v[88:89], v104 offset:13312
ds_read_b64_tr_b16 v[90:91], v104 offset:15360
v_exp_f32 v2, v2
v_exp_f32 v18, v18
v_mfma_f32_32x32x16_bf16 a[208:223], v[72:75], v[48:51], a[208:223]
v_add_f32 v108, v0, v1
v_add_f32 v109, v16, v17
v_cvt_pk_bf16_f32 v0, v0, v1
v_cvt_pk_bf16_f32 v16, v16, v17
v_mul_f32 v116, v116, v115
s_waitcnt lgkmcnt(6)
v_mfma_f32_32x32x16_bf16 a[224:239], v[76:79], v[32:35], a[224:239]
ds_read_b64_tr_b16 v[92:93], v104 offset:13824
ds_read_b64_tr_b16 v[94:95], v104 offset:15872
v_exp_f32 v3, v3
v_exp_f32 v19, v19
v_mfma_f32_32x32x16_bf16 a[240:255], v[76:79], v[48:51], a[240:255]
v_add_f32 v108, v108, v2
v_add_f32 v109, v109, v18
v_exp_f32 v4, v4
v_mul_f32 v117, v117, v115
s_waitcnt lgkmcnt(6)
v_mfma_f32_32x32x16_bf16 a[128:143], v[80:83], v[40:43], a[128:143]
ds_read_b128 v[64:67], v96 offset:8192
v_exp_f32 v20, v20
v_add_f32 v108, v108, v3
v_add_f32 v109, v109, v19
v_cvt_pk_bf16_f32 v1, v2, v3
v_mfma_f32_32x32x16_bf16 a[144:159], v[80:83], v[56:59], a[144:159]
v_cvt_pk_bf16_f32 v17, v18, v19
v_exp_f32 v5, v5
v_exp_f32 v21, v21
s_waitcnt lgkmcnt(5)
v_mfma_f32_32x32x16_bf16 a[160:175], v[84:87], v[40:43], a[160:175]
ds_read_b128 v[68:71], v97 offset:8192
v_add_f32 v108, v108, v4
v_add_f32 v109, v109, v20
v_exp_f32 v6, v6
v_cvt_pk_bf16_f32 v116, v116, v117
v_mfma_f32_32x32x16_bf16 a[176:191], v[84:87], v[56:59], a[176:191]
v_exp_f32 v22, v22
v_add_f32 v108, v108, v5
v_add_f32 v109, v109, v21
v_cvt_pk_bf16_f32 v2, v4, v5
s_waitcnt lgkmcnt(4)
v_mfma_f32_32x32x16_bf16 a[192:207], v[88:91], v[40:43], a[192:207]
ds_read_b128 v[72:75], v98 offset:8192
v_cvt_pk_bf16_f32 v18, v20, v21
v_exp_f32 v7, v7
v_exp_f32 v23, v23
v_mfma_f32_32x32x16_bf16 a[208:223], v[88:91], v[56:59], a[208:223]
v_add_f32 v108, v108, v6
v_add_f32 v109, v109, v22
v_exp_f32 v8, v8
v_accvgpr_write_b32 a20, v116
s_waitcnt lgkmcnt(3)
v_mfma_f32_32x32x16_bf16 a[224:239], v[92:95], v[40:43], a[224:239]
ds_read_b128 v[76:79], v99 offset:8192
v_exp_f32 v24, v24
v_add_f32 v108, v108, v7
v_add_f32 v109, v109, v23
v_cvt_pk_bf16_f32 v3, v6, v7
v_mfma_f32_32x32x16_bf16 a[240:255], v[92:95], v[56:59], a[240:255]
v_cvt_pk_bf16_f32 v19, v22, v23
v_exp_f32 v9, v9
v_exp_f32 v25, v25
s_waitcnt lgkmcnt(3)
v_mfma_f32_32x32x16_bf16 v[32:47], v[64:67], v[192:195], v[128:143]
ds_read_b128 v[80:83], v100 offset:8192
v_add_f32 v108, v108, v8
v_add_f32 v109, v109, v24
v_exp_f32 v10, v10
v_accvgpr_read_b32 v118, a26
v_mfma_f32_32x32x16_bf16 v[48:63], v[64:67], v[208:211], v[144:159]
v_exp_f32 v26, v26
v_add_f32 v108, v108, v9
v_add_f32 v109, v109, v25
v_cvt_pk_bf16_f32 v8, v8, v9
s_waitcnt lgkmcnt(3)
v_mfma_f32_32x32x16_bf16 v[32:47], v[68:71], v[196:199], v[32:47]
ds_read_b128 v[84:87], v101 offset:8192
v_cvt_pk_bf16_f32 v24, v24, v25
v_exp_f32 v11, v11
v_exp_f32 v27, v27
v_mfma_f32_32x32x16_bf16 v[48:63], v[68:71], v[212:215], v[48:63]
v_add_f32 v108, v108, v10
v_add_f32 v109, v109, v26
v_exp_f32 v12, v12
v_accvgpr_read_b32 v119, a27
s_waitcnt lgkmcnt(3)
v_mfma_f32_32x32x16_bf16 v[32:47], v[72:75], v[224:227], v[32:47]
ds_read_b128 v[88:91], v102 offset:8192
v_exp_f32 v28, v28
v_add_f32 v108, v108, v11
v_add_f32 v109, v109, v27
v_cvt_pk_bf16_f32 v9, v10, v11
v_mfma_f32_32x32x16_bf16 v[48:63], v[72:75], v[240:243], v[48:63]
v_cvt_pk_bf16_f32 v25, v26, v27
v_exp_f32 v13, v13
v_exp_f32 v29, v29
s_waitcnt lgkmcnt(3)
v_mfma_f32_32x32x16_bf16 v[32:47], v[76:79], v[228:231], v[32:47]
ds_read_b128 v[92:95], v103 offset:8192
v_add_u32 v104, 0xfffe0000, v104
v_add_f32 v108, v108, v12
v_add_f32 v109, v109, v28
v_exp_f32 v14, v14
v_mfma_f32_32x32x16_bf16 v[48:63], v[76:79], v[244:247], v[48:63]
v_exp_f32 v30, v30
v_add_f32 v108, v108, v13
v_add_f32 v109, v109, v29
v_cvt_pk_bf16_f32 v10, v12, v13
s_waitcnt lgkmcnt(3)
v_mfma_f32_32x32x16_bf16 v[32:47], v[80:83], v[200:203], v[32:47]
ds_read_b64_tr_b16 v[64:65], v104 offset:0
ds_read_b64_tr_b16 v[66:67], v104 offset:2048
v_cvt_pk_bf16_f32 v26, v28, v29
v_exp_f32 v15, v15
v_mul_f32 v118, v118, v115
v_mfma_f32_32x32x16_bf16 v[48:63], v[80:83], v[216:219], v[48:63]
v_exp_f32 v31, v31
v_add_f32 v108, v108, v14
v_add_f32 v109, v109, v30
v_add_f32 v108, v108, v15
s_waitcnt lgkmcnt(4)
v_mfma_f32_32x32x16_bf16 v[32:47], v[84:87], v[204:207], v[32:47]
ds_read_b64_tr_b16 v[68:69], v104 offset:512
ds_read_b64_tr_b16 v[70:71], v104 offset:2560
v_add_f32 v109, v109, v31
v_cvt_pk_bf16_f32 v11, v14, v15
v_cvt_pk_bf16_f32 v27, v30, v31
v_max_f32 v160, v108, v109
v_mfma_f32_32x32x16_bf16 v[48:63], v[84:87], v[220:223], v[48:63]
v_cmp_nge_f32 vcc, 0x45800000, v160
v_mul_f32 v119, v119, v115
v_cvt_pk_bf16_f32 v118, v118, v119
v_accvgpr_write_b32 a21, v118
v_accvgpr_read_b32 v116, a28
s_waitcnt lgkmcnt(5)
v_mfma_f32_32x32x16_bf16 v[32:47], v[88:91], v[232:235], v[32:47]
s_waitcnt vmcnt(16)
s_barrier
ds_read_b64_tr_b16 v[72:73], v104 offset:1024
ds_read_b64_tr_b16 v[74:75], v104 offset:3072
v_accvgpr_read_b32 v117, a29
v_mul_f32 v116, v116, v115
v_mfma_f32_32x32x16_bf16 v[48:63], v[88:91], v[248:251], v[48:63]
v_mul_f32 v117, v117, v115
v_cvt_pk_bf16_f32 v116, v116, v117
v_accvgpr_write_b32 a22, v116
v_accvgpr_read_b32 v118, a30
v_accvgpr_read_b32 v119, a31
s_waitcnt lgkmcnt(6)
v_mfma_f32_32x32x16_bf16 v[32:47], v[92:95], v[236:239], v[32:47]
ds_read_b64_tr_b16 v[76:77], v104 offset:1536
ds_read_b64_tr_b16 v[78:79], v104 offset:3584
v_mul_f32 v118, v118, v115
v_mul_f32 v119, v119, v115
v_cvt_pk_bf16_f32 v118, v118, v119
v_accvgpr_write_b32 a23, v118
v_mfma_f32_32x32x16_bf16 v[48:63], v[92:95], v[252:255], v[48:63]
s_cbranch_vccnz L_tr_20
L_ret_20:
v_add_f32 v112, v112, v108
v_add_f32 v113, v113, v109
v_accvgpr_read_b32 v116, a32
v_accvgpr_read_b32 v117, a33
s_waitcnt lgkmcnt(6)
v_mfma_f32_32x32x16_bf16 a[128:143], v[64:67], v[0:3], a[128:143]
ds_read_b64_tr_b16 v[80:81], v104 offset:4096
ds_read_b64_tr_b16 v[82:83], v104 offset:6144
v_mul_f32 v116, v116, v114
v_mul_f32 v117, v117, v114
v_cvt_pk_bf16_f32 v116, v116, v117
v_accvgpr_write_b32 a32, v116
v_mfma_f32_32x32x16_bf16 a[144:159], v[64:67], v[16:19], a[144:159]
s_add_u32 m0, s61, 131072
s_add_u32 s66, s61, 229376
buffer_load_dwordx4 v120, s[44:47], s66 offen offset:0 nt lds
v_accvgpr_read_b32 v118, a34
v_accvgpr_read_b32 v119, a35
s_waitcnt lgkmcnt(6)
v_mfma_f32_32x32x16_bf16 a[160:175], v[68:71], v[0:3], a[160:175]
ds_read_b64_tr_b16 v[84:85], v104 offset:4608
ds_read_b64_tr_b16 v[86:87], v104 offset:6656
v_exp_f32 v32, v32
v_exp_f32 v48, v48
v_mfma_f32_32x32x16_bf16 a[176:191], v[68:71], v[16:19], a[176:191]
buffer_load_dwordx4 v121, s[44:47], s66 offen offset:1024 nt lds
v_exp_f32 v33, v33
v_exp_f32 v49, v49
s_waitcnt lgkmcnt(6)
v_mfma_f32_32x32x16_bf16 a[192:207], v[72:75], v[0:3], a[192:207]
ds_read_b64_tr_b16 v[88:89], v104 offset:5120
ds_read_b64_tr_b16 v[90:91], v104 offset:7168
v_exp_f32 v34, v34
v_exp_f32 v50, v50
v_mfma_f32_32x32x16_bf16 a[208:223], v[72:75], v[16:19], a[208:223]
v_add_f32 v108, v32, v33
v_add_f32 v109, v48, v49
v_cvt_pk_bf16_f32 v32, v32, v33
v_cvt_pk_bf16_f32 v48, v48, v49
v_mul_f32 v118, v118, v114
s_waitcnt lgkmcnt(6)
v_mfma_f32_32x32x16_bf16 a[224:239], v[76:79], v[0:3], a[224:239]
buffer_load_dwordx4 v122, s[44:47], s66 offen offset:2048 nt lds
ds_read_b64_tr_b16 v[92:93], v104 offset:5632
ds_read_b64_tr_b16 v[94:95], v104 offset:7680
v_add_u32 v96, 0x8000, v96
v_exp_f32 v35, v35
v_mfma_f32_32x32x16_bf16 a[240:255], v[76:79], v[16:19], a[240:255]
v_exp_f32 v51, v51
v_add_f32 v108, v108, v34
v_add_f32 v109, v109, v50
v_mul_f32 v119, v119, v114
s_waitcnt lgkmcnt(6)
v_mfma_f32_32x32x16_bf16 a[128:143], v[80:83], v[8:11], a[128:143]
buffer_load_dwordx4 v123, s[44:47], s66 offen offset:3072 nt lds
ds_read_b128 v[64:67], v96 offset:0
v_add_u32 v97, 0x8000, v97
v_exp_f32 v36, v36
v_cvt_pk_bf16_f32 v118, v118, v119
v_mfma_f32_32x32x16_bf16 a[144:159], v[80:83], v[24:27], a[144:159]
v_exp_f32 v52, v52
v_add_f32 v108, v108, v35
v_add_f32 v109, v109, v51
v_cvt_pk_bf16_f32 v33, v34, v35
s_waitcnt lgkmcnt(5)
v_mfma_f32_32x32x16_bf16 a[160:175], v[84:87], v[8:11], a[160:175]
ds_read_b128 v[68:71], v97 offset:0
v_add_u32 v98, 0x8000, v98
v_cvt_pk_bf16_f32 v49, v50, v51
v_exp_f32 v37, v37
v_accvgpr_write_b32 a33, v118
v_mfma_f32_32x32x16_bf16 a[176:191], v[84:87], v[24:27], a[176:191]
s_add_u32 m0, s61, 147456
s_nop 0
buffer_load_dwordx4 v124, s[48:51], s66 offen offset:0 nt lds
v_exp_f32 v53, v53
s_waitcnt lgkmcnt(4)
v_mfma_f32_32x32x16_bf16 a[192:207], v[88:91], v[8:11], a[192:207]
ds_read_b128 v[72:75], v98 offset:0
v_add_u32 v99, 0x8000, v99
v_add_f32 v108, v108, v36
v_add_f32 v109, v109, v52
v_exp_f32 v38, v38
v_mfma_f32_32x32x16_bf16 a[208:223], v[88:91], v[24:27], a[208:223]
buffer_load_dwordx4 v125, s[48:51], s66 offen offset:1024 nt lds
v_exp_f32 v54, v54
v_add_f32 v108, v108, v37
v_add_f32 v109, v109, v53
s_waitcnt lgkmcnt(3)
v_mfma_f32_32x32x16_bf16 a[224:239], v[92:95], v[8:11], a[224:239]
ds_read_b128 v[76:79], v99 offset:0
v_add_u32 v100, 0x8000, v100
v_cvt_pk_bf16_f32 v34, v36, v37
v_cvt_pk_bf16_f32 v50, v52, v53
v_exp_f32 v39, v39
v_mfma_f32_32x32x16_bf16 a[240:255], v[92:95], v[24:27], a[240:255]
v_exp_f32 v55, v55
v_add_f32 v108, v108, v38
v_add_f32 v109, v109, v54
v_accvgpr_read_b32 v116, a36
s_waitcnt lgkmcnt(3)
v_mfma_f32_32x32x16_bf16 v[0:15], v[64:67], v[192:195], v[128:143]
buffer_load_dwordx4 v124, s[48:51], s66 offen offset:2048 nt lds
ds_read_b128 v[80:83], v100 offset:0
v_add_u32 v101, 0x8000, v101
v_exp_f32 v40, v40
v_accvgpr_read_b32 v117, a37
v_mfma_f32_32x32x16_bf16 v[16:31], v[64:67], v[208:211], v[144:159]
v_exp_f32 v56, v56
v_add_f32 v108, v108, v39
v_add_f32 v109, v109, v55
v_cvt_pk_bf16_f32 v35, v38, v39
s_waitcnt lgkmcnt(3)
v_mfma_f32_32x32x16_bf16 v[0:15], v[68:71], v[196:199], v[0:15]
buffer_load_dwordx4 v125, s[48:51], s66 offen offset:3072 nt lds
ds_read_b128 v[84:87], v101 offset:0
v_add_u32 v102, 0x8000, v102
v_cvt_pk_bf16_f32 v51, v54, v55
v_exp_f32 v41, v41
v_mfma_f32_32x32x16_bf16 v[16:31], v[68:71], v[212:215], v[16:31]
v_exp_f32 v57, v57
v_add_f32 v108, v108, v40
v_add_f32 v109, v109, v56
v_mul_f32 v116, v116, v114
s_waitcnt lgkmcnt(3)
v_mfma_f32_32x32x16_bf16 v[0:15], v[72:75], v[224:227], v[0:15]
ds_read_b128 v[88:91], v102 offset:0
v_add_u32 v103, 0x8000, v103
v_exp_f32 v42, v42
v_exp_f32 v58, v58
v_mfma_f32_32x32x16_bf16 v[16:31], v[72:75], v[240:243], v[16:31]
v_add_f32 v108, v108, v41
v_add_f32 v109, v109, v57
v_cvt_pk_bf16_f32 v40, v40, v41
v_cvt_pk_bf16_f32 v56, v56, v57
v_mul_f32 v117, v117, v114
s_waitcnt lgkmcnt(3)
v_mfma_f32_32x32x16_bf16 v[0:15], v[76:79], v[228:231], v[0:15]
ds_read_b128 v[92:95], v103 offset:0
v_exp_f32 v43, v43
v_exp_f32 v59, v59
v_add_f32 v108, v108, v42
v_mfma_f32_32x32x16_bf16 v[16:31], v[76:79], v[244:247], v[16:31]
v_add_f32 v109, v109, v58
v_exp_f32 v44, v44
v_exp_f32 v60, v60
s_waitcnt lgkmcnt(3)
v_mfma_f32_32x32x16_bf16 v[0:15], v[80:83], v[200:203], v[0:15]
ds_read_b64_tr_b16 v[64:65], v104 offset:8192
ds_read_b64_tr_b16 v[66:67], v104 offset:10240
v_add_f32 v108, v108, v43
v_add_f32 v109, v109, v59
v_cvt_pk_bf16_f32 v41, v42, v43
v_cvt_pk_bf16_f32 v57, v58, v59
v_mfma_f32_32x32x16_bf16 v[16:31], v[80:83], v[216:219], v[16:31]
v_exp_f32 v45, v45
v_exp_f32 v61, v61
v_add_f32 v108, v108, v44
s_waitcnt lgkmcnt(4)
v_mfma_f32_32x32x16_bf16 v[0:15], v[84:87], v[204:207], v[0:15]
ds_read_b64_tr_b16 v[68:69], v104 offset:8704
ds_read_b64_tr_b16 v[70:71], v104 offset:10752
v_add_f32 v109, v109, v60
v_exp_f32 v46, v46
v_cvt_pk_bf16_f32 v116, v116, v117
v_mfma_f32_32x32x16_bf16 v[16:31], v[84:87], v[220:223], v[16:31]
v_exp_f32 v62, v62
v_add_f32 v108, v108, v45
v_add_f32 v109, v109, v61
v_cvt_pk_bf16_f32 v42, v44, v45
s_waitcnt lgkmcnt(5)
v_mfma_f32_32x32x16_bf16 v[0:15], v[88:91], v[232:235], v[0:15]
ds_read_b64_tr_b16 v[72:73], v104 offset:9216
ds_read_b64_tr_b16 v[74:75], v104 offset:11264
v_cvt_pk_bf16_f32 v58, v60, v61
v_exp_f32 v47, v47
v_accvgpr_write_b32 a34, v116
v_mfma_f32_32x32x16_bf16 v[16:31], v[88:91], v[248:251], v[16:31]
v_exp_f32 v63, v63
v_add_f32 v108, v108, v46
v_add_f32 v109, v109, v62
v_add_f32 v108, v108, v47
s_waitcnt lgkmcnt(6)
v_mfma_f32_32x32x16_bf16 v[0:15], v[92:95], v[236:239], v[0:15]
ds_read_b64_tr_b16 v[76:77], v104 offset:9728
ds_read_b64_tr_b16 v[78:79], v104 offset:11776
v_add_f32 v109, v109, v63
v_cvt_pk_bf16_f32 v43, v46, v47
v_cvt_pk_bf16_f32 v59, v62, v63
v_max_f32 v160, v108, v109
v_mfma_f32_32x32x16_bf16 v[16:31], v[92:95], v[252:255], v[16:31]
v_cmp_nge_f32 vcc, 0x45800000, v160
s_cbranch_vccnz L_tr_21
L_ret_21:
v_add_f32 v112, v112, v108
v_add_f32 v113, v113, v109
v_accvgpr_read_b32 v118, a38
s_waitcnt lgkmcnt(6)
v_mfma_f32_32x32x16_bf16 a[128:143], v[64:67], v[32:35], a[128:143]
ds_read_b64_tr_b16 v[80:81], v104 offset:12288
ds_read_b64_tr_b16 v[82:83], v104 offset:14336
v_accvgpr_read_b32 v119, a39
v_mul_f32 v118, v118, v114
v_mul_f32 v119, v119, v114
v_cvt_pk_bf16_f32 v118, v118, v119
v_mfma_f32_32x32x16_bf16 a[144:159], v[64:67], v[48:51], a[144:159]
v_accvgpr_write_b32 a35, v118
v_accvgpr_read_b32 v116, a40
v_accvgpr_read_b32 v117, a41
v_mul_f32 v116, v116, v114
v_mul_f32 v117, v117, v114
s_waitcnt lgkmcnt(6)
v_mfma_f32_32x32x16_bf16 a[160:175], v[68:71], v[32:35], a[160:175]
ds_read_b64_tr_b16 v[84:85], v104 offset:12800
ds_read_b64_tr_b16 v[86:87], v104 offset:14848
v_exp_f32 v0, v0
v_exp_f32 v16, v16
v_mfma_f32_32x32x16_bf16 a[176:191], v[68:71], v[48:51], a[176:191]
v_exp_f32 v1, v1
v_exp_f32 v17, v17
v_cvt_pk_bf16_f32 v116, v116, v117
s_waitcnt lgkmcnt(6)
v_mfma_f32_32x32x16_bf16 a[192:207], v[72:75], v[32:35], a[192:207]
ds_read_b64_tr_b16 v[88:89], v104 offset:13312
ds_read_b64_tr_b16 v[90:91], v104 offset:15360
v_exp_f32 v2, v2
v_exp_f32 v18, v18
v_mfma_f32_32x32x16_bf16 a[208:223], v[72:75], v[48:51], a[208:223]
v_add_f32 v108, v0, v1
v_add_f32 v109, v16, v17
v_cvt_pk_bf16_f32 v0, v0, v1
v_cvt_pk_bf16_f32 v16, v16, v17
v_accvgpr_write_b32 a36, v116
s_waitcnt lgkmcnt(6)
v_mfma_f32_32x32x16_bf16 a[224:239], v[76:79], v[32:35], a[224:239]
ds_read_b64_tr_b16 v[92:93], v104 offset:13824
ds_read_b64_tr_b16 v[94:95], v104 offset:15872
v_exp_f32 v3, v3
v_exp_f32 v19, v19
v_mfma_f32_32x32x16_bf16 a[240:255], v[76:79], v[48:51], a[240:255]
v_add_f32 v108, v108, v2
v_add_f32 v109, v109, v18
v_exp_f32 v4, v4
v_accvgpr_read_b32 v118, a42
s_waitcnt lgkmcnt(6)
v_mfma_f32_32x32x16_bf16 a[128:143], v[80:83], v[40:43], a[128:143]
ds_read_b128 v[64:67], v96 offset:8192
v_exp_f32 v20, v20
v_add_f32 v108, v108, v3
v_add_f32 v109, v109, v19
v_cvt_pk_bf16_f32 v1, v2, v3
v_mfma_f32_32x32x16_bf16 a[144:159], v[80:83], v[56:59], a[144:159]
v_cvt_pk_bf16_f32 v17, v18, v19
v_exp_f32 v5, v5
v_exp_f32 v21, v21
s_waitcnt lgkmcnt(5)
v_mfma_f32_32x32x16_bf16 a[160:175], v[84:87], v[40:43], a[160:175]
ds_read_b128 v[68:71], v97 offset:8192
v_add_f32 v108, v108, v4
v_add_f32 v109, v109, v20
v_exp_f32 v6, v6
v_accvgpr_read_b32 v119, a43
v_mfma_f32_32x32x16_bf16 a[176:191], v[84:87], v[56:59], a[176:191]
v_exp_f32 v22, v22
v_add_f32 v108, v108, v5
v_add_f32 v109, v109, v21
v_cvt_pk_bf16_f32 v2, v4, v5
s_waitcnt lgkmcnt(4)
v_mfma_f32_32x32x16_bf16 a[192:207], v[88:91], v[40:43], a[192:207]
ds_read_b128 v[72:75], v98 offset:8192
v_cvt_pk_bf16_f32 v18, v20, v21
v_exp_f32 v7, v7
v_exp_f32 v23, v23
v_mfma_f32_32x32x16_bf16 a[208:223], v[88:91], v[56:59], a[208:223]
v_add_f32 v108, v108, v6
v_add_f32 v109, v109, v22
v_exp_f32 v8, v8
v_mul_f32 v118, v118, v114
s_waitcnt lgkmcnt(3)
v_mfma_f32_32x32x16_bf16 a[224:239], v[92:95], v[40:43], a[224:239]
ds_read_b128 v[76:79], v99 offset:8192
v_exp_f32 v24, v24
v_add_f32 v108, v108, v7
v_add_f32 v109, v109, v23
v_cvt_pk_bf16_f32 v3, v6, v7
v_mfma_f32_32x32x16_bf16 a[240:255], v[92:95], v[56:59], a[240:255]
v_cvt_pk_bf16_f32 v19, v22, v23
v_exp_f32 v9, v9
v_exp_f32 v25, v25
s_waitcnt lgkmcnt(3)
v_mfma_f32_32x32x16_bf16 v[32:47], v[64:67], v[192:195], v[128:143]
ds_read_b128 v[80:83], v100 offset:8192
v_add_f32 v108, v108, v8
v_add_f32 v109, v109, v24
v_exp_f32 v10, v10
v_mul_f32 v119, v119, v114
v_mfma_f32_32x32x16_bf16 v[48:63], v[64:67], v[208:211], v[144:159]
v_exp_f32 v26, v26
v_add_f32 v108, v108, v9
v_add_f32 v109, v109, v25
v_cvt_pk_bf16_f32 v8, v8, v9
s_waitcnt lgkmcnt(3)
v_mfma_f32_32x32x16_bf16 v[32:47], v[68:71], v[196:199], v[32:47]
ds_read_b128 v[84:87], v101 offset:8192
v_cvt_pk_bf16_f32 v24, v24, v25
v_exp_f32 v11, v11
v_exp_f32 v27, v27
v_mfma_f32_32x32x16_bf16 v[48:63], v[68:71], v[212:215], v[48:63]
v_add_f32 v108, v108, v10
v_add_f32 v109, v109, v26
v_exp_f32 v12, v12
v_cvt_pk_bf16_f32 v118, v118, v119
s_waitcnt lgkmcnt(3)
v_mfma_f32_32x32x16_bf16 v[32:47], v[72:75], v[224:227], v[32:47]
ds_read_b128 v[88:91], v102 offset:8192
v_exp_f32 v28, v28
v_add_f32 v108, v108, v11
v_add_f32 v109, v109, v27
v_cvt_pk_bf16_f32 v9, v10, v11
v_mfma_f32_32x32x16_bf16 v[48:63], v[72:75], v[240:243], v[48:63]
v_cvt_pk_bf16_f32 v25, v26, v27
v_exp_f32 v13, v13
v_exp_f32 v29, v29
s_waitcnt lgkmcnt(3)
v_mfma_f32_32x32x16_bf16 v[32:47], v[76:79], v[228:231], v[32:47]
ds_read_b128 v[92:95], v103 offset:8192
v_add_u32 v104, 0x8000, v104
v_add_f32 v108, v108, v12
v_add_f32 v109, v109, v28
v_exp_f32 v14, v14
v_mfma_f32_32x32x16_bf16 v[48:63], v[76:79], v[244:247], v[48:63]
v_exp_f32 v30, v30
v_add_f32 v108, v108, v13
v_add_f32 v109, v109, v29
v_cvt_pk_bf16_f32 v10, v12, v13
s_waitcnt lgkmcnt(3)
v_mfma_f32_32x32x16_bf16 v[32:47], v[80:83], v[200:203], v[32:47]
ds_read_b64_tr_b16 v[64:65], v104 offset:0
ds_read_b64_tr_b16 v[66:67], v104 offset:2048
v_cvt_pk_bf16_f32 v26, v28, v29
v_exp_f32 v15, v15
v_accvgpr_write_b32 a37, v118
v_mfma_f32_32x32x16_bf16 v[48:63], v[80:83], v[216:219], v[48:63]
v_exp_f32 v31, v31
v_add_f32 v108, v108, v14
v_add_f32 v109, v109, v30
v_add_f32 v108, v108, v15
s_waitcnt lgkmcnt(4)
v_mfma_f32_32x32x16_bf16 v[32:47], v[84:87], v[204:207], v[32:47]
ds_read_b64_tr_b16 v[68:69], v104 offset:512
ds_read_b64_tr_b16 v[70:71], v104 offset:2560
v_add_f32 v109, v109, v31
v_cvt_pk_bf16_f32 v11, v14, v15
v_cvt_pk_bf16_f32 v27, v30, v31
v_max_f32 v160, v108, v109
v_mfma_f32_32x32x16_bf16 v[48:63], v[84:87], v[220:223], v[48:63]
v_cmp_nge_f32 vcc, 0x45800000, v160
v_accvgpr_read_b32 v116, a44
v_accvgpr_read_b32 v117, a45
v_mul_f32 v116, v116, v114
v_mul_f32 v117, v117, v114
s_waitcnt lgkmcnt(5)
v_mfma_f32_32x32x16_bf16 v[32:47], v[88:91], v[232:235], v[32:47]
s_waitcnt vmcnt(16)
s_barrier
ds_read_b64_tr_b16 v[72:73], v104 offset:1024
ds_read_b64_tr_b16 v[74:75], v104 offset:3072
v_cvt_pk_bf16_f32 v116, v116, v117
v_accvgpr_write_b32 a38, v116
v_mfma_f32_32x32x16_bf16 v[48:63], v[88:91], v[248:251], v[48:63]
v_accvgpr_read_b32 v118, a46
v_accvgpr_read_b32 v119, a47
v_mul_f32 v118, v118, v114
v_mul_f32 v119, v119, v114
v_cvt_pk_bf16_f32 v118, v118, v119
s_waitcnt lgkmcnt(6)
v_mfma_f32_32x32x16_bf16 v[32:47], v[92:95], v[236:239], v[32:47]
ds_read_b64_tr_b16 v[76:77], v104 offset:1536
ds_read_b64_tr_b16 v[78:79], v104 offset:3584
v_accvgpr_write_b32 a39, v118
v_accvgpr_read_b32 v116, a48
v_accvgpr_read_b32 v117, a49
v_mul_f32 v116, v116, v115
v_mfma_f32_32x32x16_bf16 v[48:63], v[92:95], v[252:255], v[48:63]
s_cbranch_vccnz L_tr_22
L_ret_22:
v_add_f32 v112, v112, v108
v_add_f32 v113, v113, v109
v_mul_f32 v117, v117, v115
v_cvt_pk_bf16_f32 v116, v116, v117
s_waitcnt lgkmcnt(6)
v_mfma_f32_32x32x16_bf16 a[128:143], v[64:67], v[0:3], a[128:143]
ds_read_b64_tr_b16 v[80:81], v104 offset:4096
ds_read_b64_tr_b16 v[82:83], v104 offset:6144
v_accvgpr_write_b32 a48, v116
v_accvgpr_read_b32 v118, a50
v_accvgpr_read_b32 v119, a51
v_mul_f32 v118, v118, v115
v_mfma_f32_32x32x16_bf16 a[144:159], v[64:67], v[16:19], a[144:159]
s_add_u32 m0, s61, 0
s_add_u32 s66, s61, 245760
buffer_load_dwordx4 v120, s[44:47], s66 offen offset:0 nt lds
v_mul_f32 v119, v119, v115
v_cvt_pk_bf16_f32 v118, v118, v119
s_waitcnt lgkmcnt(6)
v_mfma_f32_32x32x16_bf16 a[160:175], v[68:71], v[0:3], a[160:175]
ds_read_b64_tr_b16 v[84:85], v104 offset:4608
ds_read_b64_tr_b16 v[86:87], v104 offset:6656
v_exp_f32 v32, v32
v_exp_f32 v48, v48
v_mfma_f32_32x32x16_bf16 a[176:191], v[68:71], v[16:19], a[176:191]
buffer_load_dwordx4 v121, s[44:47], s66 offen offset:1024 nt lds
v_exp_f32 v33, v33
v_exp_f32 v49, v49
s_waitcnt lgkmcnt(6)
v_mfma_f32_32x32x16_bf16 a[192:207], v[72:75], v[0:3], a[192:207]
ds_read_b64_tr_b16 v[88:89], v104 offset:5120
ds_read_b64_tr_b16 v[90:91], v104 offset:7168
v_exp_f32 v34, v34
v_exp_f32 v50, v50
v_mfma_f32_32x32x16_bf16 a[208:223], v[72:75], v[16:19], a[208:223]
v_add_f32 v108, v32, v33
v_add_f32 v109, v48, v49
v_cvt_pk_bf16_f32 v32, v32, v33
v_cvt_pk_bf16_f32 v48, v48, v49
v_accvgpr_write_b32 a49, v118
s_waitcnt lgkmcnt(6)
v_mfma_f32_32x32x16_bf16 a[224:239], v[76:79], v[0:3], a[224:239]
buffer_load_dwordx4 v122, s[44:47], s66 offen offset:2048 nt lds
ds_read_b64_tr_b16 v[92:93], v104 offset:5632
ds_read_b64_tr_b16 v[94:95], v104 offset:7680
v_add_u32 v96, 0x8000, v96
v_exp_f32 v35, v35
v_mfma_f32_32x32x16_bf16 a[240:255], v[76:79], v[16:19], a[240:255]
v_exp_f32 v51, v51
v_add_f32 v108, v108, v34
v_add_f32 v109, v109, v50
v_accvgpr_read_b32 v116, a52
s_waitcnt lgkmcnt(6)
v_mfma_f32_32x32x16_bf16 a[128:143], v[80:83], v[8:11], a[128:143]
buffer_load_dwordx4 v123, s[44:47], s66 offen offset:3072 nt lds
ds_read_b128 v[64:67], v96 offset:0
v_add_u32 v97, 0x8000, v97
v_exp_f32 v36, v36
v_accvgpr_read_b32 v117, a53
v_mfma_f32_32x32x16_bf16 a[144:159], v[80:83], v[24:27], a[144:159]
v_exp_f32 v52, v52
v_add_f32 v108, v108, v35
v_add_f32 v109, v109, v51
v_cvt_pk_bf16_f32 v33, v34, v35
s_waitcnt lgkmcnt(5)
v_mfma_f32_32x32x16_bf16 a[160:175], v[84:87], v[8:11], a[160:175]
ds_read_b128 v[68:71], v97 offset:0
v_add_u32 v98, 0x8000, v98
v_cvt_pk_bf16_f32 v49, v50, v51
v_exp_f32 v37, v37
v_mul_f32 v116, v116, v115
v_mfma_f32_32x32x16_bf16 a[176:191], v[84:87], v[24:27], a[176:191]
s_add_u32 m0, s61, 16384
s_nop 0
buffer_load_dwordx4 v124, s[48:51], s66 offen offset:0 nt lds
v_exp_f32 v53, v53
s_waitcnt lgkmcnt(4)
v_mfma_f32_32x32x16_bf16 a[192:207], v[88:91], v[8:11], a[192:207]
ds_read_b128 v[72:75], v98 offset:0
v_add_u32 v99, 0x8000, v99
v_add_f32 v108, v108, v36
v_add_f32 v109, v109, v52
v_exp_f32 v38, v38
v_mfma_f32_32x32x16_bf16 a[208:223], v[88:91], v[24:27], a[208:223]
buffer_load_dwordx4 v125, s[48:51], s66 offen offset:1024 nt lds
v_exp_f32 v54, v54
v_add_f32 v108, v108, v37
v_add_f32 v109, v109, v53
s_waitcnt lgkmcnt(3)
v_mfma_f32_32x32x16_bf16 a[224:239], v[92:95], v[8:11], a[224:239]
ds_read_b128 v[76:79], v99 offset:0
v_add_u32 v100, 0x8000, v100
v_cvt_pk_bf16_f32 v34, v36, v37
v_cvt_pk_bf16_f32 v50, v52, v53
v_exp_f32 v39, v39
v_mfma_f32_32x32x16_bf16 a[240:255], v[92:95], v[24:27], a[240:255]
v_exp_f32 v55, v55
v_add_f32 v108, v108, v38
v_add_f32 v109, v109, v54
v_mul_f32 v117, v117, v115
s_waitcnt lgkmcnt(3)
v_mfma_f32_32x32x16_bf16 v[0:15], v[64:67], v[192:195], v[128:143]
buffer_load_dwordx4 v124, s[48:51], s66 offen offset:2048 nt lds
ds_read_b128 v[80:83], v100 offset:0
v_add_u32 v101, 0x8000, v101
v_exp_f32 v40, v40
v_cvt_pk_bf16_f32 v116, v116, v117
v_mfma_f32_32x32x16_bf16 v[16:31], v[64:67], v[208:211], v[144:159]
v_exp_f32 v56, v56
v_add_f32 v108, v108, v39
v_add_f32 v109, v109, v55
v_cvt_pk_bf16_f32 v35, v38, v39
s_waitcnt lgkmcnt(3)
v_mfma_f32_32x32x16_bf16 v[0:15], v[68:71], v[196:199], v[0:15]
buffer_load_dwordx4 v125, s[48:51], s66 offen offset:3072 nt lds
ds_read_b128 v[84:87], v101 offset:0
v_add_u32 v102, 0x8000, v102
v_cvt_pk_bf16_f32 v51, v54, v55
v_exp_f32 v41, v41
v_mfma_f32_32x32x16_bf16 v[16:31], v[68:71], v[212:215], v[16:31]
v_exp_f32 v57, v57
v_add_f32 v108, v108, v40
v_add_f32 v109, v109, v56
v_accvgpr_write_b32 a50, v116
s_waitcnt lgkmcnt(3)
v_mfma_f32_32x32x16_bf16 v[0:15], v[72:75], v[224:227], v[0:15]
ds_read_b128 v[88:91], v102 offset:0
v_add_u32 v103, 0x8000, v103
v_exp_f32 v42, v42
v_exp_f32 v58, v58
v_mfma_f32_32x32x16_bf16 v[16:31], v[72:75], v[240:243], v[16:31]
v_add_f32 v108, v108, v41
v_add_f32 v109, v109, v57
v_cvt_pk_bf16_f32 v40, v40, v41
v_cvt_pk_bf16_f32 v56, v56, v57
v_accvgpr_read_b32 v118, a54
s_waitcnt lgkmcnt(3)
v_mfma_f32_32x32x16_bf16 v[0:15], v[76:79], v[228:231], v[0:15]
ds_read_b128 v[92:95], v103 offset:0
v_exp_f32 v43, v43
v_exp_f32 v59, v59
v_add_f32 v108, v108, v42
v_mfma_f32_32x32x16_bf16 v[16:31], v[76:79], v[244:247], v[16:31]
v_add_f32 v109, v109, v58
v_exp_f32 v44, v44
v_exp_f32 v60, v60
s_waitcnt lgkmcnt(3)
v_mfma_f32_32x32x16_bf16 v[0:15], v[80:83], v[200:203], v[0:15]
ds_read_b64_tr_b16 v[64:65], v104 offset:8192
ds_read_b64_tr_b16 v[66:67], v104 offset:10240
v_add_f32 v108, v108, v43
v_add_f32 v109, v109, v59
v_cvt_pk_bf16_f32 v41, v42, v43
v_cvt_pk_bf16_f32 v57, v58, v59
v_mfma_f32_32x32x16_bf16 v[16:31], v[80:83], v[216:219], v[16:31]
v_exp_f32 v45, v45
v_exp_f32 v61, v61
v_add_f32 v108, v108, v44
s_waitcnt lgkmcnt(4)
v_mfma_f32_32x32x16_bf16 v[0:15], v[84:87], v[204:207], v[0:15]
ds_read_b64_tr_b16 v[68:69], v104 offset:8704
ds_read_b64_tr_b16 v[70:71], v104 offset:10752
v_add_f32 v109, v109, v60
v_exp_f32 v46, v46
v_accvgpr_read_b32 v119, a55
v_mfma_f32_32x32x16_bf16 v[16:31], v[84:87], v[220:223], v[16:31]
v_exp_f32 v62, v62
v_add_f32 v108, v108, v45
v_add_f32 v109, v109, v61
v_cvt_pk_bf16_f32 v42, v44, v45
s_waitcnt lgkmcnt(5)
v_mfma_f32_32x32x16_bf16 v[0:15], v[88:91], v[232:235], v[0:15]
ds_read_b64_tr_b16 v[72:73], v104 offset:9216
ds_read_b64_tr_b16 v[74:75], v104 offset:11264
v_cvt_pk_bf16_f32 v58, v60, v61
v_exp_f32 v47, v47
v_mul_f32 v118, v118, v115
v_mfma_f32_32x32x16_bf16 v[16:31], v[88:91], v[248:251], v[16:31]
v_exp_f32 v63, v63
v_add_f32 v108, v108, v46
v_add_f32 v109, v109, v62
v_add_f32 v108, v108, v47
s_waitcnt lgkmcnt(6)
v_mfma_f32_32x32x16_bf16 v[0:15], v[92:95], v[236:239], v[0:15]
ds_read_b64_tr_b16 v[76:77], v104 offset:9728
ds_read_b64_tr_b16 v[78:79], v104 offset:11776
v_add_f32 v109, v109, v63
v_cvt_pk_bf16_f32 v43, v46, v47
v_cvt_pk_bf16_f32 v59, v62, v63
v_max_f32 v160, v108, v109
v_mfma_f32_32x32x16_bf16 v[16:31], v[92:95], v[252:255], v[16:31]
v_cmp_nge_f32 vcc, 0x45800000, v160
s_cbranch_vccnz L_tr_23
L_ret_23:
v_add_f32 v112, v112, v108
v_add_f32 v113, v113, v109
v_mul_f32 v119, v119, v115
s_waitcnt lgkmcnt(6)
v_mfma_f32_32x32x16_bf16 a[128:143], v[64:67], v[32:35], a[128:143]
ds_read_b64_tr_b16 v[80:81], v104 offset:12288
ds_read_b64_tr_b16 v[82:83], v104 offset:14336
v_cvt_pk_bf16_f32 v118, v118, v119
v_accvgpr_write_b32 a51, v118
v_accvgpr_read_b32 v116, a56
v_accvgpr_read_b32 v117, a57
v_mfma_f32_32x32x16_bf16 a[144:159], v[64:67], v[48:51], a[144:159]
s_and_b32 s65, s60, 1
s_lshl_b32 s65, s65, 14
s_add_u32 s65, s65, 65536
v_add_u32 v123, s65, v127
v_mul_f32 v116, v116, v115
s_waitcnt lgkmcnt(6)
v_mfma_f32_32x32x16_bf16 a[160:175], v[68:71], v[32:35], a[160:175]
ds_read_b64_tr_b16 v[84:85], v104 offset:12800
ds_read_b64_tr_b16 v[86:87], v104 offset:14848
v_exp_f32 v0, v0
v_exp_f32 v16, v16
v_mfma_f32_32x32x16_bf16 a[176:191], v[68:71], v[48:51], a[176:191]
v_exp_f32 v1, v1
v_exp_f32 v17, v17
v_mul_f32 v117, v117, v115
s_waitcnt lgkmcnt(6)
v_mfma_f32_32x32x16_bf16 a[192:207], v[72:75], v[32:35], a[192:207]
ds_read_b64_tr_b16 v[88:89], v104 offset:13312
ds_read_b64_tr_b16 v[90:91], v104 offset:15360
v_exp_f32 v2, v2
v_exp_f32 v18, v18
v_mfma_f32_32x32x16_bf16 a[208:223], v[72:75], v[48:51], a[208:223]
v_add_f32 v108, v0, v1
v_add_f32 v109, v16, v17
v_cvt_pk_bf16_f32 v0, v0, v1
v_cvt_pk_bf16_f32 v16, v16, v17
v_cvt_pk_bf16_f32 v116, v116, v117
s_waitcnt lgkmcnt(6)
v_mfma_f32_32x32x16_bf16 a[224:239], v[76:79], v[32:35], a[224:239]
ds_read_b64_tr_b16 v[92:93], v104 offset:13824
ds_read_b64_tr_b16 v[94:95], v104 offset:15872
v_exp_f32 v3, v3
v_exp_f32 v19, v19
v_mfma_f32_32x32x16_bf16 a[240:255], v[76:79], v[48:51], a[240:255]
v_add_f32 v108, v108, v2
v_add_f32 v109, v109, v18
v_exp_f32 v4, v4
v_accvgpr_write_b32 a52, v116
s_waitcnt lgkmcnt(6)
v_mfma_f32_32x32x16_bf16 a[128:143], v[80:83], v[40:43], a[128:143]
ds_read_b128 v[64:67], v96 offset:8192
v_exp_f32 v20, v20
v_add_f32 v108, v108, v3
v_add_f32 v109, v109, v19
v_cvt_pk_bf16_f32 v1, v2, v3
v_mfma_f32_32x32x16_bf16 a[144:159], v[80:83], v[56:59], a[144:159]
v_cvt_pk_bf16_f32 v17, v18, v19
v_exp_f32 v5, v5
v_exp_f32 v21, v21
s_waitcnt lgkmcnt(5)
v_mfma_f32_32x32x16_bf16 a[160:175], v[84:87], v[40:43], a[160:175]
ds_read_b128 v[68:71], v97 offset:8192
v_add_f32 v108, v108, v4
v_add_f32 v109, v109, v20
v_exp_f32 v6, v6
v_accvgpr_read_b32 v118, a58
v_mfma_f32_32x32x16_bf16 a[176:191], v[84:87], v[56:59], a[176:191]
s_mov_b32 s46, 0
v_exp_f32 v22, v22
v_add_f32 v108, v108, v5
v_add_f32 v109, v109, v21
s_waitcnt lgkmcnt(4)
v_mfma_f32_32x32x16_bf16 a[192:207], v[88:91], v[40:43], a[192:207]
buffer_load_dwordx2 v[124:125], off, s[76:79], s94 offset:16 sc1
ds_read_b128 v[72:75], v98 offset:8192
v_cvt_pk_bf16_f32 v2, v4, v5
v_cvt_pk_bf16_f32 v18, v20, v21
v_exp_f32 v7, v7
v_mfma_f32_32x32x16_bf16 a[208:223], v[88:91], v[56:59], a[208:223]
buffer_load_dword v121, off, s[76:79], s93 offset:8 sc1
v_exp_f32 v23, v23
v_add_f32 v108, v108, v6
v_add_f32 v109, v109, v22
s_waitcnt lgkmcnt(3)
v_mfma_f32_32x32x16_bf16 a[224:239], v[92:95], v[40:43], a[224:239]
s_cmp_eq_u32 s60, 3
s_cselect_b32 s48, 0xffff, -1
s_mov_b32 s49, s48
s_and_b32 s65, s93, 0x480
s_cmp_eq_u32 s65, 0x480
s_cselect_b32 s50, 0xffff, -1
s_mov_b32 s51, s50
v_mfma_f32_32x32x16_bf16 a[240:255], v[92:95], v[56:59], a[240:255]
ds_read_b128 v[76:79], v99 offset:8192
v_exp_f32 v8, v8
v_exp_f32 v24, v24
s_waitcnt lgkmcnt(3)
v_mfma_f32_32x32x16_bf16 v[32:47], v[64:67], v[192:195], v[128:143]
ds_read_b128 v[80:83], v100 offset:8192
v_add_f32 v108, v108, v7
v_add_f32 v109, v109, v23
v_cvt_pk_bf16_f32 v3, v6, v7
v_cvt_pk_bf16_f32 v19, v22, v23
v_accvgpr_read_b32 v119, a59
v_mfma_f32_32x32x16_bf16 v[48:63], v[64:67], v[208:211], v[144:159]
v_exp_f32 v9, v9
v_exp_f32 v25, v25
v_add_f32 v108, v108, v8
s_waitcnt lgkmcnt(3)
v_mfma_f32_32x32x16_bf16 v[32:47], v[68:71], v[196:199], v[32:47]
ds_read_b128 v[84:87], v101 offset:8192
v_add_f32 v109, v109, v24
v_exp_f32 v10, v10
v_exp_f32 v26, v26
v_mfma_f32_32x32x16_bf16 v[48:63], v[68:71], v[212:215], v[48:63]
v_add_f32 v108, v108, v9
v_add_f32 v109, v109, v25
v_cvt_pk_bf16_f32 v8, v8, v9
v_cvt_pk_bf16_f32 v24, v24, v25
v_mul_f32 v118, v118, v115
s_waitcnt lgkmcnt(3)
v_mfma_f32_32x32x16_bf16 v[32:47], v[72:75], v[224:227], v[32:47]
ds_read_b128 v[88:91], v102 offset:8192
v_exp_f32 v11, v11
v_exp_f32 v27, v27
v_add_f32 v108, v108, v10
v_mfma_f32_32x32x16_bf16 v[48:63], v[72:75], v[240:243], v[48:63]
v_add_f32 v109, v109, v26
v_exp_f32 v12, v12
v_exp_f32 v28, v28
s_waitcnt lgkmcnt(3)
v_mfma_f32_32x32x16_bf16 v[32:47], v[76:79], v[228:231], v[32:47]
ds_read_b128 v[92:95], v103 offset:8192
v_add_u32 v104, 0x8000, v104
v_add_f32 v108, v108, v11
v_add_f32 v109, v109, v27
v_cvt_pk_bf16_f32 v9, v10, v11
v_cvt_pk_bf16_f32 v25, v26, v27
v_mfma_f32_32x32x16_bf16 v[48:63], v[76:79], v[244:247], v[48:63]
v_exp_f32 v13, v13
v_exp_f32 v29, v29
v_add_f32 v108, v108, v12
s_waitcnt lgkmcnt(3)
v_mfma_f32_32x32x16_bf16 v[32:47], v[80:83], v[200:203], v[32:47]
ds_read_b64_tr_b16 v[64:65], v104 offset:0
ds_read_b64_tr_b16 v[66:67], v104 offset:2048
v_add_f32 v109, v109, v28
v_exp_f32 v14, v14
v_mul_f32 v119, v119, v115
v_mfma_f32_32x32x16_bf16 v[48:63], v[80:83], v[216:219], v[48:63]
v_exp_f32 v30, v30
v_add_f32 v108, v108, v13
v_add_f32 v109, v109, v29
v_cvt_pk_bf16_f32 v10, v12, v13
s_waitcnt lgkmcnt(4)
v_mfma_f32_32x32x16_bf16 v[32:47], v[84:87], v[204:207], v[32:47]
ds_read_b64_tr_b16 v[68:69], v104 offset:512
ds_read_b64_tr_b16 v[70:71], v104 offset:2560
v_cvt_pk_bf16_f32 v26, v28, v29
v_exp_f32 v15, v15
v_cvt_pk_bf16_f32 v118, v118, v119
v_mfma_f32_32x32x16_bf16 v[48:63], v[84:87], v[220:223], v[48:63]
v_exp_f32 v31, v31
v_add_f32 v108, v108, v14
v_add_f32 v109, v109, v30
v_add_f32 v108, v108, v15
s_waitcnt lgkmcnt(5)
v_mfma_f32_32x32x16_bf16 v[32:47], v[88:91], v[232:235], v[32:47]
s_waitcnt vmcnt(18)
s_barrier
ds_read_b64_tr_b16 v[72:73], v104 offset:1024
ds_read_b64_tr_b16 v[74:75], v104 offset:3072
v_add_f32 v109, v109, v31
v_cvt_pk_bf16_f32 v11, v14, v15
v_mfma_f32_32x32x16_bf16 v[48:63], v[88:91], v[248:251], v[48:63]
v_cvt_pk_bf16_f32 v27, v30, v31
v_max_f32 v160, v108, v109
v_cmp_nge_f32 vcc, 0x45800000, v160
v_accvgpr_write_b32 a53, v118
v_accvgpr_read_b32 v116, a60
s_waitcnt lgkmcnt(6)
v_mfma_f32_32x32x16_bf16 v[32:47], v[92:95], v[236:239], v[32:47]
ds_read_b64_tr_b16 v[76:77], v104 offset:1536
ds_read_b64_tr_b16 v[78:79], v104 offset:3584
v_accvgpr_read_b32 v117, a61
v_mul_f32 v116, v116, v115
v_mul_f32 v117, v117, v115
v_cvt_pk_bf16_f32 v116, v116, v117
v_mfma_f32_32x32x16_bf16 v[48:63], v[92:95], v[252:255], v[48:63]
s_cbranch_vccnz L_tr_24
L_ret_24:
v_add_f32 v112, v112, v108
v_add_f32 v113, v113, v109
v_accvgpr_write_b32 a54, v116
v_accvgpr_read_b32 v118, a62
s_waitcnt lgkmcnt(6)
v_mfma_f32_32x32x16_bf16 a[128:143], v[64:67], v[0:3], a[128:143]
ds_read_b64_tr_b16 v[80:81], v104 offset:4096
ds_read_b64_tr_b16 v[82:83], v104 offset:6144
v_accvgpr_read_b32 v119, a63
v_mul_f32 v118, v118, v115
v_mul_f32 v119, v119, v115
v_cvt_pk_bf16_f32 v118, v118, v119
v_mfma_f32_32x32x16_bf16 a[144:159], v[64:67], v[16:19], a[144:159]
s_add_u32 m0, s63, 32768
s_add_u32 s66, s88, 0
buffer_load_dwordx4 v126, s[52:55], s66 offen offset:0 lds
v_accvgpr_write_b32 a55, v118
v_accvgpr_read_b32 v116, a64
s_waitcnt lgkmcnt(6)
v_mfma_f32_32x32x16_bf16 a[160:175], v[68:71], v[0:3], a[160:175]
ds_read_b64_tr_b16 v[84:85], v104 offset:4608
ds_read_b64_tr_b16 v[86:87], v104 offset:6656
v_exp_f32 v32, v32
v_exp_f32 v48, v48
v_mfma_f32_32x32x16_bf16 a[176:191], v[68:71], v[16:19], a[176:191]
v_exp_f32 v33, v33
v_exp_f32 v49, v49
v_accvgpr_read_b32 v117, a65
s_waitcnt lgkmcnt(6)
v_mfma_f32_32x32x16_bf16 a[192:207], v[72:75], v[0:3], a[192:207]
s_add_u32 m0, s63, 33792
s_add_u32 s66, s88, 8192
buffer_load_dwordx4 v126, s[52:55], s66 offen offset:0 lds
ds_read_b64_tr_b16 v[88:89], v104 offset:5120
ds_read_b64_tr_b16 v[90:91], v104 offset:7168
v_mul_f32 v116, v116, v114
v_mfma_f32_32x32x16_bf16 a[208:223], v[72:75], v[16:19], a[208:223]
v_exp_f32 v34, v34
v_exp_f32 v50, v50
v_add_f32 v108, v32, v33
s_waitcnt lgkmcnt(6)
v_mfma_f32_32x32x16_bf16 a[224:239], v[76:79], v[0:3], a[224:239]
ds_read_b64_tr_b16 v[92:93], v104 offset:5632
ds_read_b64_tr_b16 v[94:95], v104 offset:7680
v_add_u32 v96, 0x8000, v96
v_add_f32 v109, v48, v49
v_cvt_pk_bf16_f32 v32, v32, v33
v_cvt_pk_bf16_f32 v48, v48, v49
v_mfma_f32_32x32x16_bf16 a[240:255], v[76:79], v[16:19], a[240:255]
v_exp_f32 v35, v35
v_exp_f32 v51, v51
v_add_f32 v108, v108, v34
s_waitcnt lgkmcnt(6)
v_mfma_f32_32x32x16_bf16 a[128:143], v[80:83], v[8:11], a[128:143]
s_add_u32 m0, s63, 34816
s_add_u32 s66, s88, 16384
buffer_load_dwordx4 v126, s[52:55], s66 offen offset:0 lds
ds_read_b128 v[64:67], v96 offset:0
v_add_u32 v97, 0x8000, v97
v_add_f32 v109, v109, v50
v_mfma_f32_32x32x16_bf16 a[144:159], v[80:83], v[24:27], a[144:159]
v_exp_f32 v36, v36
v_exp_f32 v52, v52
v_add_f32 v108, v108, v35
s_waitcnt lgkmcnt(5)
v_mfma_f32_32x32x16_bf16 a[160:175], v[84:87], v[8:11], a[160:175]
ds_read_b128 v[68:71], v97 offset:0
v_add_u32 v98, 0x8000, v98
v_add_f32 v109, v109, v51
v_cvt_pk_bf16_f32 v33, v34, v35
v_cvt_pk_bf16_f32 v49, v50, v51
v_mul_f32 v117, v117, v114
v_mfma_f32_32x32x16_bf16 a[176:191], v[84:87], v[24:27], a[176:191]
s_add_u32 m0, s63, 35840
s_add_u32 s66, s88, 24576
buffer_load_dwordx4 v126, s[52:55], s66 offen offset:0 lds
v_exp_f32 v37, v37
s_waitcnt lgkmcnt(4)
v_mfma_f32_32x32x16_bf16 a[192:207], v[88:91], v[8:11], a[192:207]
ds_read_b128 v[72:75], v98 offset:0
v_add_u32 v99, 0x8000, v99
v_exp_f32 v53, v53
v_add_f32 v108, v108, v36
v_add_f32 v109, v109, v52
v_mfma_f32_32x32x16_bf16 a[208:223], v[88:91], v[24:27], a[208:223]
v_exp_f32 v38, v38
v_exp_f32 v54, v54
v_add_f32 v108, v108, v37
s_waitcnt lgkmcnt(3)
v_mfma_f32_32x32x16_bf16 a[224:239], v[92:95], v[8:11], a[224:239]
ds_read_b128 v[76:79], v99 offset:0
v_add_u32 v100, 0x8000, v100
v_add_f32 v109, v109, v53
v_cvt_pk_bf16_f32 v34, v36, v37
v_cvt_pk_bf16_f32 v50, v52, v53
v_cvt_pk_bf16_f32 v116, v116, v117
v_mfma_f32_32x32x16_bf16 a[240:255], v[92:95], v[24:27], a[240:255]
s_add_u32 m0, s63, 36864
s_add_u32 s66, s88, 32768
buffer_load_dwordx4 v126, s[52:55], s66 offen offset:0 lds
v_exp_f32 v39, v39
s_waitcnt lgkmcnt(3)
v_mfma_f32_32x32x16_bf16 v[0:15], v[64:67], v[192:195], v[128:143]
ds_read_b128 v[80:83], v100 offset:0
v_add_u32 v101, 0x8000, v101
v_exp_f32 v55, v55
v_add_f32 v108, v108, v38
v_add_f32 v109, v109, v54
v_mfma_f32_32x32x16_bf16 v[16:31], v[64:67], v[208:211], v[144:159]
v_exp_f32 v40, v40
v_exp_f32 v56, v56
v_add_f32 v108, v108, v39
s_waitcnt lgkmcnt(3)
v_mfma_f32_32x32x16_bf16 v[0:15], v[68:71], v[196:199], v[0:15]
s_add_u32 m0, s63, 37888
s_add_u32 s66, s88, 40960
buffer_load_dwordx4 v126, s[52:55], s66 offen offset:0 lds
ds_read_b128 v[84:87], v101 offset:0
v_add_u32 v102, 0x8000, v102
v_add_f32 v109, v109, v55
v_mfma_f32_32x32x16_bf16 v[16:31], v[68:71], v[212:215], v[16:31]
v_cvt_pk_bf16_f32 v35, v38, v39
v_cvt_pk_bf16_f32 v51, v54, v55
v_exp_f32 v41, v41
v_accvgpr_write_b32 a64, v116
s_waitcnt lgkmcnt(3)
v_mfma_f32_32x32x16_bf16 v[0:15], v[72:75], v[224:227], v[0:15]
ds_read_b128 v[88:91], v102 offset:0
v_add_u32 v103, 0x8000, v103
v_exp_f32 v57, v57
v_add_f32 v108, v108, v40
v_add_f32 v109, v109, v56
v_mfma_f32_32x32x16_bf16 v[16:31], v[72:75], v[240:243], v[16:31]
v_exp_f32 v42, v42
v_exp_f32 v58, v58
v_add_f32 v108, v108, v41
s_waitcnt lgkmcnt(3)
v_mfma_f32_32x32x16_bf16 v[0:15], v[76:79], v[228:231], v[0:15]
s_add_u32 m0, s63, 38912
s_add_u32 s66, s88, 49152
buffer_load_dwordx4 v126, s[52:55], s66 offen offset:0 lds
ds_read_b128 v[92:95], v103 offset:0
v_add_f32 v109, v109, v57
v_cvt_pk_bf16_f32 v40, v40, v41
v_mfma_f32_32x32x16_bf16 v[16:31], v[76:79], v[244:247], v[16:31]
v_cvt_pk_bf16_f32 v56, v56, v57
v_exp_f32 v43, v43
v_exp_f32 v59, v59
s_waitcnt lgkmcnt(3)
v_mfma_f32_32x32x16_bf16 v[0:15], v[80:83], v[200:203], v[0:15]
ds_read_b64_tr_b16 v[64:65], v104 offset:8192
ds_read_b64_tr_b16 v[66:67], v104 offset:10240
v_add_f32 v108, v108, v42
v_add_f32 v109, v109, v58
v_exp_f32 v44, v44
v_mfma_f32_32x32x16_bf16 v[16:31], v[80:83], v[216:219], v[16:31]
s_add_u32 m0, s63, 39936
s_add_u32 s66, s88, 57344
buffer_load_dwordx4 v126, s[52:55], s66 offen offset:0 lds
v_exp_f32 v60, v60
s_waitcnt lgkmcnt(4)
v_mfma_f32_32x32x16_bf16 v[0:15], v[84:87], v[204:207], v[0:15]
ds_read_b64_tr_b16 v[68:69], v104 offset:8704
ds_read_b64_tr_b16 v[70:71], v104 offset:10752
v_add_f32 v108, v108, v43
v_add_f32 v109, v109, v59
v_cvt_pk_bf16_f32 v41, v42, v43
v_cvt_pk_bf16_f32 v57, v58, v59
v_mfma_f32_32x32x16_bf16 v[16:31], v[84:87], v[220:223], v[16:31]
v_exp_f32 v45, v45
v_exp_f32 v61, v61
v_add_f32 v108, v108, v44
s_waitcnt lgkmcnt(5)
v_mfma_f32_32x32x16_bf16 v[0:15], v[88:91], v[232:235], v[0:15]
ds_read_b64_tr_b16 v[72:73], v104 offset:9216
ds_read_b64_tr_b16 v[74:75], v104 offset:11264
v_add_f32 v109, v109, v60
v_exp_f32 v46, v46
v_accvgpr_read_b32 v118, a66
v_mfma_f32_32x32x16_bf16 v[16:31], v[88:91], v[248:251], v[16:31]
v_exp_f32 v62, v62
v_add_f32 v108, v108, v45
v_add_f32 v109, v109, v61
v_cvt_pk_bf16_f32 v42, v44, v45
s_waitcnt lgkmcnt(6)
v_mfma_f32_32x32x16_bf16 v[0:15], v[92:95], v[236:239], v[0:15]
ds_read_b64_tr_b16 v[76:77], v104 offset:9728
ds_read_b64_tr_b16 v[78:79], v104 offset:11776
v_cvt_pk_bf16_f32 v58, v60, v61
v_exp_f32 v47, v47
v_accvgpr_read_b32 v119, a67
v_mfma_f32_32x32x16_bf16 v[16:31], v[92:95], v[252:255], v[16:31]
v_exp_f32 v63, v63
v_add_f32 v108, v108, v46
v_add_f32 v109, v109, v62
v_add_f32 v108, v108, v47
v_add_f32 v109, v109, v63
v_cvt_pk_bf16_f32 v43, v46, v47
v_cvt_pk_bf16_f32 v59, v62, v63
v_max_f32 v160, v108, v109
v_cmp_nge_f32 vcc, 0x45800000, v160
s_cbranch_vccnz L_tr_25
L_ret_25:
v_add_f32 v112, v112, v108
v_add_f32 v113, v113, v109
s_waitcnt lgkmcnt(6)
v_mfma_f32_32x32x16_bf16 a[128:143], v[64:67], v[32:35], a[128:143]
ds_read_b64_tr_b16 v[80:81], v104 offset:12288
ds_read_b64_tr_b16 v[82:83], v104 offset:14336
v_mul_f32 v118, v118, v114
v_mul_f32 v119, v119, v114
v_cvt_pk_bf16_f32 v118, v118, v119
v_accvgpr_write_b32 a65, v118
v_mfma_f32_32x32x16_bf16 a[144:159], v[64:67], v[48:51], a[144:159]
v_accvgpr_read_b32 v116, a68
v_accvgpr_read_b32 v117, a69
v_mul_f32 v116, v116, v114
v_mul_f32 v117, v117, v114
v_cvt_pk_bf16_f32 v116, v116, v117
s_waitcnt lgkmcnt(6)
v_mfma_f32_32x32x16_bf16 a[160:175], v[68:71], v[32:35], a[160:175]
ds_read_b64_tr_b16 v[84:85], v104 offset:12800
ds_read_b64_tr_b16 v[86:87], v104 offset:14848
v_exp_f32 v0, v0
v_exp_f32 v16, v16
v_mfma_f32_32x32x16_bf16 a[176:191], v[68:71], v[48:51], a[176:191]
v_exp_f32 v1, v1
v_exp_f32 v17, v17
v_accvgpr_write_b32 a66, v116
s_waitcnt lgkmcnt(6)
v_mfma_f32_32x32x16_bf16 a[192:207], v[72:75], v[32:35], a[192:207]
ds_read_b64_tr_b16 v[88:89], v104 offset:13312
ds_read_b64_tr_b16 v[90:91], v104 offset:15360
v_exp_f32 v2, v2
v_exp_f32 v18, v18
v_mfma_f32_32x32x16_bf16 a[208:223], v[72:75], v[48:51], a[208:223]
v_add_f32 v108, v0, v1
v_add_f32 v109, v16, v17
v_cvt_pk_bf16_f32 v0, v0, v1
v_cvt_pk_bf16_f32 v16, v16, v17
v_accvgpr_read_b32 v118, a70
s_waitcnt lgkmcnt(6)
v_mfma_f32_32x32x16_bf16 a[224:239], v[76:79], v[32:35], a[224:239]
ds_read_b64_tr_b16 v[92:93], v104 offset:13824
ds_read_b64_tr_b16 v[94:95], v104 offset:15872
v_exp_f32 v3, v3
v_exp_f32 v19, v19
v_mfma_f32_32x32x16_bf16 a[240:255], v[76:79], v[48:51], a[240:255]
v_add_f32 v108, v108, v2
v_add_f32 v109, v109, v18
v_exp_f32 v4, v4
v_accvgpr_read_b32 v119, a71
s_waitcnt lgkmcnt(6)
v_mfma_f32_32x32x16_bf16 a[128:143], v[80:83], v[40:43], a[128:143]
ds_read_b128 v[64:67], v96 offset:8192
v_exp_f32 v20, v20
v_add_f32 v108, v108, v3
v_add_f32 v109, v109, v19
v_cvt_pk_bf16_f32 v1, v2, v3
v_mfma_f32_32x32x16_bf16 a[144:159], v[80:83], v[56:59], a[144:159]
v_cvt_pk_bf16_f32 v17, v18, v19
v_exp_f32 v5, v5
v_exp_f32 v21, v21
s_waitcnt lgkmcnt(5)
v_mfma_f32_32x32x16_bf16 a[160:175], v[84:87], v[40:43], a[160:175]
ds_read_b128 v[68:71], v97 offset:8192
v_add_f32 v108, v108, v4
v_add_f32 v109, v109, v20
v_exp_f32 v6, v6
v_mul_f32 v118, v118, v114
v_mfma_f32_32x32x16_bf16 a[176:191], v[84:87], v[56:59], a[176:191]
v_exp_f32 v22, v22
v_add_f32 v108, v108, v5
v_add_f32 v109, v109, v21
v_cvt_pk_bf16_f32 v2, v4, v5
s_waitcnt lgkmcnt(4)
v_mfma_f32_32x32x16_bf16 a[192:207], v[88:91], v[40:43], a[192:207]
ds_read_b128 v[72:75], v98 offset:8192
v_cvt_pk_bf16_f32 v18, v20, v21
v_exp_f32 v7, v7
v_exp_f32 v23, v23
v_mfma_f32_32x32x16_bf16 a[208:223], v[88:91], v[56:59], a[208:223]
v_add_f32 v108, v108, v6
v_add_f32 v109, v109, v22
v_exp_f32 v8, v8
v_mul_f32 v119, v119, v114
s_waitcnt lgkmcnt(3)
v_mfma_f32_32x32x16_bf16 a[224:239], v[92:95], v[40:43], a[224:239]
ds_read_b128 v[76:79], v99 offset:8192
v_exp_f32 v24, v24
v_add_f32 v108, v108, v7
v_add_f32 v109, v109, v23
v_cvt_pk_bf16_f32 v3, v6, v7
v_mfma_f32_32x32x16_bf16 a[240:255], v[92:95], v[56:59], a[240:255]
v_cvt_pk_bf16_f32 v19, v22, v23
v_exp_f32 v9, v9
v_exp_f32 v25, v25
s_waitcnt lgkmcnt(3)
v_mfma_f32_32x32x16_bf16 v[32:47], v[64:67], v[192:195], v[128:143]
ds_read_b128 v[80:83], v100 offset:8192
v_add_f32 v108, v108, v8
v_add_f32 v109, v109, v24
v_exp_f32 v10, v10
v_cvt_pk_bf16_f32 v118, v118, v119
v_mfma_f32_32x32x16_bf16 v[48:63], v[64:67], v[208:211], v[144:159]
v_exp_f32 v26, v26
v_add_f32 v108, v108, v9
v_add_f32 v109, v109, v25
v_cvt_pk_bf16_f32 v8, v8, v9
s_waitcnt lgkmcnt(3)
v_mfma_f32_32x32x16_bf16 v[32:47], v[68:71], v[196:199], v[32:47]
ds_read_b128 v[84:87], v101 offset:8192
v_cvt_pk_bf16_f32 v24, v24, v25
v_exp_f32 v11, v11
v_exp_f32 v27, v27
v_mfma_f32_32x32x16_bf16 v[48:63], v[68:71], v[212:215], v[48:63]
v_add_f32 v108, v108, v10
v_add_f32 v109, v109, v26
v_exp_f32 v12, v12
v_accvgpr_write_b32 a67, v118
s_waitcnt lgkmcnt(3)
v_mfma_f32_32x32x16_bf16 v[32:47], v[72:75], v[224:227], v[32:47]
ds_read_b128 v[88:91], v102 offset:8192
v_exp_f32 v28, v28
v_add_f32 v108, v108, v11
v_add_f32 v109, v109, v27
v_cvt_pk_bf16_f32 v9, v10, v11
v_mfma_f32_32x32x16_bf16 v[48:63], v[72:75], v[240:243], v[48:63]
v_cvt_pk_bf16_f32 v25, v26, v27
v_exp_f32 v13, v13
v_exp_f32 v29, v29
s_waitcnt lgkmcnt(3)
v_mfma_f32_32x32x16_bf16 v[32:47], v[76:79], v[228:231], v[32:47]
ds_read_b128 v[92:95], v103 offset:8192
v_add_u32 v104, 0x8000, v104
v_add_f32 v108, v108, v12
v_add_f32 v109, v109, v28
v_exp_f32 v14, v14
v_mfma_f32_32x32x16_bf16 v[48:63], v[76:79], v[244:247], v[48:63]
v_exp_f32 v30, v30
v_add_f32 v108, v108, v13
v_add_f32 v109, v109, v29
v_cvt_pk_bf16_f32 v10, v12, v13
s_waitcnt lgkmcnt(3)
v_mfma_f32_32x32x16_bf16 v[32:47], v[80:83], v[200:203], v[32:47]
ds_read_b64_tr_b16 v[64:65], v104 offset:0
ds_read_b64_tr_b16 v[66:67], v104 offset:2048
v_cvt_pk_bf16_f32 v26, v28, v29
v_exp_f32 v15, v15
v_accvgpr_read_b32 v116, a72
v_mfma_f32_32x32x16_bf16 v[48:63], v[80:83], v[216:219], v[48:63]
v_exp_f32 v31, v31
v_add_f32 v108, v108, v14
v_add_f32 v109, v109, v30
v_add_f32 v108, v108, v15
s_waitcnt lgkmcnt(4)
v_mfma_f32_32x32x16_bf16 v[32:47], v[84:87], v[204:207], v[32:47]
ds_read_b64_tr_b16 v[68:69], v104 offset:512
ds_read_b64_tr_b16 v[70:71], v104 offset:2560
v_add_f32 v109, v109, v31
v_cvt_pk_bf16_f32 v11, v14, v15
v_cvt_pk_bf16_f32 v27, v30, v31
v_max_f32 v160, v108, v109
v_mfma_f32_32x32x16_bf16 v[48:63], v[84:87], v[220:223], v[48:63]
v_cmp_nge_f32 vcc, 0x45800000, v160
v_accvgpr_read_b32 v117, a73
v_mul_f32 v116, v116, v114
v_mul_f32 v117, v117, v114
v_cvt_pk_bf16_f32 v116, v116, v117
s_waitcnt lgkmcnt(5)
v_mfma_f32_32x32x16_bf16 v[32:47], v[88:91], v[232:235], v[32:47]
s_waitcnt vmcnt(18)
s_barrier
ds_read_b64_tr_b16 v[72:73], v104 offset:1024
ds_read_b64_tr_b16 v[74:75], v104 offset:3072
v_accvgpr_write_b32 a68, v116
v_accvgpr_read_b32 v118, a74
v_mfma_f32_32x32x16_bf16 v[48:63], v[88:91], v[248:251], v[48:63]
v_accvgpr_read_b32 v119, a75
v_mul_f32 v118, v118, v114
v_mul_f32 v119, v119, v114
v_cvt_pk_bf16_f32 v118, v118, v119
v_accvgpr_write_b32 a69, v118
s_waitcnt lgkmcnt(6)
v_mfma_f32_32x32x16_bf16 v[32:47], v[92:95], v[236:239], v[32:47]
ds_read_b64_tr_b16 v[76:77], v104 offset:1536
ds_read_b64_tr_b16 v[78:79], v104 offset:3584
v_accvgpr_read_b32 v116, a76
v_accvgpr_read_b32 v117, a77
v_mul_f32 v116, v116, v114
v_mul_f32 v117, v117, v114
v_mfma_f32_32x32x16_bf16 v[48:63], v[92:95], v[252:255], v[48:63]
s_cbranch_vccnz L_tr_26
L_ret_26:
v_add_f32 v112, v112, v108
v_add_f32 v113, v113, v109
v_cvt_pk_bf16_f32 v116, v116, v117
v_accvgpr_write_b32 a70, v116
s_waitcnt lgkmcnt(6)
v_mfma_f32_32x32x16_bf16 a[128:143], v[64:67], v[0:3], a[128:143]
ds_read_b64_tr_b16 v[80:81], v104 offset:4096
ds_read_b64_tr_b16 v[82:83], v104 offset:6144
v_accvgpr_read_b32 v118, a78
v_accvgpr_read_b32 v119, a79
v_mul_f32 v118, v118, v114
v_mul_f32 v119, v119, v114
v_mfma_f32_32x32x16_bf16 a[144:159], v[64:67], v[16:19], a[144:159]
v_cvt_pk_bf16_f32 v118, v118, v119
v_accvgpr_write_b32 a71, v118
v_accvgpr_read_b32 v116, a80
v_accvgpr_read_b32 v117, a81
v_mul_f32 v116, v116, v115
s_waitcnt lgkmcnt(6)
v_mfma_f32_32x32x16_bf16 a[160:175], v[68:71], v[0:3], a[160:175]
ds_read_b64_tr_b16 v[84:85], v104 offset:4608
ds_read_b64_tr_b16 v[86:87], v104 offset:6656
v_exp_f32 v32, v32
v_exp_f32 v48, v48
v_mfma_f32_32x32x16_bf16 a[176:191], v[68:71], v[16:19], a[176:191]
v_exp_f32 v33, v33
v_exp_f32 v49, v49
v_mul_f32 v117, v117, v115
s_waitcnt lgkmcnt(6)
v_mfma_f32_32x32x16_bf16 a[192:207], v[72:75], v[0:3], a[192:207]
ds_read_b64_tr_b16 v[88:89], v104 offset:5120
ds_read_b64_tr_b16 v[90:91], v104 offset:7168
v_exp_f32 v34, v34
v_exp_f32 v50, v50
v_mfma_f32_32x32x16_bf16 a[208:223], v[72:75], v[16:19], a[208:223]
v_add_f32 v108, v32, v33
v_add_f32 v109, v48, v49
v_cvt_pk_bf16_f32 v32, v32, v33
v_cvt_pk_bf16_f32 v48, v48, v49
v_cvt_pk_bf16_f32 v116, v116, v117
s_waitcnt lgkmcnt(6)
v_mfma_f32_32x32x16_bf16 a[224:239], v[76:79], v[0:3], a[224:239]
ds_read_b64_tr_b16 v[92:93], v104 offset:5632
ds_read_b64_tr_b16 v[94:95], v104 offset:7680
v_add_u32 v96, 0x8000, v96
v_exp_f32 v35, v35
v_accvgpr_write_b32 a80, v116
v_mfma_f32_32x32x16_bf16 a[240:255], v[76:79], v[16:19], a[240:255]
v_exp_f32 v51, v51
v_add_f32 v108, v108, v34
v_add_f32 v109, v109, v50
v_accvgpr_read_b32 v118, a82
s_waitcnt lgkmcnt(6)
v_mfma_f32_32x32x16_bf16 a[128:143], v[80:83], v[8:11], a[128:143]
ds_read_b128 v[64:67], v96 offset:0
v_add_u32 v97, 0x8000, v97
v_exp_f32 v36, v36
v_exp_f32 v52, v52
v_mfma_f32_32x32x16_bf16 a[144:159], v[80:83], v[24:27], a[144:159]
v_add_f32 v108, v108, v35
v_add_f32 v109, v109, v51
v_cvt_pk_bf16_f32 v33, v34, v35
v_cvt_pk_bf16_f32 v49, v50, v51
v_accvgpr_read_b32 v119, a83
s_waitcnt lgkmcnt(5)
v_mfma_f32_32x32x16_bf16 a[160:175], v[84:87], v[8:11], a[160:175]
ds_read_b128 v[68:71], v97 offset:0
v_add_u32 v98, 0x8000, v98
v_exp_f32 v37, v37
v_exp_f32 v53, v53
v_mfma_f32_32x32x16_bf16 a[176:191], v[84:87], v[24:27], a[176:191]
v_add_f32 v108, v108, v36
v_add_f32 v109, v109, v52
v_exp_f32 v38, v38
v_mul_f32 v118, v118, v115
s_waitcnt lgkmcnt(4)
v_mfma_f32_32x32x16_bf16 a[192:207], v[88:91], v[8:11], a[192:207]
ds_read_b128 v[72:75], v98 offset:0
v_add_u32 v99, 0x8000, v99
v_exp_f32 v54, v54
v_add_f32 v108, v108, v37
v_add_f32 v109, v109, v53
v_mfma_f32_32x32x16_bf16 a[208:223], v[88:91], v[24:27], a[208:223]
v_cvt_pk_bf16_f32 v34, v36, v37
v_cvt_pk_bf16_f32 v50, v52, v53
v_exp_f32 v39, v39
v_mul_f32 v119, v119, v115
s_waitcnt lgkmcnt(3)
v_mfma_f32_32x32x16_bf16 a[224:239], v[92:95], v[8:11], a[224:239]
ds_read_b128 v[76:79], v99 offset:0
v_add_u32 v100, 0x8000, v100
v_exp_f32 v55, v55
v_add_f32 v108, v108, v38
v_add_f32 v109, v109, v54
v_mfma_f32_32x32x16_bf16 a[240:255], v[92:95], v[24:27], a[240:255]
v_exp_f32 v40, v40
v_exp_f32 v56, v56
v_add_f32 v108, v108, v39
s_waitcnt lgkmcnt(3)
v_mfma_f32_32x32x16_bf16 v[0:15], v[64:67], v[192:195], v[128:143]
ds_read_b128 v[80:83], v100 offset:0
v_add_u32 v101, 0x8000, v101
v_add_f32 v109, v109, v55
v_cvt_pk_bf16_f32 v35, v38, v39
v_cvt_pk_bf16_f32 v51, v54, v55
v_cvt_pk_bf16_f32 v118, v118, v119
v_mfma_f32_32x32x16_bf16 v[16:31], v[64:67], v[208:211], v[144:159]
v_exp_f32 v41, v41
v_exp_f32 v57, v57
v_add_f32 v108, v108, v40
s_waitcnt lgkmcnt(3)
v_mfma_f32_32x32x16_bf16 v[0:15], v[68:71], v[196:199], v[0:15]
ds_read_b128 v[84:87], v101 offset:0
v_add_u32 v102, 0x8000, v102
v_add_f32 v109, v109, v56
v_exp_f32 v42, v42
v_accvgpr_write_b32 a81, v118
v_mfma_f32_32x32x16_bf16 v[16:31], v[68:71], v[212:215], v[16:31]
v_exp_f32 v58, v58
v_add_f32 v108, v108, v41
v_add_f32 v109, v109, v57
v_cvt_pk_bf16_f32 v40, v40, v41
s_waitcnt lgkmcnt(3)
v_mfma_f32_32x32x16_bf16 v[0:15], v[72:75], v[224:227], v[0:15]
ds_read_b128 v[88:91], v102 offset:0
v_add_u32 v103, 0x8000, v103
v_cvt_pk_bf16_f32 v56, v56, v57
v_exp_f32 v43, v43
v_accvgpr_read_b32 v116, a84
v_mfma_f32_32x32x16_bf16 v[16:31], v[72:75], v[240:243], v[16:31]
v_exp_f32 v59, v59
v_add_f32 v108, v108, v42
v_add_f32 v109, v109, v58
v_accvgpr_read_b32 v117, a85
s_waitcnt lgkmcnt(3)
v_mfma_f32_32x32x16_bf16 v[0:15], v[76:79], v[228:231], v[0:15]
ds_read_b128 v[92:95], v103 offset:0
v_exp_f32 v44, v44
v_exp_f32 v60, v60
v_add_f32 v108, v108, v43
v_mfma_f32_32x32x16_bf16 v[16:31], v[76:79], v[244:247], v[16:31]
v_add_f32 v109, v109, v59
v_cvt_pk_bf16_f32 v41, v42, v43
v_cvt_pk_bf16_f32 v57, v58, v59
v_exp_f32 v45, v45
s_waitcnt lgkmcnt(3)
v_mfma_f32_32x32x16_bf16 v[0:15], v[80:83], v[200:203], v[0:15]
ds_read_b64_tr_b16 v[64:65], v104 offset:8192
ds_read_b64_tr_b16 v[66:67], v104 offset:10240
v_exp_f32 v61, v61
v_add_f32 v108, v108, v44
v_add_f32 v109, v109, v60
v_mfma_f32_32x32x16_bf16 v[16:31], v[80:83], v[216:219], v[16:31]
v_exp_f32 v46, v46
v_exp_f32 v62, v62
v_add_f32 v108, v108, v45
s_waitcnt lgkmcnt(4)
v_mfma_f32_32x32x16_bf16 v[0:15], v[84:87], v[204:207], v[0:15]
ds_read_b64_tr_b16 v[68:69], v104 offset:8704
ds_read_b64_tr_b16 v[70:71], v104 offset:10752
v_add_f32 v109, v109, v61
v_cvt_pk_bf16_f32 v42, v44, v45
v_cvt_pk_bf16_f32 v58, v60, v61
v_mul_f32 v116, v116, v115
v_mfma_f32_32x32x16_bf16 v[16:31], v[84:87], v[220:223], v[16:31]
v_exp_f32 v47, v47
v_exp_f32 v63, v63
v_add_f32 v108, v108, v46
s_waitcnt lgkmcnt(5)
v_mfma_f32_32x32x16_bf16 v[0:15], v[88:91], v[232:235], v[0:15]
ds_read_b64_tr_b16 v[72:73], v104 offset:9216
ds_read_b64_tr_b16 v[74:75], v104 offset:11264
v_add_f32 v109, v109, v62
v_add_f32 v108, v108, v47
v_add_f32 v109, v109, v63
v_cvt_pk_bf16_f32 v43, v46, v47
v_mfma_f32_32x32x16_bf16 v[16:31], v[88:91], v[248:251], v[16:31]
v_cvt_pk_bf16_f32 v59, v62, v63
v_max_f32 v160, v108, v109
v_cmp_nge_f32 vcc, 0x45800000, v160
v_mul_f32 v117, v117, v115
v_cvt_pk_bf16_f32 v116, v116, v117
s_waitcnt lgkmcnt(6)
v_mfma_f32_32x32x16_bf16 v[0:15], v[92:95], v[236:239], v[0:15]
ds_read_b64_tr_b16 v[76:77], v104 offset:9728
ds_read_b64_tr_b16 v[78:79], v104 offset:11776
v_accvgpr_write_b32 a82, v116
v_accvgpr_read_b32 v118, a86
v_accvgpr_read_b32 v119, a87
v_mul_f32 v118, v118, v115
v_mfma_f32_32x32x16_bf16 v[16:31], v[92:95], v[252:255], v[16:31]
s_cbranch_vccnz L_tr_27
L_ret_27:
v_add_f32 v112, v112, v108
v_add_f32 v113, v113, v109
v_mul_f32 v119, v119, v115
v_cvt_pk_bf16_f32 v118, v118, v119
s_waitcnt lgkmcnt(6)
v_mfma_f32_32x32x16_bf16 a[128:143], v[64:67], v[32:35], a[128:143]
ds_read_b64_tr_b16 v[80:81], v104 offset:12288
ds_read_b64_tr_b16 v[82:83], v104 offset:14336
v_accvgpr_write_b32 a83, v118
v_accvgpr_read_b32 v116, a88
v_accvgpr_read_b32 v117, a89
v_mul_f32 v116, v116, v115
v_mfma_f32_32x32x16_bf16 a[144:159], v[64:67], v[48:51], a[144:159]
v_mul_f32 v117, v117, v115
v_cvt_pk_bf16_f32 v116, v116, v117
v_accvgpr_write_b32 a84, v116
v_accvgpr_read_b32 v118, a90
v_accvgpr_read_b32 v119, a91
s_waitcnt lgkmcnt(6)
v_mfma_f32_32x32x16_bf16 a[160:175], v[68:71], v[32:35], a[160:175]
ds_read_b64_tr_b16 v[84:85], v104 offset:12800
ds_read_b64_tr_b16 v[86:87], v104 offset:14848
v_exp_f32 v0, v0
v_exp_f32 v16, v16
v_mfma_f32_32x32x16_bf16 a[176:191], v[68:71], v[48:51], a[176:191]
v_exp_f32 v1, v1
v_exp_f32 v17, v17
v_mul_f32 v118, v118, v115
s_waitcnt lgkmcnt(6)
v_mfma_f32_32x32x16_bf16 a[192:207], v[72:75], v[32:35], a[192:207]
ds_read_b64_tr_b16 v[88:89], v104 offset:13312
ds_read_b64_tr_b16 v[90:91], v104 offset:15360
v_exp_f32 v2, v2
v_exp_f32 v18, v18
v_mfma_f32_32x32x16_bf16 a[208:223], v[72:75], v[48:51], a[208:223]
v_add_f32 v108, v0, v1
v_add_f32 v109, v16, v17
v_cvt_pk_bf16_f32 v0, v0, v1
v_cvt_pk_bf16_f32 v16, v16, v17
v_mul_f32 v119, v119, v115
s_waitcnt lgkmcnt(6)
v_mfma_f32_32x32x16_bf16 a[224:239], v[76:79], v[32:35], a[224:239]
ds_read_b64_tr_b16 v[92:93], v104 offset:13824
ds_read_b64_tr_b16 v[94:95], v104 offset:15872
v_exp_f32 v3, v3
v_exp_f32 v19, v19
v_mfma_f32_32x32x16_bf16 a[240:255], v[76:79], v[48:51], a[240:255]
v_add_f32 v108, v108, v2
v_add_f32 v109, v109, v18
v_exp_f32 v4, v4
v_cvt_pk_bf16_f32 v118, v118, v119
s_waitcnt lgkmcnt(6)
v_mfma_f32_32x32x16_bf16 a[128:143], v[80:83], v[40:43], a[128:143]
ds_read_b128 v[64:67], v96 offset:8192
v_exp_f32 v20, v20
v_add_f32 v108, v108, v3
v_add_f32 v109, v109, v19
v_cvt_pk_bf16_f32 v1, v2, v3
v_mfma_f32_32x32x16_bf16 a[144:159], v[80:83], v[56:59], a[144:159]
v_cvt_pk_bf16_f32 v17, v18, v19
v_exp_f32 v5, v5
v_exp_f32 v21, v21
s_waitcnt lgkmcnt(5)
v_mfma_f32_32x32x16_bf16 a[160:175], v[84:87], v[40:43], a[160:175]
ds_read_b128 v[68:71], v97 offset:8192
v_add_f32 v108, v108, v4
v_add_f32 v109, v109, v20
v_exp_f32 v6, v6
v_accvgpr_write_b32 a85, v118
v_mfma_f32_32x32x16_bf16 a[176:191], v[84:87], v[56:59], a[176:191]
v_exp_f32 v22, v22
v_add_f32 v108, v108, v5
v_add_f32 v109, v109, v21
v_cvt_pk_bf16_f32 v2, v4, v5
s_waitcnt lgkmcnt(4)
v_mfma_f32_32x32x16_bf16 a[192:207], v[88:91], v[40:43], a[192:207]
ds_read_b128 v[72:75], v98 offset:8192
v_cvt_pk_bf16_f32 v18, v20, v21
v_exp_f32 v7, v7
v_exp_f32 v23, v23
v_mfma_f32_32x32x16_bf16 a[208:223], v[88:91], v[56:59], a[208:223]
v_add_f32 v108, v108, v6
v_add_f32 v109, v109, v22
v_exp_f32 v8, v8
v_accvgpr_read_b32 v116, a92
s_waitcnt lgkmcnt(3)
v_mfma_f32_32x32x16_bf16 a[224:239], v[92:95], v[40:43], a[224:239]
ds_read_b128 v[76:79], v99 offset:8192
v_exp_f32 v24, v24
v_add_f32 v108, v108, v7
v_add_f32 v109, v109, v23
v_cvt_pk_bf16_f32 v3, v6, v7
v_mfma_f32_32x32x16_bf16 a[240:255], v[92:95], v[56:59], a[240:255]
v_cvt_pk_bf16_f32 v19, v22, v23
v_exp_f32 v9, v9
v_exp_f32 v25, v25
s_waitcnt lgkmcnt(3)
v_mfma_f32_32x32x16_bf16 v[32:47], v[64:67], v[192:195], v[128:143]
ds_read_b128 v[80:83], v100 offset:8192
v_add_f32 v108, v108, v8
v_add_f32 v109, v109, v24
v_exp_f32 v10, v10
v_accvgpr_read_b32 v117, a93
v_mfma_f32_32x32x16_bf16 v[48:63], v[64:67], v[208:211], v[144:159]
v_exp_f32 v26, v26
v_add_f32 v108, v108, v9
v_add_f32 v109, v109, v25
v_cvt_pk_bf16_f32 v8, v8, v9
s_waitcnt lgkmcnt(3)
v_mfma_f32_32x32x16_bf16 v[32:47], v[68:71], v[196:199], v[32:47]
ds_read_b128 v[84:87], v101 offset:8192
v_cvt_pk_bf16_f32 v24, v24, v25
v_exp_f32 v11, v11
v_exp_f32 v27, v27
v_mfma_f32_32x32x16_bf16 v[48:63], v[68:71], v[212:215], v[48:63]
v_add_f32 v108, v108, v10
v_add_f32 v109, v109, v26
v_exp_f32 v12, v12
v_mul_f32 v116, v116, v115
s_waitcnt lgkmcnt(3)
v_mfma_f32_32x32x16_bf16 v[32:47], v[72:75], v[224:227], v[32:47]
ds_read_b128 v[88:91], v102 offset:8192
v_exp_f32 v28, v28
v_add_f32 v108, v108, v11
v_add_f32 v109, v109, v27
v_cvt_pk_bf16_f32 v9, v10, v11
v_mfma_f32_32x32x16_bf16 v[48:63], v[72:75], v[240:243], v[48:63]
v_cvt_pk_bf16_f32 v25, v26, v27
v_exp_f32 v13, v13
v_exp_f32 v29, v29
s_waitcnt lgkmcnt(3)
v_mfma_f32_32x32x16_bf16 v[32:47], v[76:79], v[228:231], v[32:47]
ds_read_b128 v[92:95], v103 offset:8192
v_add_u32 v104, 0x8000, v104
v_add_f32 v108, v108, v12
v_add_f32 v109, v109, v28
v_exp_f32 v14, v14
v_mfma_f32_32x32x16_bf16 v[48:63], v[76:79], v[244:247], v[48:63]
v_exp_f32 v30, v30
v_add_f32 v108, v108, v13
v_add_f32 v109, v109, v29
v_cvt_pk_bf16_f32 v10, v12, v13
s_waitcnt lgkmcnt(3)
v_mfma_f32_32x32x16_bf16 v[32:47], v[80:83], v[200:203], v[32:47]
ds_read_b64_tr_b16 v[64:65], v104 offset:0
ds_read_b64_tr_b16 v[66:67], v104 offset:2048
v_cvt_pk_bf16_f32 v26, v28, v29
v_exp_f32 v15, v15
v_mul_f32 v117, v117, v115
v_mfma_f32_32x32x16_bf16 v[48:63], v[80:83], v[216:219], v[48:63]
v_exp_f32 v31, v31
v_add_f32 v108, v108, v14
v_add_f32 v109, v109, v30
v_add_f32 v108, v108, v15
s_waitcnt lgkmcnt(4)
v_mfma_f32_32x32x16_bf16 v[32:47], v[84:87], v[204:207], v[32:47]
ds_read_b64_tr_b16 v[68:69], v104 offset:512
ds_read_b64_tr_b16 v[70:71], v104 offset:2560
v_add_f32 v109, v109, v31
v_cvt_pk_bf16_f32 v11, v14, v15
v_cvt_pk_bf16_f32 v27, v30, v31
v_max_f32 v160, v108, v109
v_mfma_f32_32x32x16_bf16 v[48:63], v[84:87], v[220:223], v[48:63]
v_cmp_nge_f32 vcc, 0x45800000, v160
v_cvt_pk_bf16_f32 v116, v116, v117
v_accvgpr_write_b32 a86, v116
v_accvgpr_read_b32 v118, a94
v_accvgpr_read_b32 v119, a95
s_waitcnt lgkmcnt(5)
v_mfma_f32_32x32x16_bf16 v[32:47], v[88:91], v[232:235], v[32:47]
s_waitcnt vmcnt(10)
s_barrier
ds_read_b64_tr_b16 v[72:73], v104 offset:1024
ds_read_b64_tr_b16 v[74:75], v104 offset:3072
v_mul_f32 v118, v118, v115
v_mul_f32 v119, v119, v115
v_mfma_f32_32x32x16_bf16 v[48:63], v[88:91], v[248:251], v[48:63]
v_cvt_pk_bf16_f32 v118, v118, v119
v_accvgpr_write_b32 a87, v118
v_accvgpr_read_b32 v116, a96
v_accvgpr_read_b32 v117, a97
v_mul_f32 v116, v116, v114
s_waitcnt lgkmcnt(6)
v_mfma_f32_32x32x16_bf16 v[32:47], v[92:95], v[236:239], v[32:47]
ds_read_b64_tr_b16 v[76:77], v104 offset:1536
ds_read_b64_tr_b16 v[78:79], v104 offset:3584
v_mul_f32 v117, v117, v114
v_cvt_pk_bf16_f32 v116, v116, v117
v_accvgpr_write_b32 a96, v116
v_accvgpr_read_b32 v118, a98
v_mfma_f32_32x32x16_bf16 v[48:63], v[92:95], v[252:255], v[48:63]
s_cbranch_vccnz L_tr_28
L_ret_28:
v_add_f32 v112, v112, v108
v_add_f32 v113, v113, v109
v_accvgpr_read_b32 v119, a99
v_mul_f32 v118, v118, v114
s_waitcnt lgkmcnt(6)
v_mfma_f32_32x32x16_bf16 a[128:143], v[64:67], v[0:3], a[128:143]
ds_read_b64_tr_b16 v[80:81], v104 offset:4096
ds_read_b64_tr_b16 v[82:83], v104 offset:6144
v_mul_f32 v119, v119, v114
v_cvt_pk_bf16_f32 v118, v118, v119
v_accvgpr_write_b32 a97, v118
v_accvgpr_read_b32 v116, a100
v_mfma_f32_32x32x16_bf16 a[144:159], v[64:67], v[16:19], a[144:159]
s_add_u32 m0, s63, 98304
s_add_u32 s66, s88, 128
buffer_load_dwordx4 v126, s[52:55], s66 offen offset:0 lds
v_accvgpr_read_b32 v117, a101
v_mul_f32 v116, v116, v114
s_waitcnt lgkmcnt(6)
v_mfma_f32_32x32x16_bf16 a[160:175], v[68:71], v[0:3], a[160:175]
ds_read_b64_tr_b16 v[84:85], v104 offset:4608
ds_read_b64_tr_b16 v[86:87], v104 offset:6656
v_exp_f32 v32, v32
v_exp_f32 v48, v48
v_mfma_f32_32x32x16_bf16 a[176:191], v[68:71], v[16:19], a[176:191]
v_exp_f32 v33, v33
v_exp_f32 v49, v49
v_mul_f32 v117, v117, v114
s_waitcnt lgkmcnt(6)
v_mfma_f32_32x32x16_bf16 a[192:207], v[72:75], v[0:3], a[192:207]
s_add_u32 m0, s63, 99328
s_add_u32 s66, s88, 8320
buffer_load_dwordx4 v126, s[52:55], s66 offen offset:0 lds
ds_read_b64_tr_b16 v[88:89], v104 offset:5120
ds_read_b64_tr_b16 v[90:91], v104 offset:7168
v_cvt_pk_bf16_f32 v116, v116, v117
v_mfma_f32_32x32x16_bf16 a[208:223], v[72:75], v[16:19], a[208:223]
v_exp_f32 v34, v34
v_exp_f32 v50, v50
v_add_f32 v108, v32, v33
s_waitcnt lgkmcnt(6)
v_mfma_f32_32x32x16_bf16 a[224:239], v[76:79], v[0:3], a[224:239]
ds_read_b64_tr_b16 v[92:93], v104 offset:5632
ds_read_b64_tr_b16 v[94:95], v104 offset:7680
v_add_u32 v96, 0xfffe0000, v96
v_add_f32 v109, v48, v49
v_cvt_pk_bf16_f32 v32, v32, v33
v_cvt_pk_bf16_f32 v48, v48, v49
v_mfma_f32_32x32x16_bf16 a[240:255], v[76:79], v[16:19], a[240:255]
v_exp_f32 v35, v35
v_exp_f32 v51, v51
v_add_f32 v108, v108, v34
s_waitcnt lgkmcnt(6)
v_mfma_f32_32x32x16_bf16 a[128:143], v[80:83], v[8:11], a[128:143]
s_add_u32 m0, s63, 100352
s_add_u32 s66, s88, 16512
buffer_load_dwordx4 v126, s[52:55], s66 offen offset:0 lds
ds_read_b128 v[64:67], v96 offset:0
v_add_u32 v97, 0xfffe0000, v97
v_add_f32 v109, v109, v50
v_mfma_f32_32x32x16_bf16 a[144:159], v[80:83], v[24:27], a[144:159]
v_exp_f32 v36, v36
v_exp_f32 v52, v52
v_add_f32 v108, v108, v35
s_waitcnt lgkmcnt(5)
v_mfma_f32_32x32x16_bf16 a[160:175], v[84:87], v[8:11], a[160:175]
ds_read_b128 v[68:71], v97 offset:0
v_add_u32 v98, 0xfffe0000, v98
v_add_f32 v109, v109, v51
v_cvt_pk_bf16_f32 v33, v34, v35
v_cvt_pk_bf16_f32 v49, v50, v51
v_accvgpr_write_b32 a98, v116
v_mfma_f32_32x32x16_bf16 a[176:191], v[84:87], v[24:27], a[176:191]
s_add_u32 m0, s63, 101376
s_add_u32 s66, s88, 24704
buffer_load_dwordx4 v126, s[52:55], s66 offen offset:0 lds
v_exp_f32 v37, v37
s_waitcnt lgkmcnt(4)
v_mfma_f32_32x32x16_bf16 a[192:207], v[88:91], v[8:11], a[192:207]
ds_read_b128 v[72:75], v98 offset:0
v_add_u32 v99, 0xfffe0000, v99
v_exp_f32 v53, v53
v_add_f32 v108, v108, v36
v_add_f32 v109, v109, v52
v_mfma_f32_32x32x16_bf16 a[208:223], v[88:91], v[24:27], a[208:223]
v_exp_f32 v38, v38
v_exp_f32 v54, v54
v_add_f32 v108, v108, v37
s_waitcnt lgkmcnt(3)
v_mfma_f32_32x32x16_bf16 a[224:239], v[92:95], v[8:11], a[224:239]
ds_read_b128 v[76:79], v99 offset:0
v_add_u32 v100, 0xfffe0000, v100
v_add_f32 v109, v109, v53
v_cvt_pk_bf16_f32 v34, v36, v37
v_cvt_pk_bf16_f32 v50, v52, v53
v_accvgpr_read_b32 v118, a102
v_mfma_f32_32x32x16_bf16 a[240:255], v[92:95], v[24:27], a[240:255]
s_add_u32 m0, s63, 102400
s_add_u32 s66, s88, 32896
buffer_load_dwordx4 v126, s[52:55], s66 offen offset:0 lds
v_exp_f32 v39, v39
s_waitcnt lgkmcnt(3)
v_mfma_f32_32x32x16_bf16 v[0:15], v[64:67], v[192:195], v[128:143]
ds_read_b128 v[80:83], v100 offset:0
v_add_u32 v101, 0xfffe0000, v101
v_exp_f32 v55, v55
v_add_f32 v108, v108, v38
v_add_f32 v109, v109, v54
v_mfma_f32_32x32x16_bf16 v[16:31], v[64:67], v[208:211], v[144:159]
v_exp_f32 v40, v40
v_exp_f32 v56, v56
v_add_f32 v108, v108, v39
s_waitcnt lgkmcnt(3)
v_mfma_f32_32x32x16_bf16 v[0:15], v[68:71], v[196:199], v[0:15]
s_add_u32 m0, s63, 103424
s_add_u32 s66, s88, 41088
buffer_load_dwordx4 v126, s[52:55], s66 offen offset:0 lds
ds_read_b128 v[84:87], v101 offset:0
v_add_u32 v102, 0xfffe0000, v102
v_add_f32 v109, v109, v55
v_mfma_f32_32x32x16_bf16 v[16:31], v[68:71], v[212:215], v[16:31]
v_cvt_pk_bf16_f32 v35, v38, v39
v_cvt_pk_bf16_f32 v51, v54, v55
v_exp_f32 v41, v41
v_accvgpr_read_b32 v119, a103
s_waitcnt lgkmcnt(3)
v_mfma_f32_32x32x16_bf16 v[0:15], v[72:75], v[224:227], v[0:15]
ds_read_b128 v[88:91], v102 offset:0
v_add_u32 v103, 0xfffe0000, v103
v_exp_f32 v57, v57
v_add_f32 v108, v108, v40
v_add_f32 v109, v109, v56
v_mfma_f32_32x32x16_bf16 v[16:31], v[72:75], v[240:243], v[16:31]
v_exp_f32 v42, v42
v_exp_f32 v58, v58
v_add_f32 v108, v108, v41
s_waitcnt lgkmcnt(3)
v_mfma_f32_32x32x16_bf16 v[0:15], v[76:79], v[228:231], v[0:15]
s_add_u32 m0, s63, 104448
s_add_u32 s66, s88, 49280
buffer_load_dwordx4 v126, s[52:55], s66 offen offset:0 lds
ds_read_b128 v[92:95], v103 offset:0
v_add_f32 v109, v109, v57
v_cvt_pk_bf16_f32 v40, v40, v41
v_mfma_f32_32x32x16_bf16 v[16:31], v[76:79], v[244:247], v[16:31]
v_cvt_pk_bf16_f32 v56, v56, v57
v_exp_f32 v43, v43
v_exp_f32 v59, v59
s_waitcnt lgkmcnt(3)
v_mfma_f32_32x32x16_bf16 v[0:15], v[80:83], v[200:203], v[0:15]
ds_read_b64_tr_b16 v[64:65], v104 offset:8192
ds_read_b64_tr_b16 v[66:67], v104 offset:10240
v_add_f32 v108, v108, v42
v_add_f32 v109, v109, v58
v_exp_f32 v44, v44
v_mfma_f32_32x32x16_bf16 v[16:31], v[80:83], v[216:219], v[16:31]
s_add_u32 m0, s63, 105472
s_add_u32 s66, s88, 57472
buffer_load_dwordx4 v126, s[52:55], s66 offen offset:0 lds
v_exp_f32 v60, v60
s_waitcnt lgkmcnt(4)
v_mfma_f32_32x32x16_bf16 v[0:15], v[84:87], v[204:207], v[0:15]
ds_read_b64_tr_b16 v[68:69], v104 offset:8704
ds_read_b64_tr_b16 v[70:71], v104 offset:10752
v_add_f32 v108, v108, v43
v_add_f32 v109, v109, v59
v_cvt_pk_bf16_f32 v41, v42, v43
v_cvt_pk_bf16_f32 v57, v58, v59
v_mfma_f32_32x32x16_bf16 v[16:31], v[84:87], v[220:223], v[16:31]
v_exp_f32 v45, v45
v_exp_f32 v61, v61
v_add_f32 v108, v108, v44
s_waitcnt lgkmcnt(5)
v_mfma_f32_32x32x16_bf16 v[0:15], v[88:91], v[232:235], v[0:15]
ds_read_b64_tr_b16 v[72:73], v104 offset:9216
ds_read_b64_tr_b16 v[74:75], v104 offset:11264
v_add_f32 v109, v109, v60
v_exp_f32 v46, v46
v_mul_f32 v118, v118, v114
v_mfma_f32_32x32x16_bf16 v[16:31], v[88:91], v[248:251], v[16:31]
v_exp_f32 v62, v62
v_add_f32 v108, v108, v45
v_add_f32 v109, v109, v61
v_cvt_pk_bf16_f32 v42, v44, v45
s_waitcnt lgkmcnt(6)
v_mfma_f32_32x32x16_bf16 v[0:15], v[92:95], v[236:239], v[0:15]
ds_read_b64_tr_b16 v[76:77], v104 offset:9728
ds_read_b64_tr_b16 v[78:79], v104 offset:11776
v_cvt_pk_bf16_f32 v58, v60, v61
v_exp_f32 v47, v47
v_mul_f32 v119, v119, v114
v_mfma_f32_32x32x16_bf16 v[16:31], v[92:95], v[252:255], v[16:31]
v_exp_f32 v63, v63
v_add_f32 v108, v108, v46
v_add_f32 v109, v109, v62
v_add_f32 v108, v108, v47
v_add_f32 v109, v109, v63
v_cvt_pk_bf16_f32 v43, v46, v47
v_cvt_pk_bf16_f32 v59, v62, v63
v_max_f32 v160, v108, v109
v_cmp_nge_f32 vcc, 0x45800000, v160
s_cbranch_vccnz L_tr_29
L_ret_29:
v_add_f32 v112, v112, v108
v_add_f32 v113, v113, v109
s_waitcnt lgkmcnt(6)
v_mfma_f32_32x32x16_bf16 a[128:143], v[64:67], v[32:35], a[128:143]
ds_read_b64_tr_b16 v[80:81], v104 offset:12288
ds_read_b64_tr_b16 v[82:83], v104 offset:14336
v_cvt_pk_bf16_f32 v118, v118, v119
v_accvgpr_write_b32 a99, v118
v_accvgpr_read_b32 v116, a104
v_accvgpr_read_b32 v117, a105
v_mfma_f32_32x32x16_bf16 a[144:159], v[64:67], v[48:51], a[144:159]
v_mul_f32 v116, v116, v114
v_mul_f32 v117, v117, v114
v_cvt_pk_bf16_f32 v116, v116, v117
v_accvgpr_write_b32 a100, v116
v_accvgpr_read_b32 v118, a106
s_waitcnt lgkmcnt(6)
v_mfma_f32_32x32x16_bf16 a[160:175], v[68:71], v[32:35], a[160:175]
ds_read_b64_tr_b16 v[84:85], v104 offset:12800
ds_read_b64_tr_b16 v[86:87], v104 offset:14848
v_exp_f32 v0, v0
v_exp_f32 v16, v16
v_mfma_f32_32x32x16_bf16 a[176:191], v[68:71], v[48:51], a[176:191]
v_exp_f32 v1, v1
v_exp_f32 v17, v17
v_accvgpr_read_b32 v119, a107
s_waitcnt lgkmcnt(6)
v_mfma_f32_32x32x16_bf16 a[192:207], v[72:75], v[32:35], a[192:207]
ds_read_b64_tr_b16 v[88:89], v104 offset:13312
ds_read_b64_tr_b16 v[90:91], v104 offset:15360
v_exp_f32 v2, v2
v_exp_f32 v18, v18
v_mfma_f32_32x32x16_bf16 a[208:223], v[72:75], v[48:51], a[208:223]
v_add_f32 v108, v0, v1
v_add_f32 v109, v16, v17
v_cvt_pk_bf16_f32 v0, v0, v1
v_cvt_pk_bf16_f32 v16, v16, v17
v_mul_f32 v118, v118, v114
s_waitcnt lgkmcnt(6)
v_mfma_f32_32x32x16_bf16 a[224:239], v[76:79], v[32:35], a[224:239]
ds_read_b64_tr_b16 v[92:93], v104 offset:13824
ds_read_b64_tr_b16 v[94:95], v104 offset:15872
v_exp_f32 v3, v3
v_exp_f32 v19, v19
v_mfma_f32_32x32x16_bf16 a[240:255], v[76:79], v[48:51], a[240:255]
v_add_f32 v108, v108, v2
v_add_f32 v109, v109, v18
v_exp_f32 v4, v4
v_mul_f32 v119, v119, v114
s_waitcnt lgkmcnt(6)
v_mfma_f32_32x32x16_bf16 a[128:143], v[80:83], v[40:43], a[128:143]
ds_read_b128 v[64:67], v96 offset:8192
v_exp_f32 v20, v20
v_add_f32 v108, v108, v3
v_add_f32 v109, v109, v19
v_cvt_pk_bf16_f32 v1, v2, v3
v_mfma_f32_32x32x16_bf16 a[144:159], v[80:83], v[56:59], a[144:159]
v_cvt_pk_bf16_f32 v17, v18, v19
v_exp_f32 v5, v5
v_exp_f32 v21, v21
s_waitcnt lgkmcnt(5)
v_mfma_f32_32x32x16_bf16 a[160:175], v[84:87], v[40:43], a[160:175]
ds_read_b128 v[68:71], v97 offset:8192
v_add_f32 v108, v108, v4
v_add_f32 v109, v109, v20
v_exp_f32 v6, v6
v_cvt_pk_bf16_f32 v118, v118, v119
v_mfma_f32_32x32x16_bf16 a[176:191], v[84:87], v[56:59], a[176:191]
v_exp_f32 v22, v22
v_add_f32 v108, v108, v5
v_add_f32 v109, v109, v21
v_cvt_pk_bf16_f32 v2, v4, v5
s_waitcnt lgkmcnt(4)
v_mfma_f32_32x32x16_bf16 a[192:207], v[88:91], v[40:43], a[192:207]
v_accvgpr_write_b32 a101, v118
v_accvgpr_read_b32 v116, a108
v_accvgpr_read_b32 v117, a109
v_mul_f32 v116, v116, v114
v_mul_f32 v117, v117, v114
v_cvt_pk_bf16_f32 v116, v116, v117
v_accvgpr_write_b32 a102, v116
v_accvgpr_read_b32 v118, a110
v_accvgpr_read_b32 v119, a111
v_mul_f32 v118, v118, v114
v_mul_f32 v119, v119, v114
v_cvt_pk_bf16_f32 v118, v118, v119
v_accvgpr_write_b32 a103, v118
v_accvgpr_read_b32 v116, a112
v_accvgpr_read_b32 v117, a113
v_mul_f32 v116, v116, v115
v_mul_f32 v117, v117, v115
v_cvt_pk_bf16_f32 v116, v116, v117
v_accvgpr_write_b32 a112, v116
v_accvgpr_read_b32 v118, a114
v_accvgpr_read_b32 v119, a115
v_mul_f32 v118, v118, v115
v_mul_f32 v119, v119, v115
v_cvt_pk_bf16_f32 v118, v118, v119
v_accvgpr_write_b32 a113, v118
v_accvgpr_read_b32 v116, a116
v_accvgpr_read_b32 v117, a117
v_mul_f32 v116, v116, v115
v_mul_f32 v117, v117, v115
v_cvt_pk_bf16_f32 v116, v116, v117
v_accvgpr_write_b32 a114, v116
v_accvgpr_read_b32 v118, a118
v_accvgpr_read_b32 v119, a119
v_mul_f32 v118, v118, v115
v_mul_f32 v119, v119, v115
v_cvt_pk_bf16_f32 v118, v118, v119
v_accvgpr_write_b32 a115, v118
v_accvgpr_read_b32 v116, a120
v_accvgpr_read_b32 v117, a121
v_mul_f32 v116, v116, v115
v_mul_f32 v117, v117, v115
v_cvt_pk_bf16_f32 v116, v116, v117
v_accvgpr_write_b32 a116, v116
v_accvgpr_read_b32 v118, a122
v_accvgpr_read_b32 v119, a123
v_mul_f32 v118, v118, v115
v_mul_f32 v119, v119, v115
v_cvt_pk_bf16_f32 v118, v118, v119
v_accvgpr_write_b32 a117, v118
v_accvgpr_read_b32 v116, a124
v_accvgpr_read_b32 v117, a125
v_mul_f32 v116, v116, v115
v_mul_f32 v117, v117, v115
v_cvt_pk_bf16_f32 v116, v116, v117
v_accvgpr_write_b32 a118, v116
v_accvgpr_read_b32 v118, a126
v_accvgpr_read_b32 v119, a127
v_mul_f32 v118, v118, v115
v_mul_f32 v119, v119, v115
v_cvt_pk_bf16_f32 v118, v118, v119
v_accvgpr_write_b32 a119, v118
v_mfma_f32_32x32x16_bf16 a[208:223], v[88:91], v[56:59], a[208:223]
ds_read_b128 v[72:75], v98 offset:8192
v_cvt_pk_bf16_f32 v18, v20, v21
v_exp_f32 v7, v7
s_waitcnt lgkmcnt(3)
v_mfma_f32_32x32x16_bf16 a[224:239], v[92:95], v[40:43], a[224:239]
s_cmp_lg_u32 s90, 0
s_cbranch_scc1 L_kx00
s_add_u32 s65, s91, 0
buffer_store_dwordx4 a[0:3], v127, s[84:87], s65 offen offset:0 sc1
buffer_store_dwordx4 a[4:7], v127, s[84:87], s65 offen offset:1024 sc1
s_add_u32 s65, s91, 8192
s_mov_b64 exec, s[48:49]
buffer_store_dwordx4 a[16:19], v127, s[84:87], s65 offen offset:0 sc1
buffer_store_dwordx4 a[20:23], v127, s[84:87], s65 offen offset:1024 sc1
s_mov_b64 exec, -1
s_branch L_dx00
L_kx00:
ds_write_b128 v123, a[0:3] offset:0
ds_write_b128 v123, a[4:7] offset:1024
ds_write_b128 v123, a[16:19] offset:8192
ds_write_b128 v123, a[20:23] offset:9216
buffer_store_dword v127, off, s[44:47], 0
buffer_store_dword v127, off, s[44:47], 0
buffer_store_dword v127, off, s[44:47], 0
buffer_store_dword v127, off, s[44:47], 0
L_dx00:
v_mfma_f32_32x32x16_bf16 a[240:255], v[92:95], v[56:59], a[240:255]
ds_read_b128 v[76:79], v99 offset:8192
v_exp_f32 v23, v23
v_add_f32 v108, v108, v6
v_add_f32 v109, v109, v22
s_waitcnt lgkmcnt(3)
v_mfma_f32_32x32x16_bf16 v[32:47], v[64:67], v[192:195], v[128:143]
s_cmp_lg_u32 s90, 0
s_cbranch_scc1 L_kx01
s_add_u32 s65, s91, 2048
buffer_store_dwordx4 a[32:35], v127, s[84:87], s65 offen offset:0 sc1
buffer_store_dwordx4 a[36:39], v127, s[84:87], s65 offen offset:1024 sc1
s_add_u32 s65, s91, 10240
s_mov_b64 exec, s[48:49]
buffer_store_dwordx4 a[48:51], v127, s[84:87], s65 offen offset:0 sc1
buffer_store_dwordx4 a[52:55], v127, s[84:87], s65 offen offset:1024 sc1
s_mov_b64 exec, -1
s_branch L_dx01
L_kx01:
ds_write_b128 v123, a[32:35] offset:2048
ds_write_b128 v123, a[36:39] offset:3072
ds_write_b128 v123, a[48:51] offset:10240
ds_write_b128 v123, a[52:55] offset:11264
buffer_store_dword v127, off, s[44:47], 0
buffer_store_dword v127, off, s[44:47], 0
buffer_store_dword v127, off, s[44:47], 0
buffer_store_dword v127, off, s[44:47], 0
L_dx01:
v_mfma_f32_32x32x16_bf16 v[48:63], v[64:67], v[208:211], v[144:159]
ds_read_b128 v[80:83], v100 offset:8192
v_exp_f32 v8, v8
v_exp_f32 v24, v24
s_waitcnt lgkmcnt(3)
v_mfma_f32_32x32x16_bf16 v[32:47], v[68:71], v[196:199], v[32:47]
s_cmp_lg_u32 s90, 0
s_cbranch_scc1 L_kx02
s_add_u32 s65, s91, 4096
buffer_store_dwordx4 a[64:67], v127, s[84:87], s65 offen offset:0 sc1
buffer_store_dwordx4 a[68:71], v127, s[84:87], s65 offen offset:1024 sc1
s_add_u32 s65, s91, 12288
s_mov_b64 exec, s[48:49]
buffer_store_dwordx4 a[80:83], v127, s[84:87], s65 offen offset:0 sc1
buffer_store_dwordx4 a[84:87], v127, s[84:87], s65 offen offset:1024 sc1
s_mov_b64 exec, -1
s_branch L_dx02
L_kx02:
ds_write_b128 v123, a[64:67] offset:4096
ds_write_b128 v123, a[68:71] offset:5120
ds_write_b128 v123, a[80:83] offset:12288
ds_write_b128 v123, a[84:87] offset:13312
buffer_store_dword v127, off, s[44:47], 0
buffer_store_dword v127, off, s[44:47], 0
buffer_store_dword v127, off, s[44:47], 0
buffer_store_dword v127, off, s[44:47], 0
L_dx02:
v_mfma_f32_32x32x16_bf16 v[48:63], v[68:71], v[212:215], v[48:63]
ds_read_b128 v[84:87], v101 offset:8192
v_add_f32 v108, v108, v7
v_add_f32 v109, v109, v23
v_cvt_pk_bf16_f32 v3, v6, v7
v_cvt_pk_bf16_f32 v19, v22, v23
s_waitcnt lgkmcnt(3)
v_mfma_f32_32x32x16_bf16 v[32:47], v[72:75], v[224:227], v[32:47]
s_cmp_lg_u32 s90, 0
s_cbranch_scc1 L_kx03
s_add_u32 s65, s91, 6144
buffer_store_dwordx4 a[96:99], v127, s[84:87], s65 offen offset:0 sc1
buffer_store_dwordx4 a[100:103], v127, s[84:87], s65 offen offset:1024 sc1
s_add_u32 s65, s91, 14336
s_mov_b64 exec, s[48:49]
buffer_store_dwordx4 a[112:115], v127, s[84:87], s65 offen offset:0 sc1
buffer_store_dwordx4 a[116:119], v127, s[84:87], s65 offen offset:1024 sc1
s_mov_b64 exec, -1
s_branch L_dx03
L_kx03:
ds_write_b128 v123, a[96:99] offset:6144
ds_write_b128 v123, a[100:103] offset:7168
ds_write_b128 v123, a[112:115] offset:14336
ds_write_b128 v123, a[116:119] offset:15360
buffer_store_dword v127, off, s[44:47], 0
buffer_store_dword v127, off, s[44:47], 0
buffer_store_dword v127, off, s[44:47], 0
buffer_store_dword v127, off, s[44:47], 0
L_dx03:
v_mfma_f32_32x32x16_bf16 v[48:63], v[72:75], v[240:243], v[48:63]
ds_read_b128 v[88:91], v102 offset:8192
v_exp_f32 v9, v9
v_exp_f32 v25, v25
s_waitcnt lgkmcnt(3)
v_mfma_f32_32x32x16_bf16 v[32:47], v[76:79], v[228:231], v[32:47]
ds_read_b128 v[92:95], v103 offset:8192
v_add_u32 v104, 0xfffe0000, v104
v_add_f32 v108, v108, v8
v_add_f32 v109, v109, v24
v_exp_f32 v10, v10
v_mfma_f32_32x32x16_bf16 v[48:63], v[76:79], v[244:247], v[48:63]
v_exp_f32 v26, v26
v_add_f32 v108, v108, v9
v_add_f32 v109, v109, v25
v_cvt_pk_bf16_f32 v8, v8, v9
s_waitcnt lgkmcnt(3)
v_mfma_f32_32x32x16_bf16 v[32:47], v[80:83], v[200:203], v[32:47]
ds_read_b64_tr_b16 v[64:65], v104 offset:0
ds_read_b64_tr_b16 v[66:67], v104 offset:2048
v_cvt_pk_bf16_f32 v24, v24, v25
v_exp_f32 v11, v11
v_mfma_f32_32x32x16_bf16 v[48:63], v[80:83], v[216:219], v[48:63]
v_exp_f32 v27, v27
v_add_f32 v108, v108, v10
v_add_f32 v109, v109, v26
s_waitcnt lgkmcnt(4)
v_mfma_f32_32x32x16_bf16 v[32:47], v[84:87], v[204:207], v[32:47]
ds_read_b64_tr_b16 v[68:69], v104 offset:512
ds_read_b64_tr_b16 v[70:71], v104 offset:2560
v_exp_f32 v12, v12
v_exp_f32 v28, v28
v_mfma_f32_32x32x16_bf16 v[48:63], v[84:87], v[220:223], v[48:63]
v_add_f32 v108, v108, v11
v_add_f32 v109, v109, v27
v_cvt_pk_bf16_f32 v9, v10, v11
v_cvt_pk_bf16_f32 v25, v26, v27
s_waitcnt lgkmcnt(5)
v_mfma_f32_32x32x16_bf16 v[32:47], v[88:91], v[232:235], v[32:47]
s_waitcnt vmcnt(24)
s_waitcnt lgkmcnt(0)
s_barrier
ds_read_b64_tr_b16 v[72:73], v104 offset:1024
ds_read_b64_tr_b16 v[74:75], v104 offset:3072
v_mfma_f32_32x32x16_bf16 v[48:63], v[88:91], v[248:251], v[48:63]
v_exp_f32 v13, v13
v_exp_f32 v29, v29
v_add_f32 v108, v108, v12
v_mfma_f32_32x32x16_bf16 v[32:47], v[92:95], v[236:239], v[32:47]
ds_read_b64_tr_b16 v[76:77], v104 offset:1536
ds_read_b64_tr_b16 v[78:79], v104 offset:3584
v_add_f32 v109, v109, v28
v_exp_f32 v14, v14
ds_read_b128 a[0:3], v123 offset:0
v_mfma_f32_32x32x16_bf16 v[48:63], v[92:95], v[252:255], v[48:63]
v_exp_f32 v30, v30
v_add_f32 v108, v108, v13
v_add_f32 v109, v109, v29
v_cvt_pk_bf16_f32 v10, v12, v13
v_cvt_pk_bf16_f32 v26, v28, v29
v_exp_f32 v15, v15
v_exp_f32 v31, v31
v_add_f32 v108, v108, v14
v_add_f32 v109, v109, v30
v_add_f32 v108, v108, v15
v_add_f32 v109, v109, v31
v_cvt_pk_bf16_f32 v11, v14, v15
v_cvt_pk_bf16_f32 v27, v30, v31
v_max_f32 v160, v108, v109
v_cmp_nge_f32 vcc, 0x45800000, v160
s_cbranch_vccnz L_tr_30
L_ret_30:
v_add_f32 v112, v112, v108
v_add_f32 v113, v113, v109
v_mfma_f32_32x32x16_bf16 a[128:143], v[64:67], v[0:3], a[128:143]
ds_read_b64_tr_b16 v[80:81], v104 offset:4096
ds_read_b64_tr_b16 v[82:83], v104 offset:6144
ds_read_b128 a[16:19], v123 offset:8192
ds_read_b128 a[4:7], v123 offset:1024
ds_read_b128 a[20:23], v123 offset:9216
v_mfma_f32_32x32x16_bf16 a[144:159], v[64:67], v[16:19], a[144:159]
s_add_u32 m0, s63, 131072
s_add_u32 s66, s88, 256
buffer_load_dwordx4 v126, s[52:55], s66 offen offset:0 lds
ds_read_b128 a[32:35], v123 offset:2048
ds_read_b128 a[48:51], v123 offset:10240
v_mfma_f32_32x32x16_bf16 a[160:175], v[68:71], v[0:3], a[160:175]
ds_read_b64_tr_b16 v[84:85], v104 offset:4608
ds_read_b64_tr_b16 v[86:87], v104 offset:6656
v_exp_f32 v32, v32
v_exp_f32 v48, v48
v_mfma_f32_32x32x16_bf16 a[176:191], v[68:71], v[16:19], a[176:191]
v_exp_f32 v33, v33
v_exp_f32 v49, v49
ds_read_b128 a[36:39], v123 offset:3072
s_waitcnt lgkmcnt(13)
v_mfma_f32_32x32x16_bf16 a[192:207], v[72:75], v[0:3], a[192:207]
s_add_u32 m0, s63, 132096
s_add_u32 s66, s88, 8448
buffer_load_dwordx4 v126, s[52:55], s66 offen offset:0 lds
ds_read_b64_tr_b16 v[88:89], v104 offset:5120
ds_read_b64_tr_b16 v[90:91], v104 offset:7168
ds_read_b128 a[52:55], v123 offset:11264
v_mfma_f32_32x32x16_bf16 a[208:223], v[72:75], v[16:19], a[208:223]
v_exp_f32 v34, v34
v_exp_f32 v50, v50
v_add_f32 v108, v32, v33
s_waitcnt lgkmcnt(14)
v_mfma_f32_32x32x16_bf16 a[224:239], v[76:79], v[0:3], a[224:239]
ds_read_b64_tr_b16 v[92:93], v104 offset:5632
ds_read_b64_tr_b16 v[94:95], v104 offset:7680
v_add_f32 v109, v48, v49
v_cvt_pk_bf16_f32 v32, v32, v33
v_cvt_pk_bf16_f32 v48, v48, v49
ds_read_b128 a[64:67], v123 offset:4096
v_mfma_f32_32x32x16_bf16 a[240:255], v[76:79], v[16:19], a[240:255]
v_exp_f32 v35, v35
v_exp_f32 v51, v51
v_add_f32 v108, v108, v34
s_waitcnt lgkmcnt(14)
v_mfma_f32_32x32x16_bf16 a[128:143], v[80:83], v[8:11], a[128:143]
s_add_u32 m0, s63, 133120
s_add_u32 s66, s88, 16640
buffer_load_dwordx4 v126, s[52:55], s66 offen offset:0 lds
ds_read_b128 a[80:83], v123 offset:12288
ds_read_b128 a[68:71], v123 offset:5120
ds_read_b128 a[84:87], v123 offset:13312
v_mfma_f32_32x32x16_bf16 a[144:159], v[80:83], v[24:27], a[144:159]
ds_read_b64_tr_b16 v[64:65], v105 offset:0
ds_read_b64_tr_b16 v[66:67], v105 offset:1024
v_add_f32 v109, v109, v50
v_exp_f32 v36, v36
s_waitcnt lgkmcnt(12)
v_mfma_f32_32x32x16_bf16 a[160:175], v[84:87], v[8:11], a[160:175]
ds_read_b64_tr_b16 v[68:69], v105 offset:2048
ds_read_b64_tr_b16 v[70:71], v105 offset:3072
v_exp_f32 v52, v52
v_add_f32 v108, v108, v35
v_add_f32 v109, v109, v51
v_mfma_f32_32x32x16_bf16 a[176:191], v[84:87], v[24:27], a[176:191]
s_add_u32 m0, s63, 134144
s_add_u32 s66, s88, 24832
buffer_load_dwordx4 v126, s[52:55], s66 offen offset:0 lds
ds_read_b128 a[96:99], v123 offset:6144
ds_read_b128 a[112:115], v123 offset:14336
ds_read_b128 a[100:103], v123 offset:7168
ds_read_b128 a[116:119], v123 offset:15360
s_waitcnt lgkmcnt(15)
v_mfma_f32_32x32x16_bf16 a[192:207], v[88:91], v[8:11], a[192:207]
ds_read_b64_tr_b16 v[72:73], v105 offset:4096
ds_read_b64_tr_b16 v[74:75], v105 offset:5120
v_cvt_pk_bf16_f32 v33, v34, v35
v_cvt_pk_bf16_f32 v49, v50, v51
v_exp_f32 v37, v37
v_mfma_f32_32x32x16_bf16 a[208:223], v[88:91], v[24:27], a[208:223]
v_exp_f32 v53, v53
v_add_f32 v108, v108, v36
v_add_f32 v109, v109, v52
s_waitcnt lgkmcnt(14)
v_mfma_f32_32x32x16_bf16 a[224:239], v[92:95], v[8:11], a[224:239]
ds_read_b64_tr_b16 v[76:77], v105 offset:6144
ds_read_b64_tr_b16 v[78:79], v105 offset:7168
v_exp_f32 v38, v38
v_exp_f32 v54, v54
v_mfma_f32_32x32x16_bf16 a[240:255], v[92:95], v[24:27], a[240:255]
s_add_u32 m0, s63, 135168
s_add_u32 s66, s88, 33024
buffer_load_dwordx4 v126, s[52:55], s66 offen offset:0 lds
v_add_f32 v108, v108, v37
v_add_f32 v109, v109, v53
s_waitcnt lgkmcnt(10)
v_mfma_f32_32x32x16_bf16 v[0:15], v[64:67], a[0:3], 0
ds_read_b64_tr_b16 v[80:81], v105 offset:8192
ds_read_b64_tr_b16 v[82:83], v105 offset:9216
v_cvt_pk_bf16_f32 v34, v36, v37
v_cvt_pk_bf16_f32 v50, v52, v53
v_exp_f32 v39, v39
v_mfma_f32_32x32x16_bf16 v[16:31], v[64:67], a[16:19], 0
v_exp_f32 v55, v55
v_add_f32 v108, v108, v38
v_add_f32 v109, v109, v54
s_waitcnt lgkmcnt(10)
v_mfma_f32_32x32x16_bf16 v[0:15], v[68:71], a[4:7], v[0:15]
s_add_u32 m0, s63, 136192
s_add_u32 s66, s88, 41216
buffer_load_dwordx4 v126, s[52:55], s66 offen offset:0 lds
ds_read_b64_tr_b16 v[84:85], v105 offset:10240
ds_read_b64_tr_b16 v[86:87], v105 offset:11264
v_mfma_f32_32x32x16_bf16 v[16:31], v[68:71], a[20:23], v[16:31]
v_exp_f32 v40, v40
v_exp_f32 v56, v56
v_add_f32 v108, v108, v39
s_waitcnt lgkmcnt(6)
v_mfma_f32_32x32x16_bf16 v[0:15], v[72:75], a[32:35], v[0:15]
ds_read_b64_tr_b16 v[88:89], v105 offset:12288
ds_read_b64_tr_b16 v[90:91], v105 offset:13312
v_add_f32 v109, v109, v55
v_cvt_pk_bf16_f32 v35, v38, v39
v_cvt_pk_bf16_f32 v51, v54, v55
v_mfma_f32_32x32x16_bf16 v[16:31], v[72:75], a[48:51], v[16:31]
v_exp_f32 v41, v41
v_exp_f32 v57, v57
v_add_f32 v108, v108, v40
s_waitcnt lgkmcnt(6)
v_mfma_f32_32x32x16_bf16 v[0:15], v[76:79], a[36:39], v[0:15]
s_add_u32 m0, s63, 137216
s_add_u32 s66, s88, 49408
buffer_load_dwordx4 v126, s[52:55], s66 offen offset:0 lds
ds_read_b64_tr_b16 v[92:93], v105 offset:14336
ds_read_b64_tr_b16 v[94:95], v105 offset:15360
v_add_f32 v109, v109, v56
v_mfma_f32_32x32x16_bf16 v[16:31], v[76:79], a[52:55], v[16:31]
v_exp_f32 v42, v42
v_exp_f32 v58, v58
v_add_f32 v108, v108, v41
s_waitcnt lgkmcnt(6)
v_mfma_f32_32x32x16_bf16 v[0:15], v[80:83], a[64:67], v[0:15]
s_cmp_lg_u32 s90, 0
s_cbranch_scc1 L_kf0
s_waitcnt vmcnt(7)
buffer_store_dword v124, off, s[76:79], s93 offset:0 sc1
s_branch L_df0
L_kf0:
buffer_store_dword v127, off, s[44:47], 0
L_df0:
ds_read_b64_tr_b16 v[64:65], v104 offset:8192
v_mfma_f32_32x32x16_bf16 v[16:31], v[80:83], a[80:83], v[16:31]
s_add_u32 m0, s63, 138240
s_add_u32 s66, s88, 57600
buffer_load_dwordx4 v126, s[52:55], s66 offen offset:0 lds
ds_read_b64_tr_b16 v[66:67], v104 offset:10240
v_add_f32 v109, v109, v57
s_waitcnt lgkmcnt(6)
v_mfma_f32_32x32x16_bf16 v[0:15], v[84:87], a[68:71], v[0:15]
ds_read_b64_tr_b16 v[68:69], v104 offset:8704
ds_read_b64_tr_b16 v[70:71], v104 offset:10752
v_cvt_pk_bf16_f32 v40, v40, v41
v_cvt_pk_bf16_f32 v56, v56, v57
v_exp_f32 v43, v43
v_mfma_f32_32x32x16_bf16 v[16:31], v[84:87], a[84:87], v[16:31]
v_exp_f32 v59, v59
v_add_f32 v108, v108, v42
v_add_f32 v109, v109, v58
s_waitcnt lgkmcnt(6)
v_mfma_f32_32x32x16_bf16 v[0:15], v[88:91], a[96:99], v[0:15]
ds_read_b64_tr_b16 v[72:73], v104 offset:9216
ds_read_b64_tr_b16 v[74:75], v104 offset:11264
v_exp_f32 v44, v44
v_exp_f32 v60, v60
v_mfma_f32_32x32x16_bf16 v[16:31], v[88:91], a[112:115], v[16:31]
v_add_f32 v108, v108, v43
v_add_f32 v109, v109, v59
v_cvt_pk_bf16_f32 v41, v42, v43
v_cvt_pk_bf16_f32 v57, v58, v59
s_waitcnt lgkmcnt(6)
v_mfma_f32_32x32x16_bf16 v[0:15], v[92:95], a[100:103], v[0:15]
ds_read_b64_tr_b16 v[76:77], v104 offset:9728
ds_read_b64_tr_b16 v[78:79], v104 offset:11776
v_exp_f32 v45, v45
v_exp_f32 v61, v61
v_mfma_f32_32x32x16_bf16 v[16:31], v[92:95], a[116:119], v[16:31]
v_add_f32 v108, v108, v44
v_add_f32 v109, v109, v60
v_exp_f32 v46, v46
v_exp_f32 v62, v62
v_add_f32 v108, v108, v45
v_add_f32 v109, v109, v61
v_cvt_pk_bf16_f32 v42, v44, v45
v_cvt_pk_bf16_f32 v58, v60, v61
v_exp_f32 v47, v47
v_exp_f32 v63, v63
v_add_f32 v108, v108, v46
v_add_f32 v109, v109, v62
v_add_f32 v108, v108, v47
v_add_f32 v109, v109, v63
v_cvt_pk_bf16_f32 v43, v46, v47
v_cvt_pk_bf16_f32 v59, v62, v63
v_max_f32 v160, v108, v109
v_cmp_nge_f32 vcc, 0x45800000, v160
s_cbranch_vccnz L_tr_31
L_ret_31:
v_add_f32 v112, v112, v108
v_add_f32 v113, v113, v109
s_waitcnt lgkmcnt(6)
v_mfma_f32_32x32x16_bf16 a[128:143], v[64:67], v[32:35], a[128:143]
ds_read_b64_tr_b16 v[80:81], v104 offset:12288
ds_read_b64_tr_b16 v[82:83], v104 offset:14336
v_mfma_f32_32x32x16_bf16 a[144:159], v[64:67], v[48:51], a[144:159]
s_waitcnt lgkmcnt(6)
v_mfma_f32_32x32x16_bf16 a[160:175], v[68:71], v[32:35], a[160:175]
ds_read_b64_tr_b16 v[84:85], v104 offset:12800
ds_read_b64_tr_b16 v[86:87], v104 offset:14848
v_mfma_f32_32x32x16_bf16 a[176:191], v[68:71], v[48:51], a[176:191]
s_waitcnt lgkmcnt(6)
v_mfma_f32_32x32x16_bf16 a[192:207], v[72:75], v[32:35], a[192:207]
ds_read_b64_tr_b16 v[88:89], v104 offset:13312
ds_read_b64_tr_b16 v[90:91], v104 offset:15360
v_mfma_f32_32x32x16_bf16 a[208:223], v[72:75], v[48:51], a[208:223]
s_waitcnt lgkmcnt(6)
v_mfma_f32_32x32x16_bf16 a[224:239], v[76:79], v[32:35], a[224:239]
ds_read_b64_tr_b16 v[92:93], v104 offset:13824
ds_read_b64_tr_b16 v[94:95], v104 offset:15872
v_mfma_f32_32x32x16_bf16 a[240:255], v[76:79], v[48:51], a[240:255]
s_waitcnt lgkmcnt(6)
v_mfma_f32_32x32x16_bf16 a[128:143], v[80:83], v[40:43], a[128:143]
ds_read_b64_tr_b16 v[64:65], v105 offset:512
ds_read_b64_tr_b16 v[66:67], v105 offset:1536
v_mfma_f32_32x32x16_bf16 a[144:159], v[80:83], v[56:59], a[144:159]
s_waitcnt lgkmcnt(6)
v_mfma_f32_32x32x16_bf16 a[160:175], v[84:87], v[40:43], a[160:175]
ds_read_b64_tr_b16 v[68:69], v105 offset:2560
ds_read_b64_tr_b16 v[70:71], v105 offset:3584
v_mfma_f32_32x32x16_bf16 a[176:191], v[84:87], v[56:59], a[176:191]
s_waitcnt lgkmcnt(6)
v_mfma_f32_32x32x16_bf16 a[192:207], v[88:91], v[40:43], a[192:207]
ds_read_b64_tr_b16 v[72:73], v105 offset:4608
ds_read_b64_tr_b16 v[74:75], v105 offset:5632
v_mfma_f32_32x32x16_bf16 a[208:223], v[88:91], v[56:59], a[208:223]
s_waitcnt lgkmcnt(6)
v_mfma_f32_32x32x16_bf16 a[224:239], v[92:95], v[40:43], a[224:239]
ds_read_b64_tr_b16 v[76:77], v105 offset:6656
ds_read_b64_tr_b16 v[78:79], v105 offset:7680
v_mfma_f32_32x32x16_bf16 a[240:255], v[92:95], v[56:59], a[240:255]
s_waitcnt lgkmcnt(6)
v_mfma_f32_32x32x16_bf16 v[32:47], v[64:67], a[0:3], 0
ds_read_b64_tr_b16 v[80:81], v105 offset:8704
ds_read_b64_tr_b16 v[82:83], v105 offset:9728
v_mfma_f32_32x32x16_bf16 v[48:63], v[64:67], a[16:19], 0
s_waitcnt lgkmcnt(6)
v_mfma_f32_32x32x16_bf16 v[32:47], v[68:71], a[4:7], v[32:47]
ds_read_b64_tr_b16 v[84:85], v105 offset:10752
ds_read_b64_tr_b16 v[86:87], v105 offset:11776
v_mfma_f32_32x32x16_bf16 v[48:63], v[68:71], a[20:23], v[48:63]
v_mov_b32 v114, v112
v_mov_b32 v116, v112
v_mov_b32 v115, v113
v_mov_b32 v117, v113
v_permlane32_swap_b32 v114, v116
s_nop 0
v_permlane32_swap_b32 v115, v117
v_add_f32 v114, v114, v116
v_add_f32 v115, v115, v117
v_rcp_f32 v114, v114
s_waitcnt lgkmcnt(6)
v_mfma_f32_32x32x16_bf16 v[32:47], v[72:75], a[32:35], v[32:47]
ds_read_b64_tr_b16 v[88:89], v105 offset:12800
ds_read_b64_tr_b16 v[90:91], v105 offset:13824
v_rcp_f32 v115, v115
v_accvgpr_read_b32 v116, a128
v_accvgpr_read_b32 v117, a129
v_mul_f32 v116, v116, v114
v_mul_f32 v117, v117, v114
v_cvt_pk_bf16_f32 v116, v116, v117
v_accvgpr_write_b32 a128, v116
v_accvgpr_read_b32 v118, a130
v_accvgpr_read_b32 v119, a131
v_mfma_f32_32x32x16_bf16 v[48:63], v[72:75], a[48:51], v[48:63]
v_mul_f32 v118, v118, v114
v_mul_f32 v119, v119, v114
v_cvt_pk_bf16_f32 v118, v118, v119
v_accvgpr_write_b32 a129, v118
v_accvgpr_read_b32 v116, a132
v_accvgpr_read_b32 v117, a133
v_mul_f32 v116, v116, v114
v_mul_f32 v117, v117, v114
v_cvt_pk_bf16_f32 v116, v116, v117
v_accvgpr_write_b32 a130, v116
v_accvgpr_read_b32 v118, a134
s_waitcnt lgkmcnt(6)
v_mfma_f32_32x32x16_bf16 v[32:47], v[76:79], a[36:39], v[32:47]
ds_read_b64_tr_b16 v[92:93], v105 offset:14848
ds_read_b64_tr_b16 v[94:95], v105 offset:15872
v_add_u32 v105, 0x10000, v105
v_accvgpr_read_b32 v119, a135
v_mul_f32 v118, v118, v114
v_mul_f32 v119, v119, v114
v_cvt_pk_bf16_f32 v118, v118, v119
v_accvgpr_write_b32 a131, v118
v_accvgpr_read_b32 v116, a136
v_accvgpr_read_b32 v117, a137
v_mul_f32 v116, v116, v114
v_mul_f32 v117, v117, v114
v_mfma_f32_32x32x16_bf16 v[48:63], v[76:79], a[52:55], v[48:63]
v_cvt_pk_bf16_f32 v116, v116, v117
v_accvgpr_write_b32 a132, v116
v_accvgpr_read_b32 v118, a138
v_accvgpr_read_b32 v119, a139
v_mul_f32 v118, v118, v114
v_mul_f32 v119, v119, v114
v_cvt_pk_bf16_f32 v118, v118, v119
v_accvgpr_write_b32 a133, v118
v_accvgpr_read_b32 v116, a140
v_accvgpr_read_b32 v117, a141
v_mul_f32 v116, v116, v114
s_waitcnt lgkmcnt(6)
v_mfma_f32_32x32x16_bf16 v[32:47], v[80:83], a[64:67], v[32:47]
v_mul_f32 v117, v117, v114
v_cvt_pk_bf16_f32 v116, v116, v117
v_accvgpr_write_b32 a134, v116
v_accvgpr_read_b32 v118, a142
v_accvgpr_read_b32 v119, a143
v_mul_f32 v118, v118, v114
v_mul_f32 v119, v119, v114
v_cvt_pk_bf16_f32 v118, v118, v119
v_accvgpr_write_b32 a135, v118
v_accvgpr_read_b32 v116, a144
v_accvgpr_read_b32 v117, a145
v_mul_f32 v116, v116, v115
v_mfma_f32_32x32x16_bf16 v[48:63], v[80:83], a[80:83], v[48:63]
s_waitcnt vmcnt(25)
s_waitcnt lgkmcnt(0)
s_barrier
v_mul_f32 v117, v117, v115
v_cvt_pk_bf16_f32 v116, v116, v117
v_accvgpr_write_b32 a144, v116
v_accvgpr_read_b32 v118, a146
v_accvgpr_read_b32 v119, a147
v_mul_f32 v118, v118, v115
v_mul_f32 v119, v119, v115
v_cvt_pk_bf16_f32 v118, v118, v119
v_mfma_f32_32x32x16_bf16 v[32:47], v[84:87], a[68:71], v[32:47]
ds_read_b64_tr_b16 v[64:65], v105 offset:0
ds_read_b64_tr_b16 v[66:67], v105 offset:1024
ds_read_b64_tr_b16 v[68:69], v105 offset:512
ds_read_b64_tr_b16 v[70:71], v105 offset:1536
v_accvgpr_write_b32 a145, v118
v_accvgpr_read_b32 v116, a148
v_accvgpr_read_b32 v117, a149
v_mul_f32 v116, v116, v115
v_mul_f32 v117, v117, v115
v_cvt_pk_bf16_f32 v116, v116, v117
v_accvgpr_write_b32 a146, v116
v_accvgpr_read_b32 v118, a150
v_mfma_f32_32x32x16_bf16 v[48:63], v[84:87], a[84:87], v[48:63]
s_add_u32 m0, s63, 0
s_add_u32 s66, s89, 0
buffer_load_dwordx4 v126, s[52:55], s66 offen offset:0 lds
v_accvgpr_read_b32 v119, a151
v_mul_f32 v118, v118, v115
v_mul_f32 v119, v119, v115
v_cvt_pk_bf16_f32 v118, v118, v119
v_accvgpr_write_b32 a147, v118
v_accvgpr_read_b32 v116, a152
v_accvgpr_read_b32 v117, a153
v_mul_f32 v116, v116, v115
v_mfma_f32_32x32x16_bf16 v[32:47], v[88:91], a[96:99], v[32:47]
ds_read_b64_tr_b16 v[72:73], v105 offset:2048
ds_read_b64_tr_b16 v[74:75], v105 offset:3072
v_mul_f32 v117, v117, v115
v_cvt_pk_bf16_f32 v116, v116, v117
v_accvgpr_write_b32 a148, v116
v_accvgpr_read_b32 v118, a154
v_accvgpr_read_b32 v119, a155
v_mul_f32 v118, v118, v115
v_mul_f32 v119, v119, v115
v_cvt_pk_bf16_f32 v118, v118, v119
v_accvgpr_write_b32 a149, v118
v_accvgpr_read_b32 v116, a156
v_mfma_f32_32x32x16_bf16 v[48:63], v[88:91], a[112:115], v[48:63]
v_accvgpr_read_b32 v117, a157
v_mul_f32 v116, v116, v115
v_mul_f32 v117, v117, v115
v_cvt_pk_bf16_f32 v116, v116, v117
v_accvgpr_write_b32 a150, v116
v_accvgpr_read_b32 v118, a158
v_accvgpr_read_b32 v119, a159
v_mul_f32 v118, v118, v115
v_mul_f32 v119, v119, v115
v_cvt_pk_bf16_f32 v118, v118, v119
v_accvgpr_write_b32 a151, v118
v_mfma_f32_32x32x16_bf16 v[32:47], v[92:95], a[100:103], v[32:47]
s_add_u32 m0, s63, 1024
s_add_u32 s66, s89, 8192
buffer_load_dwordx4 v126, s[52:55], s66 offen offset:0 lds
ds_read_b64_tr_b16 v[76:77], v105 offset:2560
ds_read_b64_tr_b16 v[78:79], v105 offset:3584
s_cmp_lg_u32 s90, 0
s_cbranch_scc1 L_kx10
s_add_u32 s65, s91, 16384
buffer_store_dwordx4 a[128:131], v127, s[84:87], s65 offen offset:0 sc1
buffer_store_dwordx4 a[132:135], v127, s[84:87], s65 offen offset:1024 sc1
s_add_u32 s65, s91, 24576
s_mov_b64 exec, s[48:49]
buffer_store_dwordx4 a[144:147], v127, s[84:87], s65 offen offset:0 sc1
buffer_store_dwordx4 a[148:151], v127, s[84:87], s65 offen offset:1024 sc1
s_mov_b64 exec, -1
s_branch L_dx10
L_kx10:
ds_write_b128 v123, a[128:131] offset:0
ds_write_b128 v123, a[132:135] offset:1024
ds_write_b128 v123, a[144:147] offset:8192
ds_write_b128 v123, a[148:151] offset:9216
buffer_store_dword v127, off, s[44:47], 0
buffer_store_dword v127, off, s[44:47], 0
buffer_store_dword v127, off, s[44:47], 0
buffer_store_dword v127, off, s[44:47], 0
L_dx10:
v_mfma_f32_32x32x16_bf16 v[48:63], v[92:95], a[116:119], v[48:63]
v_accvgpr_read_b32 v116, a160
v_accvgpr_read_b32 v117, a161
v_mul_f32 v116, v116, v114
v_mul_f32 v117, v117, v114
v_cvt_pk_bf16_f32 v116, v116, v117
v_accvgpr_write_b32 a160, v116
v_accvgpr_read_b32 v118, a162
v_accvgpr_read_b32 v119, a163
v_mul_f32 v118, v118, v114
v_mul_f32 v119, v119, v114
v_cvt_pk_bf16_f32 v118, v118, v119
s_waitcnt lgkmcnt(6)
v_mfma_f32_32x32x16_bf16 v[128:143], v[64:67], a[0:3], 0
ds_read_b64_tr_b16 v[80:81], v105 offset:4096
ds_read_b64_tr_b16 v[82:83], v105 offset:5120
v_accvgpr_write_b32 a161, v118
v_accvgpr_read_b32 v116, a164
v_accvgpr_read_b32 v117, a165
v_mul_f32 v116, v116, v114
v_mul_f32 v117, v117, v114
v_cvt_pk_bf16_f32 v116, v116, v117
v_accvgpr_write_b32 a162, v116
v_accvgpr_read_b32 v118, a166
v_accvgpr_read_b32 v119, a167
v_mul_f32 v118, v118, v114
v_mfma_f32_32x32x16_bf16 v[144:159], v[64:67], a[16:19], 0
s_add_u32 m0, s63, 2048
s_add_u32 s66, s89, 16384
buffer_load_dwordx4 v126, s[52:55], s66 offen offset:0 lds
buffer_load_dword v120, off, s[76:79], s94 offset:0 sc1
v_mul_f32 v119, v119, v114
v_cvt_pk_bf16_f32 v118, v118, v119
v_accvgpr_write_b32 a163, v118
v_accvgpr_read_b32 v116, a168
v_accvgpr_read_b32 v117, a169
v_mul_f32 v116, v116, v114
v_mul_f32 v117, v117, v114
s_waitcnt lgkmcnt(6)
v_mfma_f32_32x32x16_bf16 v[160:175], v[68:71], a[0:3], 0
ds_read_b64_tr_b16 v[84:85], v105 offset:4608
ds_read_b64_tr_b16 v[86:87], v105 offset:5632
v_cvt_pk_bf16_f32 v116, v116, v117
v_accvgpr_write_b32 a164, v116
v_accvgpr_read_b32 v118, a170
v_accvgpr_read_b32 v119, a171
v_mul_f32 v118, v118, v114
v_mul_f32 v119, v119, v114
v_cvt_pk_bf16_f32 v118, v118, v119
v_accvgpr_write_b32 a165, v118
v_accvgpr_read_b32 v116, a172
v_accvgpr_read_b32 v117, a173
v_mfma_f32_32x32x16_bf16 v[176:191], v[68:71], a[16:19], 0
v_mul_f32 v116, v116, v114
v_mul_f32 v117, v117, v114
v_cvt_pk_bf16_f32 v116, v116, v117
v_accvgpr_write_b32 a166, v116
v_accvgpr_read_b32 v118, a174
v_accvgpr_read_b32 v119, a175
v_mul_f32 v118, v118, v114
v_mul_f32 v119, v119, v114
v_cvt_pk_bf16_f32 v118, v118, v119
v_accvgpr_write_b32 a167, v118
v_accvgpr_read_b32 v116, a176
s_waitcnt lgkmcnt(6)
v_mfma_f32_32x32x16_bf16 v[128:143], v[72:75], a[4:7], v[128:143]
s_add_u32 m0, s63, 3072
s_add_u32 s66, s89, 24576
buffer_load_dwordx4 v126, s[52:55], s66 offen offset:0 lds
ds_read_b64_tr_b16 v[88:89], v105 offset:6144
ds_read_b64_tr_b16 v[90:91], v105 offset:7168
v_accvgpr_read_b32 v117, a177
v_mul_f32 v116, v116, v115
v_mul_f32 v117, v117, v115
v_cvt_pk_bf16_f32 v116, v116, v117
v_accvgpr_write_b32 a176, v116
v_accvgpr_read_b32 v118, a178
v_accvgpr_read_b32 v119, a179
v_mfma_f32_32x32x16_bf16 v[144:159], v[72:75], a[20:23], v[144:159]
v_mul_f32 v118, v118, v115
v_mul_f32 v119, v119, v115
v_cvt_pk_bf16_f32 v118, v118, v119
v_accvgpr_write_b32 a177, v118
v_accvgpr_read_b32 v116, a180
v_accvgpr_read_b32 v117, a181
v_mul_f32 v116, v116, v115
v_mul_f32 v117, v117, v115
v_cvt_pk_bf16_f32 v116, v116, v117
v_accvgpr_write_b32 a178, v116
v_accvgpr_read_b32 v118, a182
s_waitcnt lgkmcnt(6)
v_mfma_f32_32x32x16_bf16 v[160:175], v[76:79], a[4:7], v[160:175]
ds_read_b64_tr_b16 v[92:93], v105 offset:6656
ds_read_b64_tr_b16 v[94:95], v105 offset:7680
v_accvgpr_read_b32 v119, a183
v_mul_f32 v118, v118, v115
v_mul_f32 v119, v119, v115
v_cvt_pk_bf16_f32 v118, v118, v119
v_accvgpr_write_b32 a179, v118
v_accvgpr_read_b32 v116, a184
v_accvgpr_read_b32 v117, a185
v_mul_f32 v116, v116, v115
v_mul_f32 v117, v117, v115
v_cvt_pk_bf16_f32 v116, v116, v117
v_mfma_f32_32x32x16_bf16 v[176:191], v[76:79], a[20:23], v[176:191]
s_add_u32 m0, s63, 4096
s_add_u32 s66, s89, 32768
buffer_load_dwordx4 v126, s[52:55], s66 offen offset:0 lds
v_accvgpr_write_b32 a180, v116
v_accvgpr_read_b32 v118, a186
v_accvgpr_read_b32 v119, a187
v_mul_f32 v118, v118, v115
v_mul_f32 v119, v119, v115
v_cvt_pk_bf16_f32 v118, v118, v119
v_accvgpr_write_b32 a181, v118
v_accvgpr_read_b32 v116, a188
s_waitcnt lgkmcnt(6)
v_mfma_f32_32x32x16_bf16 v[128:143], v[80:83], a[32:35], v[128:143]
ds_read_b64_tr_b16 v[64:65], v105 offset:8192
ds_read_b64_tr_b16 v[66:67], v105 offset:9216
v_accvgpr_read_b32 v117, a189
v_mul_f32 v116, v116, v115
v_mul_f32 v117, v117, v115
v_cvt_pk_bf16_f32 v116, v116, v117
v_accvgpr_write_b32 a182, v116
v_accvgpr_read_b32 v118, a190
v_accvgpr_read_b32 v119, a191
v_mul_f32 v118, v118, v115
v_mul_f32 v119, v119, v115
v_cvt_pk_bf16_f32 v118, v118, v119
v_mfma_f32_32x32x16_bf16 v[144:159], v[80:83], a[48:51], v[144:159]
v_accvgpr_write_b32 a183, v118
s_cmp_lg_u32 s90, 0
s_cbranch_scc1 L_kx11
s_add_u32 s65, s91, 18432
buffer_store_dwordx4 a[160:163], v127, s[84:87], s65 offen offset:0 sc1
buffer_store_dwordx4 a[164:167], v127, s[84:87], s65 offen offset:1024 sc1
s_add_u32 s65, s91, 26624
s_mov_b64 exec, s[48:49]
buffer_store_dwordx4 a[176:179], v127, s[84:87], s65 offen offset:0 sc1
buffer_store_dwordx4 a[180:183], v127, s[84:87], s65 offen offset:1024 sc1
s_mov_b64 exec, -1
s_branch L_dx11
L_kx11:
ds_write_b128 v123, a[160:163] offset:2048
ds_write_b128 v123, a[164:167] offset:3072
ds_write_b128 v123, a[176:179] offset:10240
ds_write_b128 v123, a[180:183] offset:11264
buffer_store_dword v127, off, s[44:47], 0
buffer_store_dword v127, off, s[44:47], 0
buffer_store_dword v127, off, s[44:47], 0
buffer_store_dword v127, off, s[44:47], 0
L_dx11:
s_waitcnt lgkmcnt(6)
v_mfma_f32_32x32x16_bf16 v[160:175], v[84:87], a[32:35], v[160:175]
s_add_u32 m0, s63, 5120
s_add_u32 s66, s89, 40960
buffer_load_dwordx4 v126, s[52:55], s66 offen offset:0 lds
ds_read_b64_tr_b16 v[68:69], v105 offset:8704
ds_read_b64_tr_b16 v[70:71], v105 offset:9728
v_accvgpr_read_b32 v116, a192
v_accvgpr_read_b32 v117, a193
v_mul_f32 v116, v116, v114
v_mul_f32 v117, v117, v114
v_cvt_pk_bf16_f32 v116, v116, v117
v_accvgpr_write_b32 a192, v116
v_accvgpr_read_b32 v118, a194
v_mfma_f32_32x32x16_bf16 v[176:191], v[84:87], a[48:51], v[176:191]
v_accvgpr_read_b32 v119, a195
v_mul_f32 v118, v118, v114
v_mul_f32 v119, v119, v114
v_cvt_pk_bf16_f32 v118, v118, v119
v_accvgpr_write_b32 a193, v118
v_accvgpr_read_b32 v116, a196
v_accvgpr_read_b32 v117, a197
v_mul_f32 v116, v116, v114
v_mul_f32 v117, v117, v114
v_cvt_pk_bf16_f32 v116, v116, v117
v_accvgpr_write_b32 a194, v116
s_waitcnt lgkmcnt(6)
v_mfma_f32_32x32x16_bf16 v[128:143], v[88:91], a[36:39], v[128:143]
ds_read_b64_tr_b16 v[72:73], v105 offset:10240
ds_read_b64_tr_b16 v[74:75], v105 offset:11264
v_accvgpr_read_b32 v118, a198
v_accvgpr_read_b32 v119, a199
v_mul_f32 v118, v118, v114
v_mul_f32 v119, v119, v114
v_cvt_pk_bf16_f32 v118, v118, v119
v_accvgpr_write_b32 a195, v118
v_accvgpr_read_b32 v116, a200
v_accvgpr_read_b32 v117, a201
v_mul_f32 v116, v116, v114
v_mul_f32 v117, v117, v114
v_mfma_f32_32x32x16_bf16 v[144:159], v[88:91], a[52:55], v[144:159]
s_add_u32 m0, s63, 6144
s_add_u32 s66, s89, 49152
buffer_load_dwordx4 v126, s[52:55], s66 offen offset:0 lds
v_cvt_pk_bf16_f32 v116, v116, v117
v_accvgpr_write_b32 a196, v116
v_accvgpr_read_b32 v118, a202
v_accvgpr_read_b32 v119, a203
v_mul_f32 v118, v118, v114
v_mul_f32 v119, v119, v114
v_cvt_pk_bf16_f32 v118, v118, v119
v_accvgpr_write_b32 a197, v118
s_waitcnt lgkmcnt(6)
v_mfma_f32_32x32x16_bf16 v[160:175], v[92:95], a[36:39], v[160:175]
ds_read_b64_tr_b16 v[76:77], v105 offset:10752
ds_read_b64_tr_b16 v[78:79], v105 offset:11776
v_accvgpr_read_b32 v116, a204
v_accvgpr_read_b32 v117, a205
v_mul_f32 v116, v116, v114
v_mul_f32 v117, v117, v114
v_cvt_pk_bf16_f32 v116, v116, v117
v_accvgpr_write_b32 a198, v116
v_accvgpr_read_b32 v118, a206
v_accvgpr_read_b32 v119, a207
v_mul_f32 v118, v118, v114
v_mul_f32 v119, v119, v114
v_mfma_f32_32x32x16_bf16 v[176:191], v[92:95], a[52:55], v[176:191]
v_cvt_pk_bf16_f32 v118, v118, v119
v_accvgpr_write_b32 a199, v118
v_accvgpr_read_b32 v116, a208
v_accvgpr_read_b32 v117, a209
v_mul_f32 v116, v116, v115
v_mul_f32 v117, v117, v115
v_cvt_pk_bf16_f32 v116, v116, v117
v_accvgpr_write_b32 a208, v116
v_accvgpr_read_b32 v118, a210
v_accvgpr_read_b32 v119, a211
v_mul_f32 v118, v118, v115
s_waitcnt lgkmcnt(6)
v_mfma_f32_32x32x16_bf16 v[128:143], v[64:67], a[64:67], v[128:143]
s_add_u32 m0, s63, 7168
s_add_u32 s66, s89, 57344
buffer_load_dwordx4 v126, s[52:55], s66 offen offset:0 lds
ds_read_b64_tr_b16 v[80:81], v105 offset:12288
ds_read_b64_tr_b16 v[82:83], v105 offset:13312
v_mul_f32 v119, v119, v115
v_cvt_pk_bf16_f32 v118, v118, v119
v_accvgpr_write_b32 a209, v118
v_accvgpr_read_b32 v116, a212
v_accvgpr_read_b32 v117, a213
v_mul_f32 v116, v116, v115
v_mul_f32 v117, v117, v115
v_mfma_f32_32x32x16_bf16 v[144:159], v[64:67], a[80:83], v[144:159]
v_cvt_pk_bf16_f32 v116, v116, v117
v_accvgpr_write_b32 a210, v116
v_accvgpr_read_b32 v118, a214
v_accvgpr_read_b32 v119, a215
v_mul_f32 v118, v118, v115
v_mul_f32 v119, v119, v115
v_cvt_pk_bf16_f32 v118, v118, v119
v_accvgpr_write_b32 a211, v118
v_accvgpr_read_b32 v116, a216
v_accvgpr_read_b32 v117, a217
v_mul_f32 v116, v116, v115
s_waitcnt lgkmcnt(6)
v_mfma_f32_32x32x16_bf16 v[160:175], v[68:71], a[64:67], v[160:175]
ds_read_b64_tr_b16 v[84:85], v105 offset:12800
ds_read_b64_tr_b16 v[86:87], v105 offset:13824
v_mul_f32 v117, v117, v115
v_cvt_pk_bf16_f32 v116, v116, v117
v_accvgpr_write_b32 a212, v116
v_accvgpr_read_b32 v118, a218
v_accvgpr_read_b32 v119, a219
v_mul_f32 v118, v118, v115
v_mul_f32 v119, v119, v115
v_cvt_pk_bf16_f32 v118, v118, v119
v_accvgpr_write_b32 a213, v118
v_accvgpr_read_b32 v116, a220
v_mfma_f32_32x32x16_bf16 v[176:191], v[68:71], a[80:83], v[176:191]
v_accvgpr_read_b32 v117, a221
v_mul_f32 v116, v116, v115
v_mul_f32 v117, v117, v115
v_cvt_pk_bf16_f32 v116, v116, v117
v_accvgpr_write_b32 a214, v116
v_accvgpr_read_b32 v118, a222
v_accvgpr_read_b32 v119, a223
v_mul_f32 v118, v118, v115
v_mul_f32 v119, v119, v115
v_cvt_pk_bf16_f32 v118, v118, v119
v_accvgpr_write_b32 a215, v118
s_waitcnt lgkmcnt(6)
v_mfma_f32_32x32x16_bf16 v[128:143], v[72:75], a[68:71], v[128:143]
ds_read_b64_tr_b16 v[88:89], v105 offset:14336
ds_read_b64_tr_b16 v[90:91], v105 offset:15360
s_cmp_lg_u32 s90, 0
s_cbranch_scc1 L_kx12
s_add_u32 s65, s91, 20480
buffer_store_dwordx4 a[192:195], v127, s[84:87], s65 offen offset:0 sc1
buffer_store_dwordx4 a[196:199], v127, s[84:87], s65 offen offset:1024 sc1
s_add_u32 s65, s91, 28672
s_mov_b64 exec, s[48:49]
buffer_store_dwordx4 a[208:211], v127, s[84:87], s65 offen offset:0 sc1
buffer_store_dwordx4 a[212:215], v127, s[84:87], s65 offen offset:1024 sc1
s_mov_b64 exec, -1
s_branch L_dx12
L_kx12:
ds_write_b128 v123, a[192:195] offset:4096
ds_write_b128 v123, a[196:199] offset:5120
ds_write_b128 v123, a[208:211] offset:12288
ds_write_b128 v123, a[212:215] offset:13312
buffer_store_dword v127, off, s[44:47], 0
buffer_store_dword v127, off, s[44:47], 0
buffer_store_dword v127, off, s[44:47], 0
buffer_store_dword v127, off, s[44:47], 0
L_dx12:
v_mfma_f32_32x32x16_bf16 v[144:159], v[72:75], a[84:87], v[144:159]
v_accvgpr_read_b32 v116, a224
v_accvgpr_read_b32 v117, a225
v_mul_f32 v116, v116, v114
v_mul_f32 v117, v117, v114
v_cvt_pk_bf16_f32 v116, v116, v117
v_accvgpr_write_b32 a224, v116
v_accvgpr_read_b32 v118, a226
v_accvgpr_read_b32 v119, a227
v_mul_f32 v118, v118, v114
v_mul_f32 v119, v119, v114
v_cvt_pk_bf16_f32 v118, v118, v119
s_waitcnt lgkmcnt(6)
v_mfma_f32_32x32x16_bf16 v[160:175], v[76:79], a[68:71], v[160:175]
ds_read_b64_tr_b16 v[92:93], v105 offset:14848
ds_read_b64_tr_b16 v[94:95], v105 offset:15872
v_add_u32 v105, 0x8000, v105
v_accvgpr_write_b32 a225, v118
v_accvgpr_read_b32 v116, a228
v_accvgpr_read_b32 v117, a229
v_mul_f32 v116, v116, v114
v_mul_f32 v117, v117, v114
v_cvt_pk_bf16_f32 v116, v116, v117
v_accvgpr_write_b32 a226, v116
v_accvgpr_read_b32 v118, a230
v_accvgpr_read_b32 v119, a231
v_mfma_f32_32x32x16_bf16 v[176:191], v[76:79], a[84:87], v[176:191]
v_mul_f32 v118, v118, v114
v_mul_f32 v119, v119, v114
v_cvt_pk_bf16_f32 v118, v118, v119
v_accvgpr_write_b32 a227, v118
v_accvgpr_read_b32 v116, a232
v_accvgpr_read_b32 v117, a233
v_mul_f32 v116, v116, v114
v_mul_f32 v117, v117, v114
v_cvt_pk_bf16_f32 v116, v116, v117
v_accvgpr_write_b32 a228, v116
v_accvgpr_read_b32 v118, a234
s_waitcnt lgkmcnt(6)
v_mfma_f32_32x32x16_bf16 v[128:143], v[80:83], a[96:99], v[128:143]
v_accvgpr_read_b32 v119, a235
v_mul_f32 v118, v118, v114
v_mul_f32 v119, v119, v114
v_cvt_pk_bf16_f32 v118, v118, v119
v_accvgpr_write_b32 a229, v118
v_accvgpr_read_b32 v116, a236
v_accvgpr_read_b32 v117, a237
v_mul_f32 v116, v116, v114
v_mul_f32 v117, v117, v114
v_cvt_pk_bf16_f32 v116, v116, v117
v_accvgpr_write_b32 a230, v116
v_accvgpr_read_b32 v118, a238
v_mfma_f32_32x32x16_bf16 v[144:159], v[80:83], a[112:115], v[144:159]
s_waitcnt vmcnt(21)
s_waitcnt vmcnt(13)
v_readfirstlane_b32 s65, v120
s_xor_b32 s65, s65, s95
s_or_b32 s65, s65, s90
s_cbranch_scc0 L_sp0
L_spd0:
s_waitcnt lgkmcnt(0)
s_barrier
v_accvgpr_read_b32 v119, a239
v_mul_f32 v118, v118, v114
v_mul_f32 v119, v119, v114
v_mfma_f32_32x32x16_bf16 v[160:175], v[84:87], a[96:99], v[160:175]
s_add_u32 s65, s92, 0
buffer_load_dwordx4 a[8:11], v127, s[84:87], s65 offen offset:0 sc1
buffer_load_dwordx4 a[12:15], v127, s[84:87], s65 offen offset:1024 sc1
ds_read_b64_tr_b16 v[64:65], v105 offset:0
ds_read_b64_tr_b16 v[66:67], v105 offset:1024
ds_read_b64_tr_b16 v[68:69], v105 offset:512
ds_read_b64_tr_b16 v[70:71], v105 offset:1536
v_cvt_pk_bf16_f32 v118, v118, v119
v_accvgpr_write_b32 a231, v118
v_accvgpr_read_b32 v116, a240
v_accvgpr_read_b32 v117, a241
v_mul_f32 v116, v116, v115
v_mfma_f32_32x32x16_bf16 v[176:191], v[84:87], a[112:115], v[176:191]
s_add_u32 m0, s63, 32768
s_add_u32 s66, s89, 128
buffer_load_dwordx4 v126, s[52:55], s66 offen offset:0 lds
v_mul_f32 v117, v117, v115
v_cvt_pk_bf16_f32 v116, v116, v117
v_accvgpr_write_b32 a240, v116
v_accvgpr_read_b32 v118, a242
v_accvgpr_read_b32 v119, a243
v_mul_f32 v118, v118, v115
v_mul_f32 v119, v119, v115
v_cvt_pk_bf16_f32 v118, v118, v119
v_mfma_f32_32x32x16_bf16 v[128:143], v[88:91], a[100:103], v[128:143]
ds_read_b64_tr_b16 v[72:73], v105 offset:2048
ds_read_b64_tr_b16 v[74:75], v105 offset:3072
v_accvgpr_write_b32 a241, v118
v_accvgpr_read_b32 v116, a244
v_accvgpr_read_b32 v117, a245
v_mul_f32 v116, v116, v115
v_mul_f32 v117, v117, v115
v_cvt_pk_bf16_f32 v116, v116, v117
v_accvgpr_write_b32 a242, v116
v_accvgpr_read_b32 v118, a246
v_accvgpr_read_b32 v119, a247
v_mul_f32 v118, v118, v115
v_mfma_f32_32x32x16_bf16 v[144:159], v[88:91], a[116:119], v[144:159]
v_mul_f32 v119, v119, v115
v_cvt_pk_bf16_f32 v118, v118, v119
v_accvgpr_write_b32 a243, v118
v_accvgpr_read_b32 v116, a248
v_accvgpr_read_b32 v117, a249
v_mul_f32 v116, v116, v115
v_mul_f32 v117, v117, v115
v_cvt_pk_bf16_f32 v116, v116, v117
v_accvgpr_write_b32 a244, v116
v_accvgpr_read_b32 v118, a250
v_accvgpr_read_b32 v119, a251
v_mfma_f32_32x32x16_bf16 v[160:175], v[92:95], a[100:103], v[160:175]
s_add_u32 s65, s92, 0
buffer_load_dwordx4 a[40:43], v127, s[84:87], s65 offen offset:2048 sc1
buffer_load_dwordx4 a[44:47], v127, s[84:87], s65 offen offset:3072 sc1
s_add_u32 m0, s63, 33792
s_add_u32 s66, s89, 8320
buffer_load_dwordx4 v126, s[52:55], s66 offen offset:0 lds
ds_read_b64_tr_b16 v[76:77], v105 offset:2560
ds_read_b64_tr_b16 v[78:79], v105 offset:3584
v_mul_f32 v118, v118, v115
v_mul_f32 v119, v119, v115
v_cvt_pk_bf16_f32 v118, v118, v119
v_accvgpr_write_b32 a245, v118
v_mfma_f32_32x32x16_bf16 v[176:191], v[92:95], a[116:119], v[176:191]
v_accvgpr_read_b32 v116, a252
v_accvgpr_read_b32 v117, a253
v_mul_f32 v116, v116, v115
v_mul_f32 v117, v117, v115
v_cvt_pk_bf16_f32 v116, v116, v117
v_accvgpr_write_b32 a246, v116
v_accvgpr_read_b32 v118, a254
v_accvgpr_read_b32 v119, a255
v_mul_f32 v118, v118, v115
v_mul_f32 v119, v119, v115
v_cvt_pk_bf16_f32 v118, v118, v119
s_waitcnt lgkmcnt(6)
v_mfma_f32_32x32x16_bf16 v[192:207], v[64:67], a[0:3], 0
ds_read_b64_tr_b16 v[80:81], v105 offset:4096
ds_read_b64_tr_b16 v[82:83], v105 offset:5120
v_accvgpr_write_b32 a247, v118
s_cmp_lg_u32 s90, 0
s_cbranch_scc1 L_kx13
s_add_u32 s65, s91, 22528
buffer_store_dwordx4 a[224:227], v127, s[84:87], s65 offen offset:0 sc1
buffer_store_dwordx4 a[228:231], v127, s[84:87], s65 offen offset:1024 sc1
s_add_u32 s65, s91, 30720
s_mov_b64 exec, s[48:49]
buffer_store_dwordx4 a[240:243], v127, s[84:87], s65 offen offset:0 sc1
buffer_store_dwordx4 a[244:247], v127, s[84:87], s65 offen offset:1024 sc1
s_mov_b64 exec, -1
s_branch L_dx13
L_kx13:
ds_write_b128 v123, a[224:227] offset:6144
ds_write_b128 v123, a[228:231] offset:7168
ds_write_b128 v123, a[240:243] offset:14336
ds_write_b128 v123, a[244:247] offset:15360
buffer_store_dword v127, off, s[44:47], 0
buffer_store_dword v127, off, s[44:47], 0
buffer_store_dword v127, off, s[44:47], 0
buffer_store_dword v127, off, s[44:47], 0
L_dx13:
v_mfma_f32_32x32x16_bf16 v[208:223], v[64:67], a[16:19], 0
s_add_u32 m0, s63, 34816
s_add_u32 s66, s89, 16512
buffer_load_dwordx4 v126, s[52:55], s66 offen offset:0 lds
s_waitcnt lgkmcnt(6)
v_mfma_f32_32x32x16_bf16 v[224:239], v[68:71], a[0:3], 0
s_add_u32 s65, s92, 8192
buffer_load_dwordx4 a[24:27], v127, s[84:87], s65 offen offset:0 sc1
buffer_load_dwordx4 a[28:31], v127, s[84:87], s65 offen offset:1024 sc1
ds_read_b64_tr_b16 v[84:85], v105 offset:4608
ds_read_b64_tr_b16 v[86:87], v105 offset:5632
v_mfma_f32_32x32x16_bf16 v[240:255], v[68:71], a[16:19], 0
s_waitcnt lgkmcnt(6)
v_mfma_f32_32x32x16_bf16 v[192:207], v[72:75], a[4:7], v[192:207]
s_add_u32 m0, s63, 35840
s_add_u32 s66, s89, 24704
buffer_load_dwordx4 v126, s[52:55], s66 offen offset:0 lds
ds_read_b64_tr_b16 v[88:89], v105 offset:6144
ds_read_b64_tr_b16 v[90:91], v105 offset:7168
v_mfma_f32_32x32x16_bf16 v[208:223], v[72:75], a[20:23], v[208:223]
s_waitcnt lgkmcnt(6)
v_mfma_f32_32x32x16_bf16 v[224:239], v[76:79], a[4:7], v[224:239]
s_add_u32 s65, s92, 8192
buffer_load_dwordx4 a[56:59], v127, s[84:87], s65 offen offset:2048 sc1
buffer_load_dwordx4 a[60:63], v127, s[84:87], s65 offen offset:3072 sc1
ds_read_b64_tr_b16 v[92:93], v105 offset:6656
ds_read_b64_tr_b16 v[94:95], v105 offset:7680
v_mfma_f32_32x32x16_bf16 v[240:255], v[76:79], a[20:23], v[240:255]
s_add_u32 m0, s63, 36864
s_add_u32 s66, s89, 32896
buffer_load_dwordx4 v126, s[52:55], s66 offen offset:0 lds
s_waitcnt lgkmcnt(6)
v_mfma_f32_32x32x16_bf16 v[192:207], v[80:83], a[32:35], v[192:207]
ds_read_b64_tr_b16 v[64:65], v105 offset:8192
ds_read_b64_tr_b16 v[66:67], v105 offset:9216
v_mfma_f32_32x32x16_bf16 v[208:223], v[80:83], a[48:51], v[208:223]
s_waitcnt lgkmcnt(6)
v_mfma_f32_32x32x16_bf16 v[224:239], v[84:87], a[32:35], v[224:239]
s_add_u32 s65, s92, 4096
buffer_load_dwordx4 a[72:75], v127, s[84:87], s65 offen offset:0 sc1
buffer_load_dwordx4 a[76:79], v127, s[84:87], s65 offen offset:1024 sc1
s_add_u32 m0, s63, 37888
s_add_u32 s66, s89, 41088
buffer_load_dwordx4 v126, s[52:55], s66 offen offset:0 lds
v_mfma_f32_32x32x16_bf16 v[240:255], v[84:87], a[48:51], v[240:255]
ds_read_b64_tr_b16 v[68:69], v105 offset:8704
ds_read_b64_tr_b16 v[70:71], v105 offset:9728
s_waitcnt lgkmcnt(6)
v_mfma_f32_32x32x16_bf16 v[192:207], v[88:91], a[36:39], v[192:207]
ds_read_b64_tr_b16 v[72:73], v105 offset:10240
ds_read_b64_tr_b16 v[74:75], v105 offset:11264
v_mfma_f32_32x32x16_bf16 v[208:223], v[88:91], a[52:55], v[208:223]
s_add_u32 m0, s63, 38912
s_add_u32 s66, s89, 49280
buffer_load_dwordx4 v126, s[52:55], s66 offen offset:0 lds
s_waitcnt lgkmcnt(6)
v_mfma_f32_32x32x16_bf16 v[224:239], v[92:95], a[36:39], v[224:239]
s_add_u32 s65, s92, 4096
buffer_load_dwordx4 a[104:107], v127, s[84:87], s65 offen offset:2048 sc1
buffer_load_dwordx4 a[108:111], v127, s[84:87], s65 offen offset:3072 sc1
ds_read_b64_tr_b16 v[76:77], v105 offset:10752
ds_read_b64_tr_b16 v[78:79], v105 offset:11776
v_mfma_f32_32x32x16_bf16 v[240:255], v[92:95], a[52:55], v[240:255]
s_waitcnt lgkmcnt(6)
v_mfma_f32_32x32x16_bf16 v[192:207], v[64:67], a[64:67], v[192:207]
s_add_u32 m0, s63, 39936
s_add_u32 s66, s89, 57472
buffer_load_dwordx4 v126, s[52:55], s66 offen offset:0 lds
ds_read_b64_tr_b16 v[80:81], v105 offset:12288
ds_read_b64_tr_b16 v[82:83], v105 offset:13312
v_mfma_f32_32x32x16_bf16 v[208:223], v[64:67], a[80:83], v[208:223]
s_waitcnt lgkmcnt(6)
v_mfma_f32_32x32x16_bf16 v[224:239], v[68:71], a[64:67], v[224:239]
s_add_u32 s65, s92, 12288
buffer_load_dwordx4 a[88:91], v127, s[84:87], s65 offen offset:0 sc1
buffer_load_dwordx4 a[92:95], v127, s[84:87], s65 offen offset:1024 sc1
ds_read_b64_tr_b16 v[84:85], v105 offset:12800
ds_read_b64_tr_b16 v[86:87], v105 offset:13824
v_mfma_f32_32x32x16_bf16 v[240:255], v[68:71], a[80:83], v[240:255]
s_waitcnt lgkmcnt(6)
v_mfma_f32_32x32x16_bf16 v[192:207], v[72:75], a[68:71], v[192:207]
ds_read_b64_tr_b16 v[88:89], v105 offset:14336
ds_read_b64_tr_b16 v[90:91], v105 offset:15360
v_mfma_f32_32x32x16_bf16 v[208:223], v[72:75], a[84:87], v[208:223]
s_waitcnt lgkmcnt(6)
v_mfma_f32_32x32x16_bf16 v[224:239], v[76:79], a[68:71], v[224:239]
s_add_u32 s65, s92, 12288
buffer_load_dwordx4 a[120:123], v127, s[84:87], s65 offen offset:2048 sc1
buffer_load_dwordx4 a[124:127], v127, s[84:87], s65 offen offset:3072 sc1
ds_read_b64_tr_b16 v[92:93], v105 offset:14848
ds_read_b64_tr_b16 v[94:95], v105 offset:15872
v_add_u32 v105, 0xfffe0000, v105
v_mfma_f32_32x32x16_bf16 v[240:255], v[76:79], a[84:87], v[240:255]
s_waitcnt lgkmcnt(6)
v_mfma_f32_32x32x16_bf16 v[192:207], v[80:83], a[96:99], v[192:207]
v_mfma_f32_32x32x16_bf16 v[208:223], v[80:83], a[112:115], v[208:223]
s_waitcnt vmcnt(32)
s_waitcnt lgkmcnt(0)
s_barrier
v_mfma_f32_32x32x16_bf16 v[224:239], v[84:87], a[96:99], v[224:239]
ds_read_b64_tr_b16 v[64:65], v105 offset:0
ds_read_b64_tr_b16 v[66:67], v105 offset:1024
ds_read_b64_tr_b16 v[68:69], v105 offset:512
ds_read_b64_tr_b16 v[70:71], v105 offset:1536
v_mfma_f32_32x32x16_bf16 v[240:255], v[84:87], a[112:115], v[240:255]
s_add_u32 m0, s63, 98304
s_add_u32 s66, s89, 256
buffer_load_dwordx4 v126, s[52:55], s66 offen offset:0 lds
ds_read_b128 a[128:131], v123 offset:0
ds_read_b128 a[144:147], v123 offset:8192
v_mfma_f32_32x32x16_bf16 v[192:207], v[88:91], a[100:103], v[192:207]
ds_read_b64_tr_b16 v[72:73], v105 offset:2048
ds_read_b64_tr_b16 v[74:75], v105 offset:3072
ds_read_b128 a[132:135], v123 offset:1024
ds_read_b128 a[148:151], v123 offset:9216
v_mfma_f32_32x32x16_bf16 v[208:223], v[88:91], a[116:119], v[208:223]
v_mfma_f32_32x32x16_bf16 v[224:239], v[92:95], a[100:103], v[224:239]
s_add_u32 m0, s63, 99328
s_add_u32 s66, s89, 8448
buffer_load_dwordx4 v126, s[52:55], s66 offen offset:0 lds
ds_read_b64_tr_b16 v[76:77], v105 offset:2560
ds_read_b64_tr_b16 v[78:79], v105 offset:3584
ds_read_b128 a[160:163], v123 offset:2048
v_mfma_f32_32x32x16_bf16 v[240:255], v[92:95], a[116:119], v[240:255]
s_waitcnt vmcnt(14)
ds_read_b128 a[176:179], v123 offset:10240
ds_read_b128 a[164:167], v123 offset:3072
ds_read_b128 a[180:183], v123 offset:11264
s_waitcnt lgkmcnt(14)
v_mfma_f32_32x32x16_bf16 v[0:15], v[64:67], a[8:11], v[0:15]
ds_read_b64_tr_b16 v[80:81], v105 offset:4096
ds_read_b64_tr_b16 v[82:83], v105 offset:5120
v_mfma_f32_32x32x16_bf16 v[16:31], v[64:67], a[24:27], v[16:31]
s_add_u32 m0, s63, 100352
s_add_u32 s66, s89, 16640
buffer_load_dwordx4 v126, s[52:55], s66 offen offset:0 lds
ds_read_b128 a[192:195], v123 offset:4096
ds_read_b128 a[208:211], v123 offset:12288
s_waitcnt lgkmcnt(15)
v_mfma_f32_32x32x16_bf16 v[32:47], v[68:71], a[8:11], v[32:47]
ds_read_b64_tr_b16 v[84:85], v105 offset:4608
ds_read_b64_tr_b16 v[86:87], v105 offset:5632
ds_read_b128 a[196:199], v123 offset:5120
ds_read_b128 a[212:215], v123 offset:13312
v_mfma_f32_32x32x16_bf16 v[48:63], v[68:71], a[24:27], v[48:63]
s_waitcnt lgkmcnt(15)
v_mfma_f32_32x32x16_bf16 v[0:15], v[72:75], a[12:15], v[0:15]
s_add_u32 m0, s63, 101376
s_add_u32 s66, s89, 24832
buffer_load_dwordx4 v126, s[52:55], s66 offen offset:0 lds
ds_read_b64_tr_b16 v[88:89], v105 offset:6144
ds_read_b64_tr_b16 v[90:91], v105 offset:7168
ds_read_b128 a[224:227], v123 offset:6144
v_mfma_f32_32x32x16_bf16 v[16:31], v[72:75], a[28:31], v[16:31]
ds_read_b128 a[240:243], v123 offset:14336
ds_read_b128 a[228:231], v123 offset:7168
ds_read_b128 a[244:247], v123 offset:15360
s_waitcnt lgkmcnt(15)
v_mfma_f32_32x32x16_bf16 v[32:47], v[76:79], a[12:15], v[32:47]
ds_read_b64_tr_b16 v[92:93], v105 offset:6656
ds_read_b64_tr_b16 v[94:95], v105 offset:7680
v_mfma_f32_32x32x16_bf16 v[48:63], v[76:79], a[28:31], v[48:63]
s_add_u32 m0, s63, 102400
s_add_u32 s66, s89, 33024
buffer_load_dwordx4 v126, s[52:55], s66 offen offset:0 lds
s_waitcnt lgkmcnt(14)
v_mfma_f32_32x32x16_bf16 v[0:15], v[80:83], a[40:43], v[0:15]
ds_read_b64_tr_b16 v[64:65], v105 offset:8192
ds_read_b64_tr_b16 v[66:67], v105 offset:9216
v_mfma_f32_32x32x16_bf16 v[16:31], v[80:83], a[56:59], v[16:31]
s_waitcnt lgkmcnt(12)
v_mfma_f32_32x32x16_bf16 v[32:47], v[84:87], a[40:43], v[32:47]
s_add_u32 m0, s63, 103424
s_add_u32 s66, s89, 41216
buffer_load_dwordx4 v126, s[52:55], s66 offen offset:0 lds
ds_read_b64_tr_b16 v[68:69], v105 offset:8704
ds_read_b64_tr_b16 v[70:71], v105 offset:9728
v_mfma_f32_32x32x16_bf16 v[48:63], v[84:87], a[56:59], v[48:63]
s_waitcnt lgkmcnt(10)
v_mfma_f32_32x32x16_bf16 v[0:15], v[88:91], a[44:47], v[0:15]
ds_read_b64_tr_b16 v[72:73], v105 offset:10240
ds_read_b64_tr_b16 v[74:75], v105 offset:11264
v_mfma_f32_32x32x16_bf16 v[16:31], v[88:91], a[60:63], v[16:31]
s_add_u32 m0, s63, 104448
s_add_u32 s66, s89, 49408
buffer_load_dwordx4 v126, s[52:55], s66 offen offset:0 lds
s_waitcnt lgkmcnt(6)
v_mfma_f32_32x32x16_bf16 v[32:47], v[92:95], a[44:47], v[32:47]
s_cmp_lg_u32 s90, 0
s_cbranch_scc1 L_kf1
s_waitcnt vmcnt(19)
buffer_store_dword v125, off, s[76:79], s93 offset:4 sc1
s_branch L_df1
L_kf1:
buffer_store_dword v127, off, s[44:47], 0
L_df1:
ds_read_b64_tr_b16 v[76:77], v105 offset:10752
v_mfma_f32_32x32x16_bf16 v[48:63], v[92:95], a[60:63], v[48:63]
s_waitcnt vmcnt(8)
ds_read_b64_tr_b16 v[78:79], v105 offset:11776
s_waitcnt lgkmcnt(6)
v_mfma_f32_32x32x16_bf16 v[0:15], v[64:67], a[72:75], v[0:15]
s_add_u32 m0, s63, 105472
s_add_u32 s66, s89, 57600
buffer_load_dwordx4 v126, s[52:55], s66 offen offset:0 lds
ds_read_b64_tr_b16 v[80:81], v105 offset:12288
ds_read_b64_tr_b16 v[82:83], v105 offset:13312
v_mfma_f32_32x32x16_bf16 v[16:31], v[64:67], a[88:91], v[16:31]
s_waitcnt lgkmcnt(6)
v_mfma_f32_32x32x16_bf16 v[32:47], v[68:71], a[72:75], v[32:47]
ds_read_b64_tr_b16 v[84:85], v105 offset:12800
ds_read_b64_tr_b16 v[86:87], v105 offset:13824
v_mfma_f32_32x32x16_bf16 v[48:63], v[68:71], a[88:91], v[48:63]
s_waitcnt lgkmcnt(6)
v_mfma_f32_32x32x16_bf16 v[0:15], v[72:75], a[76:79], v[0:15]
ds_read_b64_tr_b16 v[88:89], v105 offset:14336
ds_read_b64_tr_b16 v[90:91], v105 offset:15360
v_mfma_f32_32x32x16_bf16 v[16:31], v[72:75], a[92:95], v[16:31]
s_waitcnt lgkmcnt(6)
v_mfma_f32_32x32x16_bf16 v[32:47], v[76:79], a[76:79], v[32:47]
ds_read_b64_tr_b16 v[92:93], v105 offset:14848
ds_read_b64_tr_b16 v[94:95], v105 offset:15872
v_add_u32 v105, 0x8000, v105
v_mfma_f32_32x32x16_bf16 v[48:63], v[76:79], a[92:95], v[48:63]
s_waitcnt lgkmcnt(6)
v_mfma_f32_32x32x16_bf16 v[0:15], v[80:83], a[104:107], v[0:15]
v_mfma_f32_32x32x16_bf16 v[16:31], v[80:83], a[120:123], v[16:31]
s_waitcnt lgkmcnt(0)
s_barrier
v_mfma_f32_32x32x16_bf16 v[32:47], v[84:87], a[104:107], v[32:47]
ds_read_b64_tr_b16 v[64:65], v105 offset:0
ds_read_b64_tr_b16 v[66:67], v105 offset:1024
ds_read_b64_tr_b16 v[68:69], v105 offset:512
ds_read_b64_tr_b16 v[70:71], v105 offset:1536
v_mfma_f32_32x32x16_bf16 v[48:63], v[84:87], a[120:123], v[48:63]
s_add_u32 m0, s63, 131072
s_add_u32 s66, s88, 131072
buffer_load_dwordx4 v126, s[52:55], s66 offen offset:0 lds
v_mfma_f32_32x32x16_bf16 v[0:15], v[88:91], a[108:111], v[0:15]
s_add_u32 m0, s63, 132096
s_add_u32 s66, s88, 139264
buffer_load_dwordx4 v126, s[52:55], s66 offen offset:0 lds
ds_read_b64_tr_b16 v[72:73], v105 offset:2048
ds_read_b64_tr_b16 v[74:75], v105 offset:3072
v_mfma_f32_32x32x16_bf16 v[16:31], v[88:91], a[124:127], v[16:31]
v_mfma_f32_32x32x16_bf16 v[32:47], v[92:95], a[108:111], v[32:47]
s_add_u32 m0, s63, 133120
s_add_u32 s66, s88, 147456
buffer_load_dwordx4 v126, s[52:55], s66 offen offset:0 lds
ds_read_b64_tr_b16 v[76:77], v105 offset:2560
ds_read_b64_tr_b16 v[78:79], v105 offset:3584
v_mfma_f32_32x32x16_bf16 v[48:63], v[92:95], a[124:127], v[48:63]
s_waitcnt lgkmcnt(6)
v_mfma_f32_32x32x16_bf16 v[128:143], v[64:67], a[8:11], v[128:143]
s_add_u32 m0, s63, 134144
s_add_u32 s66, s88, 155648
buffer_load_dwordx4 v126, s[52:55], s66 offen offset:0 lds
ds_read_b64_tr_b16 v[80:81], v105 offset:4096
ds_read_b64_tr_b16 v[82:83], v105 offset:5120
v_mfma_f32_32x32x16_bf16 v[144:159], v[64:67], a[24:27], v[144:159]
s_waitcnt lgkmcnt(6)
v_mfma_f32_32x32x16_bf16 v[160:175], v[68:71], a[8:11], v[160:175]
s_add_u32 m0, s63, 135168
s_add_u32 s66, s88, 163840
buffer_load_dwordx4 v126, s[52:55], s66 offen offset:0 lds
ds_read_b64_tr_b16 v[84:85], v105 offset:4608
ds_read_b64_tr_b16 v[86:87], v105 offset:5632
v_mfma_f32_32x32x16_bf16 v[176:191], v[68:71], a[24:27], v[176:191]
s_add_u32 m0, s63, 136192
s_add_u32 s66, s88, 172032
buffer_load_dwordx4 v126, s[52:55], s66 offen offset:0 lds
s_waitcnt lgkmcnt(6)
v_mfma_f32_32x32x16_bf16 v[128:143], v[72:75], a[12:15], v[128:143]
ds_read_b64_tr_b16 v[88:89], v105 offset:6144
ds_read_b64_tr_b16 v[90:91], v105 offset:7168
v_mfma_f32_32x32x16_bf16 v[144:159], v[72:75], a[28:31], v[144:159]
s_add_u32 m0, s63, 137216
s_add_u32 s66, s88, 180224
buffer_load_dwordx4 v126, s[52:55], s66 offen offset:0 lds
s_waitcnt lgkmcnt(6)
v_mfma_f32_32x32x16_bf16 v[160:175], v[76:79], a[12:15], v[160:175]
ds_read_b64_tr_b16 v[92:93], v105 offset:6656
ds_read_b64_tr_b16 v[94:95], v105 offset:7680
v_mfma_f32_32x32x16_bf16 v[176:191], v[76:79], a[28:31], v[176:191]
s_add_u32 m0, s63, 138240
s_add_u32 s66, s88, 188416
buffer_load_dwordx4 v126, s[52:55], s66 offen offset:0 lds
s_waitcnt lgkmcnt(6)
v_mfma_f32_32x32x16_bf16 v[128:143], v[80:83], a[40:43], v[128:143]
ds_read_b64_tr_b16 v[64:65], v105 offset:8192
ds_read_b64_tr_b16 v[66:67], v105 offset:9216
v_mfma_f32_32x32x16_bf16 v[144:159], v[80:83], a[56:59], v[144:159]
s_add_u32 m0, s63, 0
s_add_u32 s66, s89, 131072
buffer_load_dwordx4 v126, s[52:55], s66 offen offset:0 lds
s_waitcnt lgkmcnt(6)
v_mfma_f32_32x32x16_bf16 v[160:175], v[84:87], a[40:43], v[160:175]
ds_read_b64_tr_b16 v[68:69], v105 offset:8704
ds_read_b64_tr_b16 v[70:71], v105 offset:9728
v_mfma_f32_32x32x16_bf16 v[176:191], v[84:87], a[56:59], v[176:191]
s_add_u32 m0, s63, 1024
s_add_u32 s66, s89, 139264
buffer_load_dwordx4 v126, s[52:55], s66 offen offset:0 lds
s_waitcnt lgkmcnt(6)
v_mfma_f32_32x32x16_bf16 v[128:143], v[88:91], a[44:47], v[128:143]
ds_read_b64_tr_b16 v[72:73], v105 offset:10240
ds_read_b64_tr_b16 v[74:75], v105 offset:11264
v_mfma_f32_32x32x16_bf16 v[144:159], v[88:91], a[60:63], v[144:159]
s_add_u32 m0, s63, 2048
s_add_u32 s66, s89, 147456
buffer_load_dwordx4 v126, s[52:55], s66 offen offset:0 lds
s_waitcnt lgkmcnt(6)
v_mfma_f32_32x32x16_bf16 v[160:175], v[92:95], a[44:47], v[160:175]
ds_read_b64_tr_b16 v[76:77], v105 offset:10752
ds_read_b64_tr_b16 v[78:79], v105 offset:11776
v_mfma_f32_32x32x16_bf16 v[176:191], v[92:95], a[60:63], v[176:191]
s_add_u32 m0, s63, 3072
s_add_u32 s66, s89, 155648
buffer_load_dwordx4 v126, s[52:55], s66 offen offset:0 lds
s_waitcnt lgkmcnt(6)
v_mfma_f32_32x32x16_bf16 v[128:143], v[64:67], a[72:75], v[128:143]
ds_read_b64_tr_b16 v[80:81], v105 offset:12288
ds_read_b64_tr_b16 v[82:83], v105 offset:13312
v_mfma_f32_32x32x16_bf16 v[144:159], v[64:67], a[88:91], v[144:159]
s_add_u32 m0, s63, 4096
s_add_u32 s66, s89, 163840
buffer_load_dwordx4 v126, s[52:55], s66 offen offset:0 lds
s_waitcnt lgkmcnt(6)
v_mfma_f32_32x32x16_bf16 v[160:175], v[68:71], a[72:75], v[160:175]
ds_read_b64_tr_b16 v[84:85], v105 offset:12800
ds_read_b64_tr_b16 v[86:87], v105 offset:13824
v_mfma_f32_32x32x16_bf16 v[176:191], v[68:71], a[88:91], v[176:191]
s_add_u32 m0, s63, 5120
s_add_u32 s66, s89, 172032
buffer_load_dwordx4 v126, s[52:55], s66 offen offset:0 lds
s_waitcnt lgkmcnt(6)
v_mfma_f32_32x32x16_bf16 v[128:143], v[72:75], a[76:79], v[128:143]
ds_read_b64_tr_b16 v[88:89], v105 offset:14336
ds_read_b64_tr_b16 v[90:91], v105 offset:15360
v_mfma_f32_32x32x16_bf16 v[144:159], v[72:75], a[92:95], v[144:159]
s_add_u32 m0, s63, 6144
s_add_u32 s66, s89, 180224
buffer_load_dwordx4 v126, s[52:55], s66 offen offset:0 lds
s_waitcnt lgkmcnt(6)
v_mfma_f32_32x32x16_bf16 v[160:175], v[76:79], a[76:79], v[160:175]
ds_read_b64_tr_b16 v[92:93], v105 offset:14848
ds_read_b64_tr_b16 v[94:95], v105 offset:15872
v_add_u32 v105, 0x10000, v105
v_mfma_f32_32x32x16_bf16 v[176:191], v[76:79], a[92:95], v[176:191]
s_add_u32 m0, s63, 7168
s_add_u32 s66, s89, 188416
buffer_load_dwordx4 v126, s[52:55], s66 offen offset:0 lds
s_waitcnt lgkmcnt(6)
v_mfma_f32_32x32x16_bf16 v[128:143], v[80:83], a[104:107], v[128:143]
v_mfma_f32_32x32x16_bf16 v[144:159], v[80:83], a[120:123], v[144:159]
s_waitcnt vmcnt(16)
s_waitcnt lgkmcnt(0)
s_barrier
v_mfma_f32_32x32x16_bf16 v[160:175], v[84:87], a[104:107], v[160:175]
ds_read_b64_tr_b16 v[64:65], v105 offset:0
ds_read_b64_tr_b16 v[66:67], v105 offset:1024
ds_read_b64_tr_b16 v[68:69], v105 offset:512
ds_read_b64_tr_b16 v[70:71], v105 offset:1536
v_mfma_f32_32x32x16_bf16 v[176:191], v[84:87], a[120:123], v[176:191]
s_add_u32 m0, s63, 32768
s_add_u32 s66, s88, 131200
buffer_load_dwordx4 v126, s[52:55], s66 offen offset:0 lds
v_mfma_f32_32x32x16_bf16 v[128:143], v[88:91], a[108:111], v[128:143]
s_add_u32 m0, s63, 33792
s_add_u32 s66, s88, 139392
buffer_load_dwordx4 v126, s[52:55], s66 offen offset:0 lds
ds_read_b64_tr_b16 v[72:73], v105 offset:2048
ds_read_b64_tr_b16 v[74:75], v105 offset:3072
v_mfma_f32_32x32x16_bf16 v[144:159], v[88:91], a[124:127], v[144:159]
v_mfma_f32_32x32x16_bf16 v[160:175], v[92:95], a[108:111], v[160:175]
s_add_u32 m0, s63, 34816
s_add_u32 s66, s88, 147584
buffer_load_dwordx4 v126, s[52:55], s66 offen offset:0 lds
ds_read_b64_tr_b16 v[76:77], v105 offset:2560
ds_read_b64_tr_b16 v[78:79], v105 offset:3584
v_mfma_f32_32x32x16_bf16 v[176:191], v[92:95], a[124:127], v[176:191]
s_waitcnt lgkmcnt(6)
v_mfma_f32_32x32x16_bf16 v[192:207], v[64:67], a[8:11], v[192:207]
s_add_u32 m0, s63, 35840
s_add_u32 s66, s88, 155776
buffer_load_dwordx4 v126, s[52:55], s66 offen offset:0 lds
ds_read_b64_tr_b16 v[80:81], v105 offset:4096
ds_read_b64_tr_b16 v[82:83], v105 offset:5120
v_mfma_f32_32x32x16_bf16 v[208:223], v[64:67], a[24:27], v[208:223]
buffer_load_dword v120, off, s[76:79], s94 offset:4 sc1
s_waitcnt lgkmcnt(6)
v_mfma_f32_32x32x16_bf16 v[224:239], v[68:71], a[8:11], v[224:239]
s_add_u32 m0, s63, 36864
s_add_u32 s66, s88, 163968
buffer_load_dwordx4 v126, s[52:55], s66 offen offset:0 lds
ds_read_b64_tr_b16 v[84:85], v105 offset:4608
ds_read_b64_tr_b16 v[86:87], v105 offset:5632
v_mfma_f32_32x32x16_bf16 v[240:255], v[68:71], a[24:27], v[240:255]
s_add_u32 m0, s63, 37888
s_add_u32 s66, s88, 172160
buffer_load_dwordx4 v126, s[52:55], s66 offen offset:0 lds
s_waitcnt lgkmcnt(6)
v_mfma_f32_32x32x16_bf16 v[192:207], v[72:75], a[12:15], v[192:207]
ds_read_b64_tr_b16 v[88:89], v105 offset:6144
ds_read_b64_tr_b16 v[90:91], v105 offset:7168
v_mfma_f32_32x32x16_bf16 v[208:223], v[72:75], a[28:31], v[208:223]
s_add_u32 m0, s63, 38912
s_add_u32 s66, s88, 180352
buffer_load_dwordx4 v126, s[52:55], s66 offen offset:0 lds
s_waitcnt lgkmcnt(6)
v_mfma_f32_32x32x16_bf16 v[224:239], v[76:79], a[12:15], v[224:239]
ds_read_b64_tr_b16 v[92:93], v105 offset:6656
ds_read_b64_tr_b16 v[94:95], v105 offset:7680
v_mfma_f32_32x32x16_bf16 v[240:255], v[76:79], a[28:31], v[240:255]
s_add_u32 m0, s63, 39936
s_add_u32 s66, s88, 188544
buffer_load_dwordx4 v126, s[52:55], s66 offen offset:0 lds
s_waitcnt lgkmcnt(6)
v_mfma_f32_32x32x16_bf16 v[192:207], v[80:83], a[40:43], v[192:207]
ds_read_b64_tr_b16 v[64:65], v105 offset:8192
ds_read_b64_tr_b16 v[66:67], v105 offset:9216
v_mfma_f32_32x32x16_bf16 v[208:223], v[80:83], a[56:59], v[208:223]
s_waitcnt lgkmcnt(6)
v_mfma_f32_32x32x16_bf16 v[224:239], v[84:87], a[40:43], v[224:239]
ds_read_b64_tr_b16 v[68:69], v105 offset:8704
ds_read_b64_tr_b16 v[70:71], v105 offset:9728
v_mfma_f32_32x32x16_bf16 v[240:255], v[84:87], a[56:59], v[240:255]
s_waitcnt lgkmcnt(6)
v_mfma_f32_32x32x16_bf16 v[192:207], v[88:91], a[44:47], v[192:207]
ds_read_b64_tr_b16 v[72:73], v105 offset:10240
ds_read_b64_tr_b16 v[74:75], v105 offset:11264
v_mfma_f32_32x32x16_bf16 v[208:223], v[88:91], a[60:63], v[208:223]
s_waitcnt lgkmcnt(6)
v_mfma_f32_32x32x16_bf16 v[224:239], v[92:95], a[44:47], v[224:239]
ds_read_b64_tr_b16 v[76:77], v105 offset:10752
ds_read_b64_tr_b16 v[78:79], v105 offset:11776
v_mfma_f32_32x32x16_bf16 v[240:255], v[92:95], a[60:63], v[240:255]
s_waitcnt lgkmcnt(6)
v_mfma_f32_32x32x16_bf16 v[192:207], v[64:67], a[72:75], v[192:207]
ds_read_b64_tr_b16 v[80:81], v105 offset:12288
ds_read_b64_tr_b16 v[82:83], v105 offset:13312
v_mfma_f32_32x32x16_bf16 v[208:223], v[64:67], a[88:91], v[208:223]
s_waitcnt lgkmcnt(6)
v_mfma_f32_32x32x16_bf16 v[224:239], v[68:71], a[72:75], v[224:239]
ds_read_b64_tr_b16 v[84:85], v105 offset:12800
ds_read_b64_tr_b16 v[86:87], v105 offset:13824
v_mfma_f32_32x32x16_bf16 v[240:255], v[68:71], a[88:91], v[240:255]
s_waitcnt lgkmcnt(6)
v_mfma_f32_32x32x16_bf16 v[192:207], v[72:75], a[76:79], v[192:207]
ds_read_b64_tr_b16 v[88:89], v105 offset:14336
ds_read_b64_tr_b16 v[90:91], v105 offset:15360
v_mfma_f32_32x32x16_bf16 v[208:223], v[72:75], a[92:95], v[208:223]
s_waitcnt lgkmcnt(6)
v_mfma_f32_32x32x16_bf16 v[224:239], v[76:79], a[76:79], v[224:239]
ds_read_b64_tr_b16 v[92:93], v105 offset:14848
ds_read_b64_tr_b16 v[94:95], v105 offset:15872
v_add_u32 v105, 0x8000, v105
v_mfma_f32_32x32x16_bf16 v[240:255], v[76:79], a[92:95], v[240:255]
s_waitcnt lgkmcnt(6)
v_mfma_f32_32x32x16_bf16 v[192:207], v[80:83], a[104:107], v[192:207]
v_mfma_f32_32x32x16_bf16 v[208:223], v[80:83], a[120:123], v[208:223]
s_waitcnt vmcnt(17)
s_waitcnt vmcnt(4)
v_readfirstlane_b32 s65, v120
s_xor_b32 s65, s65, s96
s_or_b32 s65, s65, s90
s_cbranch_scc0 L_sp1
L_spd1:
s_waitcnt lgkmcnt(0)
s_barrier
v_mfma_f32_32x32x16_bf16 v[224:239], v[84:87], a[104:107], v[224:239]
s_add_u32 s65, s92, 16384
buffer_load_dwordx4 a[136:139], v127, s[84:87], s65 offen offset:0 sc1
buffer_load_dwordx4 a[140:143], v127, s[84:87], s65 offen offset:1024 sc1
ds_read_b64_tr_b16 v[64:65], v105 offset:0
ds_read_b64_tr_b16 v[66:67], v105 offset:1024
ds_read_b64_tr_b16 v[68:69], v105 offset:512
v_mfma_f32_32x32x16_bf16 v[240:255], v[84:87], a[120:123], v[240:255]
s_add_u32 m0, s63, 98304
s_add_u32 s66, s89, 131200
buffer_load_dwordx4 v126, s[52:55], s66 offen offset:0 lds
ds_read_b64_tr_b16 v[70:71], v105 offset:1536
v_mfma_f32_32x32x16_bf16 v[192:207], v[88:91], a[108:111], v[192:207]
s_add_u32 m0, s63, 99328
s_add_u32 s66, s89, 139392
buffer_load_dwordx4 v126, s[52:55], s66 offen offset:0 lds
ds_read_b64_tr_b16 v[72:73], v105 offset:2048
ds_read_b64_tr_b16 v[74:75], v105 offset:3072
v_mfma_f32_32x32x16_bf16 v[208:223], v[88:91], a[124:127], v[208:223]
v_mfma_f32_32x32x16_bf16 v[224:239], v[92:95], a[108:111], v[224:239]
s_add_u32 s65, s92, 16384
buffer_load_dwordx4 a[168:171], v127, s[84:87], s65 offen offset:2048 sc1
buffer_load_dwordx4 a[172:175], v127, s[84:87], s65 offen offset:3072 sc1
s_add_u32 m0, s63, 100352
s_add_u32 s66, s89, 147584
buffer_load_dwordx4 v126, s[52:55], s66 offen offset:0 lds
v_mfma_f32_32x32x16_bf16 v[240:255], v[92:95], a[124:127], v[240:255]
s_cmp_lg_u32 s90, 0
s_cbranch_scc1 L_kdn
v_add_u32 v121, 1, v121
buffer_store_dword v121, off, s[76:79], s93 offset:8 sc1
s_branch L_ddn
L_kdn:
buffer_store_dword v127, off, s[44:47], 0
L_ddn:
s_waitcnt lgkmcnt(4)
v_mfma_f32_32x32x16_bf16 v[0:15], v[64:67], a[128:131], v[0:15]
s_add_u32 m0, s63, 101376
s_add_u32 s66, s89, 155776
buffer_load_dwordx4 v126, s[52:55], s66 offen offset:0 lds
ds_read_b64_tr_b16 v[76:77], v105 offset:2560
ds_read_b64_tr_b16 v[78:79], v105 offset:3584
ds_read_b64_tr_b16 v[80:81], v105 offset:4096
v_mfma_f32_32x32x16_bf16 v[16:31], v[64:67], a[144:147], v[16:31]
ds_read_b64_tr_b16 v[82:83], v105 offset:5120
s_waitcnt lgkmcnt(6)
v_mfma_f32_32x32x16_bf16 v[32:47], v[68:71], a[128:131], v[32:47]
s_add_u32 s65, s92, 24576
buffer_load_dwordx4 a[152:155], v127, s[84:87], s65 offen offset:0 sc1
buffer_load_dwordx4 a[156:159], v127, s[84:87], s65 offen offset:1024 sc1
s_add_u32 m0, s63, 102400
s_add_u32 s66, s89, 163968
buffer_load_dwordx4 v126, s[52:55], s66 offen offset:0 lds
v_mfma_f32_32x32x16_bf16 v[48:63], v[68:71], a[144:147], v[48:63]
s_add_u32 m0, s63, 103424
s_add_u32 s66, s89, 172160
buffer_load_dwordx4 v126, s[52:55], s66 offen offset:0 lds
ds_read_b64_tr_b16 v[84:85], v105 offset:4608
ds_read_b64_tr_b16 v[86:87], v105 offset:5632
s_waitcnt lgkmcnt(6)
v_mfma_f32_32x32x16_bf16 v[0:15], v[72:75], a[132:135], v[0:15]
ds_read_b64_tr_b16 v[88:89], v105 offset:6144
ds_read_b64_tr_b16 v[90:91], v105 offset:7168
v_mfma_f32_32x32x16_bf16 v[16:31], v[72:75], a[148:151], v[16:31]
s_add_u32 m0, s63, 104448
s_add_u32 s66, s89, 180352
buffer_load_dwordx4 v126, s[52:55], s66 offen offset:0 lds
s_waitcnt lgkmcnt(6)
v_mfma_f32_32x32x16_bf16 v[32:47], v[76:79], a[132:135], v[32:47]
s_add_u32 s65, s92, 24576
buffer_load_dwordx4 a[184:187], v127, s[84:87], s65 offen offset:2048 sc1
buffer_load_dwordx4 a[188:191], v127, s[84:87], s65 offen offset:3072 sc1
ds_read_b64_tr_b16 v[92:93], v105 offset:6656
ds_read_b64_tr_b16 v[94:95], v105 offset:7680
v_mfma_f32_32x32x16_bf16 v[48:63], v[76:79], a[148:151], v[48:63]
s_add_u32 m0, s63, 105472
s_add_u32 s66, s89, 188544
buffer_load_dwordx4 v126, s[52:55], s66 offen offset:0 lds
s_waitcnt lgkmcnt(6)
v_mfma_f32_32x32x16_bf16 v[0:15], v[80:83], a[160:163], v[0:15]
ds_read_b64_tr_b16 v[64:65], v105 offset:8192
ds_read_b64_tr_b16 v[66:67], v105 offset:9216
v_mfma_f32_32x32x16_bf16 v[16:31], v[80:83], a[176:179], v[16:31]
s_waitcnt lgkmcnt(6)
v_mfma_f32_32x32x16_bf16 v[32:47], v[84:87], a[160:163], v[32:47]
s_add_u32 s65, s92, 20480
buffer_load_dwordx4 a[200:203], v127, s[84:87], s65 offen offset:0 sc1
buffer_load_dwordx4 a[204:207], v127, s[84:87], s65 offen offset:1024 sc1
ds_read_b64_tr_b16 v[68:69], v105 offset:8704
ds_read_b64_tr_b16 v[70:71], v105 offset:9728
v_mfma_f32_32x32x16_bf16 v[48:63], v[84:87], a[176:179], v[48:63]
s_waitcnt lgkmcnt(6)
v_mfma_f32_32x32x16_bf16 v[0:15], v[88:91], a[164:167], v[0:15]
ds_read_b64_tr_b16 v[72:73], v105 offset:10240
ds_read_b64_tr_b16 v[74:75], v105 offset:11264
v_mfma_f32_32x32x16_bf16 v[16:31], v[88:91], a[180:183], v[16:31]
s_waitcnt lgkmcnt(6)
v_mfma_f32_32x32x16_bf16 v[32:47], v[92:95], a[164:167], v[32:47]
s_add_u32 s65, s92, 20480
buffer_load_dwordx4 a[232:235], v127, s[84:87], s65 offen offset:2048 sc1
buffer_load_dwordx4 a[236:239], v127, s[84:87], s65 offen offset:3072 sc1
ds_read_b64_tr_b16 v[76:77], v105 offset:10752
ds_read_b64_tr_b16 v[78:79], v105 offset:11776
v_mfma_f32_32x32x16_bf16 v[48:63], v[92:95], a[180:183], v[48:63]
s_waitcnt lgkmcnt(6)
v_mfma_f32_32x32x16_bf16 v[0:15], v[64:67], a[192:195], v[0:15]
ds_read_b64_tr_b16 v[80:81], v105 offset:12288
ds_read_b64_tr_b16 v[82:83], v105 offset:13312
v_mfma_f32_32x32x16_bf16 v[16:31], v[64:67], a[208:211], v[16:31]
s_waitcnt lgkmcnt(6)
v_mfma_f32_32x32x16_bf16 v[32:47], v[68:71], a[192:195], v[32:47]
s_add_u32 s65, s92, 28672
buffer_load_dwordx4 a[216:219], v127, s[84:87], s65 offen offset:0 sc1
buffer_load_dwordx4 a[220:223], v127, s[84:87], s65 offen offset:1024 sc1
ds_read_b64_tr_b16 v[84:85], v105 offset:12800
ds_read_b64_tr_b16 v[86:87], v105 offset:13824
v_mfma_f32_32x32x16_bf16 v[48:63], v[68:71], a[208:211], v[48:63]
s_waitcnt lgkmcnt(6)
v_mfma_f32_32x32x16_bf16 v[0:15], v[72:75], a[196:199], v[0:15]
ds_read_b64_tr_b16 v[88:89], v105 offset:14336
ds_read_b64_tr_b16 v[90:91], v105 offset:15360
v_mfma_f32_32x32x16_bf16 v[16:31], v[72:75], a[212:215], v[16:31]
s_waitcnt lgkmcnt(6)
v_mfma_f32_32x32x16_bf16 v[32:47], v[76:79], a[196:199], v[32:47]
s_add_u32 s65, s92, 28672
buffer_load_dwordx4 a[248:251], v127, s[84:87], s65 offen offset:2048 sc1
buffer_load_dwordx4 a[252:255], v127, s[84:87], s65 offen offset:3072 sc1
ds_read_b64_tr_b16 v[92:93], v105 offset:14848
ds_read_b64_tr_b16 v[94:95], v105 offset:15872
v_add_u32 v105, 0xfffe0000, v105
v_mfma_f32_32x32x16_bf16 v[48:63], v[76:79], a[212:215], v[48:63]
s_waitcnt lgkmcnt(6)
v_mfma_f32_32x32x16_bf16 v[0:15], v[80:83], a[224:227], v[0:15]
v_mfma_f32_32x32x16_bf16 v[16:31], v[80:83], a[240:243], v[16:31]
s_waitcnt lgkmcnt(0)
s_barrier
v_mfma_f32_32x32x16_bf16 v[32:47], v[84:87], a[224:227], v[32:47]
ds_read_b64_tr_b16 v[64:65], v105 offset:0
ds_read_b64_tr_b16 v[66:67], v105 offset:1024
ds_read_b64_tr_b16 v[68:69], v105 offset:512
ds_read_b64_tr_b16 v[70:71], v105 offset:1536
v_mfma_f32_32x32x16_bf16 v[48:63], v[84:87], a[240:243], v[48:63]
s_add_u32 m0, s63, 131072
s_add_u32 s66, s88, 131328
buffer_load_dwordx4 v126, s[52:55], s66 offen offset:0 lds
v_mfma_f32_32x32x16_bf16 v[0:15], v[88:91], a[228:231], v[0:15]
s_add_u32 m0, s63, 132096
s_add_u32 s66, s88, 139520
buffer_load_dwordx4 v126, s[52:55], s66 offen offset:0 lds
ds_read_b64_tr_b16 v[72:73], v105 offset:2048
ds_read_b64_tr_b16 v[74:75], v105 offset:3072
v_mfma_f32_32x32x16_bf16 v[16:31], v[88:91], a[244:247], v[16:31]
v_mfma_f32_32x32x16_bf16 v[32:47], v[92:95], a[228:231], v[32:47]
s_add_u32 m0, s63, 133120
s_add_u32 s66, s88, 147712
buffer_load_dwordx4 v126, s[52:55], s66 offen offset:0 lds
ds_read_b64_tr_b16 v[76:77], v105 offset:2560
ds_read_b64_tr_b16 v[78:79], v105 offset:3584
v_mfma_f32_32x32x16_bf16 v[48:63], v[92:95], a[244:247], v[48:63]
s_waitcnt vmcnt(12)
s_waitcnt lgkmcnt(6)
v_mfma_f32_32x32x16_bf16 v[0:15], v[64:67], a[136:139], v[0:15]
s_add_u32 m0, s63, 134144
s_add_u32 s66, s88, 155904
buffer_load_dwordx4 v126, s[52:55], s66 offen offset:0 lds
ds_read_b64_tr_b16 v[80:81], v105 offset:4096
ds_read_b64_tr_b16 v[82:83], v105 offset:5120
v_mfma_f32_32x32x16_bf16 v[16:31], v[64:67], a[152:155], v[16:31]
s_waitcnt lgkmcnt(6)
v_mfma_f32_32x32x16_bf16 v[32:47], v[68:71], a[136:139], v[32:47]
s_add_u32 m0, s63, 135168
s_add_u32 s66, s88, 164096
buffer_load_dwordx4 v126, s[52:55], s66 offen offset:0 lds
ds_read_b64_tr_b16 v[84:85], v105 offset:4608
ds_read_b64_tr_b16 v[86:87], v105 offset:5632
v_mfma_f32_32x32x16_bf16 v[48:63], v[68:71], a[152:155], v[48:63]
s_add_u32 m0, s63, 136192
s_add_u32 s66, s88, 172288
buffer_load_dwordx4 v126, s[52:55], s66 offen offset:0 lds
s_waitcnt lgkmcnt(6)
v_mfma_f32_32x32x16_bf16 v[0:15], v[72:75], a[140:143], v[0:15]
ds_read_b64_tr_b16 v[88:89], v105 offset:6144
ds_read_b64_tr_b16 v[90:91], v105 offset:7168
v_mfma_f32_32x32x16_bf16 v[16:31], v[72:75], a[156:159], v[16:31]
s_add_u32 m0, s63, 137216
s_add_u32 s66, s88, 180480
buffer_load_dwordx4 v126, s[52:55], s66 offen offset:0 lds
s_waitcnt lgkmcnt(6)
v_mfma_f32_32x32x16_bf16 v[32:47], v[76:79], a[140:143], v[32:47]
ds_read_b64_tr_b16 v[92:93], v105 offset:6656
ds_read_b64_tr_b16 v[94:95], v105 offset:7680
v_mfma_f32_32x32x16_bf16 v[48:63], v[76:79], a[156:159], v[48:63]
s_add_u32 m0, s63, 138240
s_add_u32 s66, s88, 188672
buffer_load_dwordx4 v126, s[52:55], s66 offen offset:0 lds
s_waitcnt lgkmcnt(6)
v_mfma_f32_32x32x16_bf16 v[0:15], v[80:83], a[168:171], v[0:15]
ds_read_b64_tr_b16 v[64:65], v105 offset:8192
ds_read_b64_tr_b16 v[66:67], v105 offset:9216
v_mfma_f32_32x32x16_bf16 v[16:31], v[80:83], a[184:187], v[16:31]
s_waitcnt lgkmcnt(6)
v_mfma_f32_32x32x16_bf16 v[32:47], v[84:87], a[168:171], v[32:47]
ds_read_b64_tr_b16 v[68:69], v105 offset:8704
ds_read_b64_tr_b16 v[70:71], v105 offset:9728
v_mfma_f32_32x32x16_bf16 v[48:63], v[84:87], a[184:187], v[48:63]
s_waitcnt lgkmcnt(6)
v_mfma_f32_32x32x16_bf16 v[0:15], v[88:91], a[172:175], v[0:15]
ds_read_b64_tr_b16 v[72:73], v105 offset:10240
ds_read_b64_tr_b16 v[74:75], v105 offset:11264
v_mfma_f32_32x32x16_bf16 v[16:31], v[88:91], a[188:191], v[16:31]
s_waitcnt lgkmcnt(6)
v_mfma_f32_32x32x16_bf16 v[32:47], v[92:95], a[172:175], v[32:47]
ds_read_b64_tr_b16 v[76:77], v105 offset:10752
ds_read_b64_tr_b16 v[78:79], v105 offset:11776
v_mfma_f32_32x32x16_bf16 v[48:63], v[92:95], a[188:191], v[48:63]
s_waitcnt vmcnt(8)
s_waitcnt lgkmcnt(6)
v_mfma_f32_32x32x16_bf16 v[0:15], v[64:67], a[200:203], v[0:15]
ds_read_b64_tr_b16 v[80:81], v105 offset:12288
ds_read_b64_tr_b16 v[82:83], v105 offset:13312
v_mfma_f32_32x32x16_bf16 v[16:31], v[64:67], a[216:219], v[16:31]
s_waitcnt lgkmcnt(6)
v_mfma_f32_32x32x16_bf16 v[32:47], v[68:71], a[200:203], v[32:47]
ds_read_b64_tr_b16 v[84:85], v105 offset:12800
ds_read_b64_tr_b16 v[86:87], v105 offset:13824
v_mfma_f32_32x32x16_bf16 v[48:63], v[68:71], a[216:219], v[48:63]
s_waitcnt lgkmcnt(6)
v_mfma_f32_32x32x16_bf16 v[0:15], v[72:75], a[204:207], v[0:15]
ds_read_b64_tr_b16 v[88:89], v105 offset:14336
ds_read_b64_tr_b16 v[90:91], v105 offset:15360
v_mfma_f32_32x32x16_bf16 v[16:31], v[72:75], a[220:223], v[16:31]
s_waitcnt lgkmcnt(6)
v_mfma_f32_32x32x16_bf16 v[32:47], v[76:79], a[204:207], v[32:47]
ds_read_b64_tr_b16 v[92:93], v105 offset:14848
ds_read_b64_tr_b16 v[94:95], v105 offset:15872
v_add_u32 v105, 0x8000, v105
v_mfma_f32_32x32x16_bf16 v[48:63], v[76:79], a[220:223], v[48:63]
s_waitcnt lgkmcnt(6)
v_mfma_f32_32x32x16_bf16 v[0:15], v[80:83], a[232:235], v[0:15]
v_mfma_f32_32x32x16_bf16 v[16:31], v[80:83], a[248:251], v[16:31]
s_waitcnt lgkmcnt(0)
s_barrier
v_mfma_f32_32x32x16_bf16 v[32:47], v[84:87], a[232:235], v[32:47]
ds_read_b64_tr_b16 v[64:65], v105 offset:0
ds_read_b64_tr_b16 v[66:67], v105 offset:1024
ds_read_b64_tr_b16 v[68:69], v105 offset:512
ds_read_b64_tr_b16 v[70:71], v105 offset:1536
v_mfma_f32_32x32x16_bf16 v[48:63], v[84:87], a[248:251], v[48:63]
s_add_u32 m0, s63, 0
s_add_u32 s66, s89, 131328
buffer_load_dwordx4 v126, s[52:55], s66 offen offset:0 lds
v_mfma_f32_32x32x16_bf16 v[0:15], v[88:91], a[236:239], v[0:15]
s_add_u32 m0, s63, 1024
s_add_u32 s66, s89, 139520
buffer_load_dwordx4 v126, s[52:55], s66 offen offset:0 lds
ds_read_b64_tr_b16 v[72:73], v105 offset:2048
ds_read_b64_tr_b16 v[74:75], v105 offset:3072
v_mfma_f32_32x32x16_bf16 v[16:31], v[88:91], a[252:255], v[16:31]
v_mfma_f32_32x32x16_bf16 v[32:47], v[92:95], a[236:239], v[32:47]
s_add_u32 m0, s63, 2048
s_add_u32 s66, s89, 147712
buffer_load_dwordx4 v126, s[52:55], s66 offen offset:0 lds
ds_read_b64_tr_b16 v[76:77], v105 offset:2560
ds_read_b64_tr_b16 v[78:79], v105 offset:3584
v_mfma_f32_32x32x16_bf16 v[48:63], v[92:95], a[252:255], v[48:63]
s_waitcnt lgkmcnt(6)
v_mfma_f32_32x32x16_bf16 v[128:143], v[64:67], a[128:131], v[128:143]
s_add_u32 m0, s63, 3072
s_add_u32 s66, s89, 155904
buffer_load_dwordx4 v126, s[52:55], s66 offen offset:0 lds
ds_read_b64_tr_b16 v[80:81], v105 offset:4096
ds_read_b64_tr_b16 v[82:83], v105 offset:5120
v_mfma_f32_32x32x16_bf16 v[144:159], v[64:67], a[144:147], v[144:159]
s_waitcnt lgkmcnt(6)
v_mfma_f32_32x32x16_bf16 v[160:175], v[68:71], a[128:131], v[160:175]
s_add_u32 m0, s63, 4096
s_add_u32 s66, s89, 164096
buffer_load_dwordx4 v126, s[52:55], s66 offen offset:0 lds
ds_read_b64_tr_b16 v[84:85], v105 offset:4608
ds_read_b64_tr_b16 v[86:87], v105 offset:5632
v_mfma_f32_32x32x16_bf16 v[176:191], v[68:71], a[144:147], v[176:191]
s_add_u32 m0, s63, 5120
s_add_u32 s66, s89, 172288
buffer_load_dwordx4 v126, s[52:55], s66 offen offset:0 lds
s_waitcnt lgkmcnt(6)
v_mfma_f32_32x32x16_bf16 v[128:143], v[72:75], a[132:135], v[128:143]
ds_read_b64_tr_b16 v[88:89], v105 offset:6144
ds_read_b64_tr_b16 v[90:91], v105 offset:7168
v_mfma_f32_32x32x16_bf16 v[144:159], v[72:75], a[148:151], v[144:159]
s_add_u32 m0, s63, 6144
s_add_u32 s66, s89, 180480
buffer_load_dwordx4 v126, s[52:55], s66 offen offset:0 lds
s_waitcnt lgkmcnt(6)
v_mfma_f32_32x32x16_bf16 v[160:175], v[76:79], a[132:135], v[160:175]
ds_read_b64_tr_b16 v[92:93], v105 offset:6656
ds_read_b64_tr_b16 v[94:95], v105 offset:7680
v_mfma_f32_32x32x16_bf16 v[176:191], v[76:79], a[148:151], v[176:191]
s_add_u32 m0, s63, 7168
s_add_u32 s66, s89, 188672
buffer_load_dwordx4 v126, s[52:55], s66 offen offset:0 lds
s_waitcnt lgkmcnt(6)
v_mfma_f32_32x32x16_bf16 v[128:143], v[80:83], a[160:163], v[128:143]
ds_read_b64_tr_b16 v[64:65], v105 offset:8192
ds_read_b64_tr_b16 v[66:67], v105 offset:9216
v_mfma_f32_32x32x16_bf16 v[144:159], v[80:83], a[176:179], v[144:159]
s_add_u32 s67, s97, 0
v_cvt_pk_bf16_f32 v0, v0, v1
v_cvt_pk_bf16_f32 v1, v2, v3
v_cvt_pk_bf16_f32 v2, v4, v5
v_cvt_pk_bf16_f32 v3, v6, v7
buffer_store_dwordx4 v[0:3], v127, s[56:59], s67 offen offset:0 sc1
s_waitcnt lgkmcnt(6)
v_mfma_f32_32x32x16_bf16 v[160:175], v[84:87], a[160:163], v[160:175]
ds_read_b64_tr_b16 v[68:69], v105 offset:8704
ds_read_b64_tr_b16 v[70:71], v105 offset:9728
v_mfma_f32_32x32x16_bf16 v[176:191], v[84:87], a[176:179], v[176:191]
v_cvt_pk_bf16_f32 v8, v8, v9
v_cvt_pk_bf16_f32 v9, v10, v11
v_cvt_pk_bf16_f32 v10, v12, v13
v_cvt_pk_bf16_f32 v11, v14, v15
buffer_store_dwordx4 v[8:11], v127, s[56:59], s67 offen offset:1024 sc1
s_waitcnt lgkmcnt(6)
v_mfma_f32_32x32x16_bf16 v[128:143], v[88:91], a[164:167], v[128:143]
ds_read_b64_tr_b16 v[72:73], v105 offset:10240
ds_read_b64_tr_b16 v[74:75], v105 offset:11264
v_mfma_f32_32x32x16_bf16 v[144:159], v[88:91], a[180:183], v[144:159]
v_cvt_pk_bf16_f32 v16, v16, v17
v_cvt_pk_bf16_f32 v17, v18, v19
v_cvt_pk_bf16_f32 v18, v20, v21
v_cvt_pk_bf16_f32 v19, v22, v23
s_mov_b64 exec, s[50:51]
buffer_store_dwordx4 v[16:19], v127, s[56:59], s67 offen offset:2048 sc1
s_mov_b64 exec, -1
s_waitcnt lgkmcnt(6)
v_mfma_f32_32x32x16_bf16 v[160:175], v[92:95], a[164:167], v[160:175]
ds_read_b64_tr_b16 v[76:77], v105 offset:10752
ds_read_b64_tr_b16 v[78:79], v105 offset:11776
v_mfma_f32_32x32x16_bf16 v[176:191], v[92:95], a[180:183], v[176:191]
v_cvt_pk_bf16_f32 v24, v24, v25
v_cvt_pk_bf16_f32 v25, v26, v27
v_cvt_pk_bf16_f32 v26, v28, v29
v_cvt_pk_bf16_f32 v27, v30, v31
s_mov_b64 exec, s[50:51]
buffer_store_dwordx4 v[24:27], v127, s[56:59], s67 offen offset:3072 sc1
s_mov_b64 exec, -1
s_waitcnt lgkmcnt(6)
v_mfma_f32_32x32x16_bf16 v[128:143], v[64:67], a[192:195], v[128:143]
ds_read_b64_tr_b16 v[80:81], v105 offset:12288
ds_read_b64_tr_b16 v[82:83], v105 offset:13312
v_mfma_f32_32x32x16_bf16 v[144:159], v[64:67], a[208:211], v[144:159]
s_add_u32 s67, s97, 4096
v_cvt_pk_bf16_f32 v32, v32, v33
v_cvt_pk_bf16_f32 v33, v34, v35
v_cvt_pk_bf16_f32 v34, v36, v37
v_cvt_pk_bf16_f32 v35, v38, v39
buffer_store_dwordx4 v[32:35], v127, s[56:59], s67 offen offset:0 sc1
s_waitcnt lgkmcnt(6)
v_mfma_f32_32x32x16_bf16 v[160:175], v[68:71], a[192:195], v[160:175]
ds_read_b64_tr_b16 v[84:85], v105 offset:12800
ds_read_b64_tr_b16 v[86:87], v105 offset:13824
v_mfma_f32_32x32x16_bf16 v[176:191], v[68:71], a[208:211], v[176:191]
v_cvt_pk_bf16_f32 v40, v40, v41
v_cvt_pk_bf16_f32 v41, v42, v43
v_cvt_pk_bf16_f32 v42, v44, v45
v_cvt_pk_bf16_f32 v43, v46, v47
buffer_store_dwordx4 v[40:43], v127, s[56:59], s67 offen offset:1024 sc1
s_waitcnt lgkmcnt(6)
v_mfma_f32_32x32x16_bf16 v[128:143], v[72:75], a[196:199], v[128:143]
ds_read_b64_tr_b16 v[88:89], v105 offset:14336
ds_read_b64_tr_b16 v[90:91], v105 offset:15360
v_mfma_f32_32x32x16_bf16 v[144:159], v[72:75], a[212:215], v[144:159]
v_cvt_pk_bf16_f32 v48, v48, v49
v_cvt_pk_bf16_f32 v49, v50, v51
v_cvt_pk_bf16_f32 v50, v52, v53
v_cvt_pk_bf16_f32 v51, v54, v55
s_mov_b64 exec, s[50:51]
buffer_store_dwordx4 v[48:51], v127, s[56:59], s67 offen offset:2048 sc1
s_mov_b64 exec, -1
s_waitcnt lgkmcnt(6)
v_mfma_f32_32x32x16_bf16 v[160:175], v[76:79], a[196:199], v[160:175]
ds_read_b64_tr_b16 v[92:93], v105 offset:14848
ds_read_b64_tr_b16 v[94:95], v105 offset:15872
v_add_u32 v105, 0x10000, v105
v_mfma_f32_32x32x16_bf16 v[176:191], v[76:79], a[212:215], v[176:191]
v_cvt_pk_bf16_f32 v56, v56, v57
v_cvt_pk_bf16_f32 v57, v58, v59
v_cvt_pk_bf16_f32 v58, v60, v61
v_cvt_pk_bf16_f32 v59, v62, v63
s_mov_b64 exec, s[50:51]
buffer_store_dwordx4 v[56:59], v127, s[56:59], s67 offen offset:3072 sc1
s_mov_b64 exec, -1
s_waitcnt lgkmcnt(6)
v_mfma_f32_32x32x16_bf16 v[128:143], v[80:83], a[224:227], v[128:143]
v_mfma_f32_32x32x16_bf16 v[144:159], v[80:83], a[240:243], v[144:159]
s_waitcnt lgkmcnt(0)
s_barrier
v_mfma_f32_32x32x16_bf16 v[160:175], v[84:87], a[224:227], v[160:175]
ds_read_b64_tr_b16 v[64:65], v105 offset:0
ds_read_b64_tr_b16 v[66:67], v105 offset:1024
ds_read_b64_tr_b16 v[68:69], v105 offset:512
ds_read_b64_tr_b16 v[70:71], v105 offset:1536
v_mfma_f32_32x32x16_bf16 v[176:191], v[84:87], a[240:243], v[176:191]
s_add_u32 m0, s63, 32768
s_add_u32 s66, s88, 384
buffer_load_dwordx4 v126, s[52:55], s66 offen offset:0 lds
v_mfma_f32_32x32x16_bf16 v[128:143], v[88:91], a[228:231], v[128:143]
s_add_u32 m0, s63, 33792
s_add_u32 s66, s88, 8576
buffer_load_dwordx4 v126, s[52:55], s66 offen offset:0 lds
ds_read_b64_tr_b16 v[72:73], v105 offset:2048
ds_read_b64_tr_b16 v[74:75], v105 offset:3072
v_mfma_f32_32x32x16_bf16 v[144:159], v[88:91], a[244:247], v[144:159]
v_mfma_f32_32x32x16_bf16 v[160:175], v[92:95], a[228:231], v[160:175]
s_add_u32 m0, s63, 34816
s_add_u32 s66, s88, 16768
buffer_load_dwordx4 v126, s[52:55], s66 offen offset:0 lds
ds_read_b64_tr_b16 v[76:77], v105 offset:2560
ds_read_b64_tr_b16 v[78:79], v105 offset:3584
v_mfma_f32_32x32x16_bf16 v[176:191], v[92:95], a[244:247], v[176:191]
s_waitcnt lgkmcnt(6)
v_mfma_f32_32x32x16_bf16 v[128:143], v[64:67], a[136:139], v[128:143]
s_add_u32 m0, s63, 35840
s_add_u32 s66, s88, 24960
buffer_load_dwordx4 v126, s[52:55], s66 offen offset:0 lds
ds_read_b64_tr_b16 v[80:81], v105 offset:4096
ds_read_b64_tr_b16 v[82:83], v105 offset:5120
v_mfma_f32_32x32x16_bf16 v[144:159], v[64:67], a[152:155], v[144:159]
s_waitcnt lgkmcnt(6)
v_mfma_f32_32x32x16_bf16 v[160:175], v[68:71], a[136:139], v[160:175]
s_add_u32 m0, s63, 36864
s_add_u32 s66, s88, 33152
buffer_load_dwordx4 v126, s[52:55], s66 offen offset:0 lds
ds_read_b64_tr_b16 v[84:85], v105 offset:4608
ds_read_b64_tr_b16 v[86:87], v105 offset:5632
v_mfma_f32_32x32x16_bf16 v[176:191], v[68:71], a[152:155], v[176:191]
s_add_u32 m0, s63, 37888
s_add_u32 s66, s88, 41344
buffer_load_dwordx4 v126, s[52:55], s66 offen offset:0 lds
s_waitcnt lgkmcnt(6)
v_mfma_f32_32x32x16_bf16 v[128:143], v[72:75], a[140:143], v[128:143]
ds_read_b64_tr_b16 v[88:89], v105 offset:6144
ds_read_b64_tr_b16 v[90:91], v105 offset:7168
v_mfma_f32_32x32x16_bf16 v[144:159], v[72:75], a[156:159], v[144:159]
s_add_u32 m0, s63, 38912
s_add_u32 s66, s88, 49536
buffer_load_dwordx4 v126, s[52:55], s66 offen offset:0 lds
s_waitcnt lgkmcnt(6)
v_mfma_f32_32x32x16_bf16 v[160:175], v[76:79], a[140:143], v[160:175]
ds_read_b64_tr_b16 v[92:93], v105 offset:6656
ds_read_b64_tr_b16 v[94:95], v105 offset:7680
v_mfma_f32_32x32x16_bf16 v[176:191], v[76:79], a[156:159], v[176:191]
s_add_u32 m0, s63, 39936
s_add_u32 s66, s88, 57728
buffer_load_dwordx4 v126, s[52:55], s66 offen offset:0 lds
s_waitcnt lgkmcnt(6)
v_mfma_f32_32x32x16_bf16 v[128:143], v[80:83], a[168:171], v[128:143]
ds_read_b64_tr_b16 v[64:65], v105 offset:8192
ds_read_b64_tr_b16 v[66:67], v105 offset:9216
v_mfma_f32_32x32x16_bf16 v[144:159], v[80:83], a[184:187], v[144:159]
s_add_u32 m0, s63, 65536
s_add_u32 s66, s89, 131456
buffer_load_dwordx4 v126, s[52:55], s66 offen offset:0 lds
s_waitcnt lgkmcnt(6)
v_mfma_f32_32x32x16_bf16 v[160:175], v[84:87], a[168:171], v[160:175]
ds_read_b64_tr_b16 v[68:69], v105 offset:8704
ds_read_b64_tr_b16 v[70:71], v105 offset:9728
v_mfma_f32_32x32x16_bf16 v[176:191], v[84:87], a[184:187], v[176:191]
s_add_u32 m0, s63, 66560
s_add_u32 s66, s89, 139648
buffer_load_dwordx4 v126, s[52:55], s66 offen offset:0 lds
s_waitcnt lgkmcnt(6)
v_mfma_f32_32x32x16_bf16 v[128:143], v[88:91], a[172:175], v[128:143]
ds_read_b64_tr_b16 v[72:73], v105 offset:10240
ds_read_b64_tr_b16 v[74:75], v105 offset:11264
v_mfma_f32_32x32x16_bf16 v[144:159], v[88:91], a[188:191], v[144:159]
s_add_u32 m0, s63, 67584
s_add_u32 s66, s89, 147840
buffer_load_dwordx4 v126, s[52:55], s66 offen offset:0 lds
s_waitcnt lgkmcnt(6)
v_mfma_f32_32x32x16_bf16 v[160:175], v[92:95], a[172:175], v[160:175]
ds_read_b64_tr_b16 v[76:77], v105 offset:10752
ds_read_b64_tr_b16 v[78:79], v105 offset:11776
v_mfma_f32_32x32x16_bf16 v[176:191], v[92:95], a[188:191], v[176:191]
s_add_u32 m0, s63, 68608
s_add_u32 s66, s89, 156032
buffer_load_dwordx4 v126, s[52:55], s66 offen offset:0 lds
s_waitcnt lgkmcnt(6)
v_mfma_f32_32x32x16_bf16 v[128:143], v[64:67], a[200:203], v[128:143]
ds_read_b64_tr_b16 v[80:81], v105 offset:12288
ds_read_b64_tr_b16 v[82:83], v105 offset:13312
v_mfma_f32_32x32x16_bf16 v[144:159], v[64:67], a[216:219], v[144:159]
s_add_u32 m0, s63, 69632
s_add_u32 s66, s89, 164224
buffer_load_dwordx4 v126, s[52:55], s66 offen offset:0 lds
s_waitcnt lgkmcnt(6)
v_mfma_f32_32x32x16_bf16 v[160:175], v[68:71], a[200:203], v[160:175]
ds_read_b64_tr_b16 v[84:85], v105 offset:12800
ds_read_b64_tr_b16 v[86:87], v105 offset:13824
v_mfma_f32_32x32x16_bf16 v[176:191], v[68:71], a[216:219], v[176:191]
s_add_u32 m0, s63, 70656
s_add_u32 s66, s89, 172416
buffer_load_dwordx4 v126, s[52:55], s66 offen offset:0 lds
s_waitcnt lgkmcnt(6)
v_mfma_f32_32x32x16_bf16 v[128:143], v[72:75], a[204:207], v[128:143]
ds_read_b64_tr_b16 v[88:89], v105 offset:14336
ds_read_b64_tr_b16 v[90:91], v105 offset:15360
v_mfma_f32_32x32x16_bf16 v[144:159], v[72:75], a[220:223], v[144:159]
s_add_u32 m0, s63, 71680
s_add_u32 s66, s89, 180608
buffer_load_dwordx4 v126, s[52:55], s66 offen offset:0 lds
s_waitcnt lgkmcnt(6)
v_mfma_f32_32x32x16_bf16 v[160:175], v[76:79], a[204:207], v[160:175]
ds_read_b64_tr_b16 v[92:93], v105 offset:14848
ds_read_b64_tr_b16 v[94:95], v105 offset:15872
v_add_u32 v105, 0x8000, v105
v_mfma_f32_32x32x16_bf16 v[176:191], v[76:79], a[220:223], v[176:191]
s_add_u32 m0, s63, 72704
s_add_u32 s66, s89, 188800
buffer_load_dwordx4 v126, s[52:55], s66 offen offset:0 lds
s_waitcnt lgkmcnt(6)
v_mfma_f32_32x32x16_bf16 v[128:143], v[80:83], a[232:235], v[128:143]
v_mfma_f32_32x32x16_bf16 v[144:159], v[80:83], a[248:251], v[144:159]
s_waitcnt vmcnt(32)
s_waitcnt lgkmcnt(0)
s_barrier
v_mfma_f32_32x32x16_bf16 v[160:175], v[84:87], a[232:235], v[160:175]
ds_read_b64_tr_b16 v[64:65], v105 offset:0
ds_read_b64_tr_b16 v[66:67], v105 offset:1024
ds_read_b64_tr_b16 v[68:69], v105 offset:512
ds_read_b64_tr_b16 v[70:71], v105 offset:1536
v_mfma_f32_32x32x16_bf16 v[176:191], v[84:87], a[248:251], v[176:191]
s_add_u32 m0, s63, 98304
s_add_u32 s66, s89, 384
buffer_load_dwordx4 v126, s[52:55], s66 offen offset:0 lds
v_mfma_f32_32x32x16_bf16 v[128:143], v[88:91], a[236:239], v[128:143]
s_add_u32 m0, s63, 99328
s_add_u32 s66, s89, 8576
buffer_load_dwordx4 v126, s[52:55], s66 offen offset:0 lds
ds_read_b64_tr_b16 v[72:73], v105 offset:2048
ds_read_b64_tr_b16 v[74:75], v105 offset:3072
v_mfma_f32_32x32x16_bf16 v[144:159], v[88:91], a[252:255], v[144:159]
v_mfma_f32_32x32x16_bf16 v[160:175], v[92:95], a[236:239], v[160:175]
s_add_u32 m0, s63, 100352
s_add_u32 s66, s89, 16768
buffer_load_dwordx4 v126, s[52:55], s66 offen offset:0 lds
ds_read_b64_tr_b16 v[76:77], v105 offset:2560
ds_read_b64_tr_b16 v[78:79], v105 offset:3584
v_mfma_f32_32x32x16_bf16 v[176:191], v[92:95], a[252:255], v[176:191]
s_waitcnt lgkmcnt(6)
v_mfma_f32_32x32x16_bf16 v[192:207], v[64:67], a[128:131], v[192:207]
s_add_u32 m0, s63, 101376
s_add_u32 s66, s89, 24960
buffer_load_dwordx4 v126, s[52:55], s66 offen offset:0 lds
ds_read_b64_tr_b16 v[80:81], v105 offset:4096
ds_read_b64_tr_b16 v[82:83], v105 offset:5120
v_mfma_f32_32x32x16_bf16 v[208:223], v[64:67], a[144:147], v[208:223]
s_waitcnt lgkmcnt(6)
v_mfma_f32_32x32x16_bf16 v[224:239], v[68:71], a[128:131], v[224:239]
s_add_u32 m0, s63, 102400
s_add_u32 s66, s89, 33152
buffer_load_dwordx4 v126, s[52:55], s66 offen offset:0 lds
ds_read_b64_tr_b16 v[84:85], v105 offset:4608
ds_read_b64_tr_b16 v[86:87], v105 offset:5632
v_mfma_f32_32x32x16_bf16 v[240:255], v[68:71], a[144:147], v[240:255]
s_add_u32 m0, s63, 103424
s_add_u32 s66, s89, 41344
buffer_load_dwordx4 v126, s[52:55], s66 offen offset:0 lds
s_waitcnt lgkmcnt(6)
v_mfma_f32_32x32x16_bf16 v[192:207], v[72:75], a[132:135], v[192:207]
ds_read_b64_tr_b16 v[88:89], v105 offset:6144
ds_read_b64_tr_b16 v[90:91], v105 offset:7168
v_mfma_f32_32x32x16_bf16 v[208:223], v[72:75], a[148:151], v[208:223]
s_add_u32 m0, s63, 104448
s_add_u32 s66, s89, 49536
buffer_load_dwordx4 v126, s[52:55], s66 offen offset:0 lds
s_waitcnt lgkmcnt(6)
v_mfma_f32_32x32x16_bf16 v[224:239], v[76:79], a[132:135], v[224:239]
ds_read_b64_tr_b16 v[92:93], v105 offset:6656
ds_read_b64_tr_b16 v[94:95], v105 offset:7680
v_mfma_f32_32x32x16_bf16 v[240:255], v[76:79], a[148:151], v[240:255]
s_add_u32 m0, s63, 105472
s_add_u32 s66, s89, 57728
buffer_load_dwordx4 v126, s[52:55], s66 offen offset:0 lds
s_waitcnt lgkmcnt(6)
v_mfma_f32_32x32x16_bf16 v[192:207], v[80:83], a[160:163], v[192:207]
ds_read_b64_tr_b16 v[64:65], v105 offset:8192
ds_read_b64_tr_b16 v[66:67], v105 offset:9216
v_mfma_f32_32x32x16_bf16 v[208:223], v[80:83], a[176:179], v[208:223]
s_waitcnt lgkmcnt(6)
v_mfma_f32_32x32x16_bf16 v[224:239], v[84:87], a[160:163], v[224:239]
ds_read_b64_tr_b16 v[68:69], v105 offset:8704
ds_read_b64_tr_b16 v[70:71], v105 offset:9728
v_mfma_f32_32x32x16_bf16 v[240:255], v[84:87], a[176:179], v[240:255]
s_waitcnt lgkmcnt(6)
v_mfma_f32_32x32x16_bf16 v[192:207], v[88:91], a[164:167], v[192:207]
ds_read_b64_tr_b16 v[72:73], v105 offset:10240
ds_read_b64_tr_b16 v[74:75], v105 offset:11264
v_mfma_f32_32x32x16_bf16 v[208:223], v[88:91], a[180:183], v[208:223]
s_waitcnt lgkmcnt(6)
v_mfma_f32_32x32x16_bf16 v[224:239], v[92:95], a[164:167], v[224:239]
ds_read_b64_tr_b16 v[76:77], v105 offset:10752
ds_read_b64_tr_b16 v[78:79], v105 offset:11776
v_mfma_f32_32x32x16_bf16 v[240:255], v[92:95], a[180:183], v[240:255]
s_waitcnt lgkmcnt(6)
v_mfma_f32_32x32x16_bf16 v[192:207], v[64:67], a[192:195], v[192:207]
ds_read_b64_tr_b16 v[80:81], v105 offset:12288
ds_read_b64_tr_b16 v[82:83], v105 offset:13312
v_mfma_f32_32x32x16_bf16 v[208:223], v[64:67], a[208:211], v[208:223]
s_waitcnt lgkmcnt(6)
v_mfma_f32_32x32x16_bf16 v[224:239], v[68:71], a[192:195], v[224:239]
ds_read_b64_tr_b16 v[84:85], v105 offset:12800
ds_read_b64_tr_b16 v[86:87], v105 offset:13824
v_mfma_f32_32x32x16_bf16 v[240:255], v[68:71], a[208:211], v[240:255]
s_waitcnt lgkmcnt(6)
v_mfma_f32_32x32x16_bf16 v[192:207], v[72:75], a[196:199], v[192:207]
ds_read_b64_tr_b16 v[88:89], v105 offset:14336
ds_read_b64_tr_b16 v[90:91], v105 offset:15360
v_mfma_f32_32x32x16_bf16 v[208:223], v[72:75], a[212:215], v[208:223]
s_waitcnt lgkmcnt(6)
v_mfma_f32_32x32x16_bf16 v[224:239], v[76:79], a[196:199], v[224:239]
ds_read_b64_tr_b16 v[92:93], v105 offset:14848
ds_read_b64_tr_b16 v[94:95], v105 offset:15872
v_add_u32 v105, 0xfffe0000, v105
v_mfma_f32_32x32x16_bf16 v[240:255], v[76:79], a[212:215], v[240:255]
s_waitcnt lgkmcnt(6)
v_mfma_f32_32x32x16_bf16 v[192:207], v[80:83], a[224:227], v[192:207]
v_mfma_f32_32x32x16_bf16 v[208:223], v[80:83], a[240:243], v[208:223]
s_waitcnt vmcnt(32)
s_waitcnt lgkmcnt(0)
s_barrier
v_mfma_f32_32x32x16_bf16 v[224:239], v[84:87], a[224:227], v[224:239]
ds_read_b64_tr_b16 v[64:65], v105 offset:0
ds_read_b64_tr_b16 v[66:67], v105 offset:1024
ds_read_b64_tr_b16 v[68:69], v105 offset:512
ds_read_b64_tr_b16 v[70:71], v105 offset:1536
v_mfma_f32_32x32x16_bf16 v[240:255], v[84:87], a[240:243], v[240:255]
s_add_u32 m0, s63, 131072
s_add_u32 s66, s88, 131456
buffer_load_dwordx4 v126, s[52:55], s66 offen offset:0 lds
v_mfma_f32_32x32x16_bf16 v[192:207], v[88:91], a[228:231], v[192:207]
s_add_u32 m0, s63, 132096
s_add_u32 s66, s88, 139648
buffer_load_dwordx4 v126, s[52:55], s66 offen offset:0 lds
ds_read_b64_tr_b16 v[72:73], v105 offset:2048
ds_read_b64_tr_b16 v[74:75], v105 offset:3072
v_mfma_f32_32x32x16_bf16 v[208:223], v[88:91], a[244:247], v[208:223]
v_mfma_f32_32x32x16_bf16 v[224:239], v[92:95], a[228:231], v[224:239]
s_add_u32 m0, s63, 133120
s_add_u32 s66, s88, 147840
buffer_load_dwordx4 v126, s[52:55], s66 offen offset:0 lds
ds_read_b64_tr_b16 v[76:77], v105 offset:2560
ds_read_b64_tr_b16 v[78:79], v105 offset:3584
v_mfma_f32_32x32x16_bf16 v[240:255], v[92:95], a[244:247], v[240:255]
s_waitcnt lgkmcnt(6)
v_mfma_f32_32x32x16_bf16 v[192:207], v[64:67], a[136:139], v[192:207]
s_add_u32 m0, s63, 134144
s_add_u32 s66, s88, 156032
buffer_load_dwordx4 v126, s[52:55], s66 offen offset:0 lds
ds_read_b64_tr_b16 v[80:81], v105 offset:4096
ds_read_b64_tr_b16 v[82:83], v105 offset:5120
v_mfma_f32_32x32x16_bf16 v[208:223], v[64:67], a[152:155], v[208:223]
s_waitcnt lgkmcnt(6)
v_mfma_f32_32x32x16_bf16 v[224:239], v[68:71], a[136:139], v[224:239]
s_add_u32 m0, s63, 135168
s_add_u32 s66, s88, 164224
buffer_load_dwordx4 v126, s[52:55], s66 offen offset:0 lds
ds_read_b64_tr_b16 v[84:85], v105 offset:4608
ds_read_b64_tr_b16 v[86:87], v105 offset:5632
v_mfma_f32_32x32x16_bf16 v[240:255], v[68:71], a[152:155], v[240:255]
s_add_u32 m0, s63, 136192
s_add_u32 s66, s88, 172416
buffer_load_dwordx4 v126, s[52:55], s66 offen offset:0 lds
s_waitcnt lgkmcnt(6)
v_mfma_f32_32x32x16_bf16 v[192:207], v[72:75], a[140:143], v[192:207]
ds_read_b64_tr_b16 v[88:89], v105 offset:6144
ds_read_b64_tr_b16 v[90:91], v105 offset:7168
v_mfma_f32_32x32x16_bf16 v[208:223], v[72:75], a[156:159], v[208:223]
s_add_u32 m0, s63, 137216
s_add_u32 s66, s88, 180608
buffer_load_dwordx4 v126, s[52:55], s66 offen offset:0 lds
s_waitcnt lgkmcnt(6)
v_mfma_f32_32x32x16_bf16 v[224:239], v[76:79], a[140:143], v[224:239]
ds_read_b64_tr_b16 v[92:93], v105 offset:6656
ds_read_b64_tr_b16 v[94:95], v105 offset:7680
v_mfma_f32_32x32x16_bf16 v[240:255], v[76:79], a[156:159], v[240:255]
s_add_u32 m0, s63, 138240
s_add_u32 s66, s88, 188800
buffer_load_dwordx4 v126, s[52:55], s66 offen offset:0 lds
s_waitcnt lgkmcnt(6)
v_mfma_f32_32x32x16_bf16 v[192:207], v[80:83], a[168:171], v[192:207]
ds_read_b64_tr_b16 v[64:65], v105 offset:8192
ds_read_b64_tr_b16 v[66:67], v105 offset:9216
v_mfma_f32_32x32x16_bf16 v[208:223], v[80:83], a[184:187], v[208:223]
s_waitcnt lgkmcnt(6)
v_mfma_f32_32x32x16_bf16 v[224:239], v[84:87], a[168:171], v[224:239]
ds_read_b64_tr_b16 v[68:69], v105 offset:8704
ds_read_b64_tr_b16 v[70:71], v105 offset:9728
v_mfma_f32_32x32x16_bf16 v[240:255], v[84:87], a[184:187], v[240:255]
s_waitcnt lgkmcnt(6)
v_mfma_f32_32x32x16_bf16 v[192:207], v[88:91], a[172:175], v[192:207]
ds_read_b64_tr_b16 v[72:73], v105 offset:10240
ds_read_b64_tr_b16 v[74:75], v105 offset:11264
v_mfma_f32_32x32x16_bf16 v[208:223], v[88:91], a[188:191], v[208:223]
s_waitcnt lgkmcnt(6)
v_mfma_f32_32x32x16_bf16 v[224:239], v[92:95], a[172:175], v[224:239]
ds_read_b64_tr_b16 v[76:77], v105 offset:10752
ds_read_b64_tr_b16 v[78:79], v105 offset:11776
v_mfma_f32_32x32x16_bf16 v[240:255], v[92:95], a[188:191], v[240:255]
s_waitcnt lgkmcnt(6)
v_mfma_f32_32x32x16_bf16 v[192:207], v[64:67], a[200:203], v[192:207]
ds_read_b64_tr_b16 v[80:81], v105 offset:12288
ds_read_b64_tr_b16 v[82:83], v105 offset:13312
v_mfma_f32_32x32x16_bf16 v[208:223], v[64:67], a[216:219], v[208:223]
s_waitcnt lgkmcnt(6)
v_mfma_f32_32x32x16_bf16 v[224:239], v[68:71], a[200:203], v[224:239]
ds_read_b64_tr_b16 v[84:85], v105 offset:12800
ds_read_b64_tr_b16 v[86:87], v105 offset:13824
v_mfma_f32_32x32x16_bf16 v[240:255], v[68:71], a[216:219], v[240:255]
s_waitcnt lgkmcnt(6)
v_mfma_f32_32x32x16_bf16 v[192:207], v[72:75], a[204:207], v[192:207]
ds_read_b64_tr_b16 v[88:89], v105 offset:14336
ds_read_b64_tr_b16 v[90:91], v105 offset:15360
v_mfma_f32_32x32x16_bf16 v[208:223], v[72:75], a[220:223], v[208:223]
s_waitcnt lgkmcnt(6)
v_mfma_f32_32x32x16_bf16 v[224:239], v[76:79], a[204:207], v[224:239]
ds_read_b64_tr_b16 v[92:93], v105 offset:14848
ds_read_b64_tr_b16 v[94:95], v105 offset:15872
v_add_u32 v105, 0x8000, v105
v_mfma_f32_32x32x16_bf16 v[240:255], v[76:79], a[220:223], v[240:255]
s_waitcnt lgkmcnt(6)
v_mfma_f32_32x32x16_bf16 v[192:207], v[80:83], a[232:235], v[192:207]
v_mfma_f32_32x32x16_bf16 v[208:223], v[80:83], a[248:251], v[208:223]
s_waitcnt vmcnt(24)
s_waitcnt lgkmcnt(0)
s_barrier
v_mfma_f32_32x32x16_bf16 v[224:239], v[84:87], a[232:235], v[224:239]
ds_read_b64_tr_b16 v[64:65], v105 offset:0
ds_read_b64_tr_b16 v[66:67], v105 offset:1024
ds_read_b64_tr_b16 v[68:69], v105 offset:512
ds_read_b64_tr_b16 v[70:71], v105 offset:1536
v_mfma_f32_32x32x16_bf16 v[240:255], v[84:87], a[248:251], v[240:255]
v_mfma_f32_32x32x16_bf16 v[192:207], v[88:91], a[236:239], v[192:207]
ds_read_b64_tr_b16 v[72:73], v105 offset:2048
ds_read_b64_tr_b16 v[74:75], v105 offset:3072
v_mfma_f32_32x32x16_bf16 v[208:223], v[88:91], a[252:255], v[208:223]
v_mfma_f32_32x32x16_bf16 v[224:239], v[92:95], a[236:239], v[224:239]
ds_read_b64_tr_b16 v[76:77], v105 offset:2560
ds_read_b64_tr_b16 v[78:79], v105 offset:3584
v_mfma_f32_32x32x16_bf16 v[240:255], v[92:95], a[252:255], v[240:255]
s_waitcnt lgkmcnt(6)
v_mfma_f32_32x32x16_bf16 v[0:15], v[64:67], a[0:3], 0
ds_read_b64_tr_b16 v[80:81], v105 offset:4096
ds_read_b64_tr_b16 v[82:83], v105 offset:5120
v_mfma_f32_32x32x16_bf16 v[16:31], v[64:67], a[16:19], 0
s_waitcnt lgkmcnt(6)
v_mfma_f32_32x32x16_bf16 v[32:47], v[68:71], a[0:3], 0
ds_read_b64_tr_b16 v[84:85], v105 offset:4608
ds_read_b64_tr_b16 v[86:87], v105 offset:5632
v_mfma_f32_32x32x16_bf16 v[48:63], v[68:71], a[16:19], 0
s_waitcnt lgkmcnt(6)
v_mfma_f32_32x32x16_bf16 v[0:15], v[72:75], a[4:7], v[0:15]
ds_read_b64_tr_b16 v[88:89], v105 offset:6144
ds_read_b64_tr_b16 v[90:91], v105 offset:7168
v_mfma_f32_32x32x16_bf16 v[16:31], v[72:75], a[20:23], v[16:31]
s_waitcnt lgkmcnt(6)
v_mfma_f32_32x32x16_bf16 v[32:47], v[76:79], a[4:7], v[32:47]
ds_read_b64_tr_b16 v[92:93], v105 offset:6656
ds_read_b64_tr_b16 v[94:95], v105 offset:7680
v_mfma_f32_32x32x16_bf16 v[48:63], v[76:79], a[20:23], v[48:63]
s_waitcnt lgkmcnt(6)
v_mfma_f32_32x32x16_bf16 v[0:15], v[80:83], a[32:35], v[0:15]
ds_read_b64_tr_b16 v[64:65], v105 offset:8192
ds_read_b64_tr_b16 v[66:67], v105 offset:9216
v_mfma_f32_32x32x16_bf16 v[16:31], v[80:83], a[48:51], v[16:31]
s_waitcnt lgkmcnt(6)
v_mfma_f32_32x32x16_bf16 v[32:47], v[84:87], a[32:35], v[32:47]
ds_read_b64_tr_b16 v[68:69], v105 offset:8704
ds_read_b64_tr_b16 v[70:71], v105 offset:9728
v_mfma_f32_32x32x16_bf16 v[48:63], v[84:87], a[48:51], v[48:63]
s_waitcnt lgkmcnt(6)
v_mfma_f32_32x32x16_bf16 v[0:15], v[88:91], a[36:39], v[0:15]
ds_read_b64_tr_b16 v[72:73], v105 offset:10240
ds_read_b64_tr_b16 v[74:75], v105 offset:11264
v_mfma_f32_32x32x16_bf16 v[16:31], v[88:91], a[52:55], v[16:31]
s_waitcnt lgkmcnt(6)
v_mfma_f32_32x32x16_bf16 v[32:47], v[92:95], a[36:39], v[32:47]
ds_read_b64_tr_b16 v[76:77], v105 offset:10752
ds_read_b64_tr_b16 v[78:79], v105 offset:11776
v_mfma_f32_32x32x16_bf16 v[48:63], v[92:95], a[52:55], v[48:63]
s_waitcnt lgkmcnt(6)
v_mfma_f32_32x32x16_bf16 v[0:15], v[64:67], a[64:67], v[0:15]
ds_read_b64_tr_b16 v[80:81], v105 offset:12288
ds_read_b64_tr_b16 v[82:83], v105 offset:13312
v_mfma_f32_32x32x16_bf16 v[16:31], v[64:67], a[80:83], v[16:31]
s_waitcnt lgkmcnt(6)
v_mfma_f32_32x32x16_bf16 v[32:47], v[68:71], a[64:67], v[32:47]
ds_read_b64_tr_b16 v[84:85], v105 offset:12800
ds_read_b64_tr_b16 v[86:87], v105 offset:13824
v_mfma_f32_32x32x16_bf16 v[48:63], v[68:71], a[80:83], v[48:63]
s_waitcnt lgkmcnt(6)
v_mfma_f32_32x32x16_bf16 v[0:15], v[72:75], a[68:71], v[0:15]
ds_read_b64_tr_b16 v[88:89], v105 offset:14336
ds_read_b64_tr_b16 v[90:91], v105 offset:15360
v_mfma_f32_32x32x16_bf16 v[16:31], v[72:75], a[84:87], v[16:31]
s_waitcnt lgkmcnt(6)
v_mfma_f32_32x32x16_bf16 v[32:47], v[76:79], a[68:71], v[32:47]
ds_read_b64_tr_b16 v[92:93], v105 offset:14848
ds_read_b64_tr_b16 v[94:95], v105 offset:15872
v_add_u32 v105, 0x10000, v105
v_mfma_f32_32x32x16_bf16 v[48:63], v[76:79], a[84:87], v[48:63]
s_waitcnt lgkmcnt(6)
v_mfma_f32_32x32x16_bf16 v[0:15], v[80:83], a[96:99], v[0:15]
v_mfma_f32_32x32x16_bf16 v[16:31], v[80:83], a[112:115], v[16:31]
s_waitcnt vmcnt(0)
s_waitcnt lgkmcnt(0)
s_barrier
v_mfma_f32_32x32x16_bf16 v[32:47], v[84:87], a[96:99], v[32:47]
ds_read_b64_tr_b16 v[64:65], v105 offset:0
ds_read_b64_tr_b16 v[66:67], v105 offset:1024
ds_read_b64_tr_b16 v[68:69], v105 offset:512
ds_read_b64_tr_b16 v[70:71], v105 offset:1536
v_mfma_f32_32x32x16_bf16 v[48:63], v[84:87], a[112:115], v[48:63]
s_add_u32 s67, s97, 8192
v_cvt_pk_bf16_f32 v128, v128, v129
v_cvt_pk_bf16_f32 v129, v130, v131
v_cvt_pk_bf16_f32 v130, v132, v133
v_cvt_pk_bf16_f32 v131, v134, v135
buffer_store_dwordx4 v[128:131], v127, s[56:59], s67 offen offset:0 sc1
v_mfma_f32_32x32x16_bf16 v[0:15], v[88:91], a[100:103], v[0:15]
ds_read_b64_tr_b16 v[72:73], v105 offset:2048
ds_read_b64_tr_b16 v[74:75], v105 offset:3072
v_mfma_f32_32x32x16_bf16 v[16:31], v[88:91], a[116:119], v[16:31]
v_cvt_pk_bf16_f32 v136, v136, v137
v_cvt_pk_bf16_f32 v137, v138, v139
v_cvt_pk_bf16_f32 v138, v140, v141
v_cvt_pk_bf16_f32 v139, v142, v143
buffer_store_dwordx4 v[136:139], v127, s[56:59], s67 offen offset:1024 sc1
v_mfma_f32_32x32x16_bf16 v[32:47], v[92:95], a[100:103], v[32:47]
ds_read_b64_tr_b16 v[76:77], v105 offset:2560
ds_read_b64_tr_b16 v[78:79], v105 offset:3584
v_mfma_f32_32x32x16_bf16 v[48:63], v[92:95], a[116:119], v[48:63]
v_cvt_pk_bf16_f32 v144, v144, v145
v_cvt_pk_bf16_f32 v145, v146, v147
v_cvt_pk_bf16_f32 v146, v148, v149
v_cvt_pk_bf16_f32 v147, v150, v151
s_mov_b64 exec, s[50:51]
buffer_store_dwordx4 v[144:147], v127, s[56:59], s67 offen offset:2048 sc1
s_mov_b64 exec, -1
s_waitcnt lgkmcnt(6)
v_mfma_f32_32x32x16_bf16 v[0:15], v[64:67], a[8:11], v[0:15]
ds_read_b64_tr_b16 v[80:81], v105 offset:4096
ds_read_b64_tr_b16 v[82:83], v105 offset:5120
v_mfma_f32_32x32x16_bf16 v[16:31], v[64:67], a[24:27], v[16:31]
v_cvt_pk_bf16_f32 v152, v152, v153
v_cvt_pk_bf16_f32 v153, v154, v155
v_cvt_pk_bf16_f32 v154, v156, v157
v_cvt_pk_bf16_f32 v155, v158, v159
s_mov_b64 exec, s[50:51]
buffer_store_dwordx4 v[152:155], v127, s[56:59], s67 offen offset:3072 sc1
s_mov_b64 exec, -1
s_waitcnt lgkmcnt(6)
v_mfma_f32_32x32x16_bf16 v[32:47], v[68:71], a[8:11], v[32:47]
ds_read_b64_tr_b16 v[84:85], v105 offset:4608
ds_read_b64_tr_b16 v[86:87], v105 offset:5632
v_mfma_f32_32x32x16_bf16 v[48:63], v[68:71], a[24:27], v[48:63]
s_add_u32 s67, s97, 12288
v_cvt_pk_bf16_f32 v160, v160, v161
v_cvt_pk_bf16_f32 v161, v162, v163
v_cvt_pk_bf16_f32 v162, v164, v165
v_cvt_pk_bf16_f32 v163, v166, v167
buffer_store_dwordx4 v[160:163], v127, s[56:59], s67 offen offset:0 sc1
s_waitcnt lgkmcnt(6)
v_mfma_f32_32x32x16_bf16 v[0:15], v[72:75], a[12:15], v[0:15]
ds_read_b64_tr_b16 v[88:89], v105 offset:6144
ds_read_b64_tr_b16 v[90:91], v105 offset:7168
v_mfma_f32_32x32x16_bf16 v[16:31], v[72:75], a[28:31], v[16:31]
v_cvt_pk_bf16_f32 v168, v168, v169
v_cvt_pk_bf16_f32 v169, v170, v171
v_cvt_pk_bf16_f32 v170, v172, v173
v_cvt_pk_bf16_f32 v171, v174, v175
buffer_store_dwordx4 v[168:171], v127, s[56:59], s67 offen offset:1024 sc1
s_waitcnt lgkmcnt(6)
v_mfma_f32_32x32x16_bf16 v[32:47], v[76:79], a[12:15], v[32:47]
ds_read_b64_tr_b16 v[92:93], v105 offset:6656
ds_read_b64_tr_b16 v[94:95], v105 offset:7680
v_mfma_f32_32x32x16_bf16 v[48:63], v[76:79], a[28:31], v[48:63]
v_cvt_pk_bf16_f32 v176, v176, v177
v_cvt_pk_bf16_f32 v177, v178, v179
v_cvt_pk_bf16_f32 v178, v180, v181
v_cvt_pk_bf16_f32 v179, v182, v183
s_mov_b64 exec, s[50:51]
buffer_store_dwordx4 v[176:179], v127, s[56:59], s67 offen offset:2048 sc1
s_mov_b64 exec, -1
s_waitcnt lgkmcnt(6)
v_mfma_f32_32x32x16_bf16 v[0:15], v[80:83], a[40:43], v[0:15]
ds_read_b64_tr_b16 v[64:65], v105 offset:8192
ds_read_b64_tr_b16 v[66:67], v105 offset:9216
v_mfma_f32_32x32x16_bf16 v[16:31], v[80:83], a[56:59], v[16:31]
v_cvt_pk_bf16_f32 v184, v184, v185
v_cvt_pk_bf16_f32 v185, v186, v187
v_cvt_pk_bf16_f32 v186, v188, v189
v_cvt_pk_bf16_f32 v187, v190, v191
s_mov_b64 exec, s[50:51]
buffer_store_dwordx4 v[184:187], v127, s[56:59], s67 offen offset:3072 sc1
s_mov_b64 exec, -1
s_waitcnt lgkmcnt(6)
v_mfma_f32_32x32x16_bf16 v[32:47], v[84:87], a[40:43], v[32:47]
ds_read_b64_tr_b16 v[68:69], v105 offset:8704
ds_read_b64_tr_b16 v[70:71], v105 offset:9728
v_mfma_f32_32x32x16_bf16 v[48:63], v[84:87], a[56:59], v[48:63]
s_waitcnt lgkmcnt(6)
v_mfma_f32_32x32x16_bf16 v[0:15], v[88:91], a[44:47], v[0:15]
ds_read_b64_tr_b16 v[72:73], v105 offset:10240
ds_read_b64_tr_b16 v[74:75], v105 offset:11264
v_mfma_f32_32x32x16_bf16 v[16:31], v[88:91], a[60:63], v[16:31]
s_waitcnt lgkmcnt(6)
v_mfma_f32_32x32x16_bf16 v[32:47], v[92:95], a[44:47], v[32:47]
ds_read_b64_tr_b16 v[76:77], v105 offset:10752
ds_read_b64_tr_b16 v[78:79], v105 offset:11776
v_mfma_f32_32x32x16_bf16 v[48:63], v[92:95], a[60:63], v[48:63]
s_waitcnt lgkmcnt(6)
v_mfma_f32_32x32x16_bf16 v[0:15], v[64:67], a[72:75], v[0:15]
ds_read_b64_tr_b16 v[80:81], v105 offset:12288
ds_read_b64_tr_b16 v[82:83], v105 offset:13312
v_mfma_f32_32x32x16_bf16 v[16:31], v[64:67], a[88:91], v[16:31]
s_waitcnt lgkmcnt(6)
v_mfma_f32_32x32x16_bf16 v[32:47], v[68:71], a[72:75], v[32:47]
ds_read_b64_tr_b16 v[84:85], v105 offset:12800
ds_read_b64_tr_b16 v[86:87], v105 offset:13824
v_mfma_f32_32x32x16_bf16 v[48:63], v[68:71], a[88:91], v[48:63]
s_waitcnt lgkmcnt(6)
v_mfma_f32_32x32x16_bf16 v[0:15], v[72:75], a[76:79], v[0:15]
ds_read_b64_tr_b16 v[88:89], v105 offset:14336
ds_read_b64_tr_b16 v[90:91], v105 offset:15360
v_mfma_f32_32x32x16_bf16 v[16:31], v[72:75], a[92:95], v[16:31]
s_waitcnt lgkmcnt(6)
v_mfma_f32_32x32x16_bf16 v[32:47], v[76:79], a[76:79], v[32:47]
ds_read_b64_tr_b16 v[92:93], v105 offset:14848
ds_read_b64_tr_b16 v[94:95], v105 offset:15872
v_add_u32 v105, 0x8000, v105
v_mfma_f32_32x32x16_bf16 v[48:63], v[76:79], a[92:95], v[48:63]
s_waitcnt lgkmcnt(6)
v_mfma_f32_32x32x16_bf16 v[0:15], v[80:83], a[104:107], v[0:15]
v_mfma_f32_32x32x16_bf16 v[16:31], v[80:83], a[120:123], v[16:31]
s_waitcnt lgkmcnt(4)
v_mfma_f32_32x32x16_bf16 v[32:47], v[84:87], a[104:107], v[32:47]
ds_read_b64_tr_b16 v[64:65], v105 offset:0
ds_read_b64_tr_b16 v[66:67], v105 offset:1024
ds_read_b64_tr_b16 v[68:69], v105 offset:512
ds_read_b64_tr_b16 v[70:71], v105 offset:1536
v_mfma_f32_32x32x16_bf16 v[48:63], v[84:87], a[120:123], v[48:63]
s_waitcnt lgkmcnt(6)
v_mfma_f32_32x32x16_bf16 v[0:15], v[88:91], a[108:111], v[0:15]
ds_read_b64_tr_b16 v[72:73], v105 offset:2048
ds_read_b64_tr_b16 v[74:75], v105 offset:3072
v_mfma_f32_32x32x16_bf16 v[16:31], v[88:91], a[124:127], v[16:31]
s_waitcnt lgkmcnt(6)
v_mfma_f32_32x32x16_bf16 v[32:47], v[92:95], a[108:111], v[32:47]
ds_read_b64_tr_b16 v[76:77], v105 offset:2560
ds_read_b64_tr_b16 v[78:79], v105 offset:3584
v_mfma_f32_32x32x16_bf16 v[48:63], v[92:95], a[124:127], v[48:63]
s_waitcnt lgkmcnt(6)
v_mfma_f32_32x32x16_bf16 v[0:15], v[64:67], a[128:131], v[0:15]
ds_read_b64_tr_b16 v[80:81], v105 offset:4096
ds_read_b64_tr_b16 v[82:83], v105 offset:5120
v_mfma_f32_32x32x16_bf16 v[16:31], v[64:67], a[144:147], v[16:31]
s_waitcnt lgkmcnt(6)
v_mfma_f32_32x32x16_bf16 v[32:47], v[68:71], a[128:131], v[32:47]
ds_read_b64_tr_b16 v[84:85], v105 offset:4608
ds_read_b64_tr_b16 v[86:87], v105 offset:5632
v_mfma_f32_32x32x16_bf16 v[48:63], v[68:71], a[144:147], v[48:63]
s_add_u32 s67, s97, 16384
v_cvt_pk_bf16_f32 v192, v192, v193
v_cvt_pk_bf16_f32 v193, v194, v195
v_cvt_pk_bf16_f32 v194, v196, v197
v_cvt_pk_bf16_f32 v195, v198, v199
buffer_store_dwordx4 v[192:195], v127, s[56:59], s67 offen offset:0 sc1
s_waitcnt lgkmcnt(6)
v_mfma_f32_32x32x16_bf16 v[0:15], v[72:75], a[132:135], v[0:15]
ds_read_b64_tr_b16 v[88:89], v105 offset:6144
ds_read_b64_tr_b16 v[90:91], v105 offset:7168
v_mfma_f32_32x32x16_bf16 v[16:31], v[72:75], a[148:151], v[16:31]
v_cvt_pk_bf16_f32 v200, v200, v201
v_cvt_pk_bf16_f32 v201, v202, v203
v_cvt_pk_bf16_f32 v202, v204, v205
v_cvt_pk_bf16_f32 v203, v206, v207
buffer_store_dwordx4 v[200:203], v127, s[56:59], s67 offen offset:1024 sc1
s_waitcnt lgkmcnt(6)
v_mfma_f32_32x32x16_bf16 v[32:47], v[76:79], a[132:135], v[32:47]
ds_read_b64_tr_b16 v[92:93], v105 offset:6656
ds_read_b64_tr_b16 v[94:95], v105 offset:7680
v_mfma_f32_32x32x16_bf16 v[48:63], v[76:79], a[148:151], v[48:63]
v_cvt_pk_bf16_f32 v208, v208, v209
v_cvt_pk_bf16_f32 v209, v210, v211
v_cvt_pk_bf16_f32 v210, v212, v213
v_cvt_pk_bf16_f32 v211, v214, v215
s_mov_b64 exec, s[50:51]
buffer_store_dwordx4 v[208:211], v127, s[56:59], s67 offen offset:2048 sc1
s_mov_b64 exec, -1
s_waitcnt lgkmcnt(6)
v_mfma_f32_32x32x16_bf16 v[0:15], v[80:83], a[160:163], v[0:15]
ds_read_b64_tr_b16 v[64:65], v105 offset:8192
ds_read_b64_tr_b16 v[66:67], v105 offset:9216
v_mfma_f32_32x32x16_bf16 v[16:31], v[80:83], a[176:179], v[16:31]
v_cvt_pk_bf16_f32 v216, v216, v217
v_cvt_pk_bf16_f32 v217, v218, v219
v_cvt_pk_bf16_f32 v218, v220, v221
v_cvt_pk_bf16_f32 v219, v222, v223
s_mov_b64 exec, s[50:51]
buffer_store_dwordx4 v[216:219], v127, s[56:59], s67 offen offset:3072 sc1
s_mov_b64 exec, -1
s_waitcnt lgkmcnt(6)
v_mfma_f32_32x32x16_bf16 v[32:47], v[84:87], a[160:163], v[32:47]
ds_read_b64_tr_b16 v[68:69], v105 offset:8704
ds_read_b64_tr_b16 v[70:71], v105 offset:9728
v_mfma_f32_32x32x16_bf16 v[48:63], v[84:87], a[176:179], v[48:63]
s_add_u32 s67, s97, 20480
v_cvt_pk_bf16_f32 v224, v224, v225
v_cvt_pk_bf16_f32 v225, v226, v227
v_cvt_pk_bf16_f32 v226, v228, v229
v_cvt_pk_bf16_f32 v227, v230, v231
buffer_store_dwordx4 v[224:227], v127, s[56:59], s67 offen offset:0 sc1
s_waitcnt lgkmcnt(6)
v_mfma_f32_32x32x16_bf16 v[0:15], v[88:91], a[164:167], v[0:15]
ds_read_b64_tr_b16 v[72:73], v105 offset:10240
ds_read_b64_tr_b16 v[74:75], v105 offset:11264
v_mfma_f32_32x32x16_bf16 v[16:31], v[88:91], a[180:183], v[16:31]
v_cvt_pk_bf16_f32 v232, v232, v233
v_cvt_pk_bf16_f32 v233, v234, v235
v_cvt_pk_bf16_f32 v234, v236, v237
v_cvt_pk_bf16_f32 v235, v238, v239
buffer_store_dwordx4 v[232:235], v127, s[56:59], s67 offen offset:1024 sc1
s_waitcnt lgkmcnt(6)
v_mfma_f32_32x32x16_bf16 v[32:47], v[92:95], a[164:167], v[32:47]
ds_read_b64_tr_b16 v[76:77], v105 offset:10752
ds_read_b64_tr_b16 v[78:79], v105 offset:11776
v_mfma_f32_32x32x16_bf16 v[48:63], v[92:95], a[180:183], v[48:63]
v_cvt_pk_bf16_f32 v240, v240, v241
v_cvt_pk_bf16_f32 v241, v242, v243
v_cvt_pk_bf16_f32 v242, v244, v245
v_cvt_pk_bf16_f32 v243, v246, v247
s_mov_b64 exec, s[50:51]
buffer_store_dwordx4 v[240:243], v127, s[56:59], s67 offen offset:2048 sc1
s_mov_b64 exec, -1
s_waitcnt lgkmcnt(6)
v_mfma_f32_32x32x16_bf16 v[0:15], v[64:67], a[192:195], v[0:15]
ds_read_b64_tr_b16 v[80:81], v105 offset:12288
ds_read_b64_tr_b16 v[82:83], v105 offset:13312
v_mfma_f32_32x32x16_bf16 v[16:31], v[64:67], a[208:211], v[16:31]
v_cvt_pk_bf16_f32 v248, v248, v249
v_cvt_pk_bf16_f32 v249, v250, v251
v_cvt_pk_bf16_f32 v250, v252, v253
v_cvt_pk_bf16_f32 v251, v254, v255
s_mov_b64 exec, s[50:51]
buffer_store_dwordx4 v[248:251], v127, s[56:59], s67 offen offset:3072 sc1
s_mov_b64 exec, -1
s_waitcnt lgkmcnt(6)
v_mfma_f32_32x32x16_bf16 v[32:47], v[68:71], a[192:195], v[32:47]
ds_read_b64_tr_b16 v[84:85], v105 offset:12800
ds_read_b64_tr_b16 v[86:87], v105 offset:13824
v_mfma_f32_32x32x16_bf16 v[48:63], v[68:71], a[208:211], v[48:63]
s_waitcnt lgkmcnt(6)
v_mfma_f32_32x32x16_bf16 v[0:15], v[72:75], a[196:199], v[0:15]
ds_read_b64_tr_b16 v[88:89], v105 offset:14336
ds_read_b64_tr_b16 v[90:91], v105 offset:15360
v_mfma_f32_32x32x16_bf16 v[16:31], v[72:75], a[212:215], v[16:31]
s_waitcnt lgkmcnt(6)
v_mfma_f32_32x32x16_bf16 v[32:47], v[76:79], a[196:199], v[32:47]
ds_read_b64_tr_b16 v[92:93], v105 offset:14848
ds_read_b64_tr_b16 v[94:95], v105 offset:15872
v_add_u32 v105, 0xffff0000, v105
v_mfma_f32_32x32x16_bf16 v[48:63], v[76:79], a[212:215], v[48:63]
s_waitcnt lgkmcnt(6)
v_mfma_f32_32x32x16_bf16 v[0:15], v[80:83], a[224:227], v[0:15]
v_mfma_f32_32x32x16_bf16 v[16:31], v[80:83], a[240:243], v[16:31]
s_waitcnt lgkmcnt(4)
v_mfma_f32_32x32x16_bf16 v[32:47], v[84:87], a[224:227], v[32:47]
ds_read_b64_tr_b16 v[64:65], v105 offset:0
ds_read_b64_tr_b16 v[66:67], v105 offset:1024
ds_read_b64_tr_b16 v[68:69], v105 offset:512
ds_read_b64_tr_b16 v[70:71], v105 offset:1536
v_mfma_f32_32x32x16_bf16 v[48:63], v[84:87], a[240:243], v[48:63]
s_waitcnt lgkmcnt(6)
v_mfma_f32_32x32x16_bf16 v[0:15], v[88:91], a[228:231], v[0:15]
ds_read_b64_tr_b16 v[72:73], v105 offset:2048
ds_read_b64_tr_b16 v[74:75], v105 offset:3072
v_mfma_f32_32x32x16_bf16 v[16:31], v[88:91], a[244:247], v[16:31]
s_waitcnt lgkmcnt(6)
v_mfma_f32_32x32x16_bf16 v[32:47], v[92:95], a[228:231], v[32:47]
ds_read_b64_tr_b16 v[76:77], v105 offset:2560
ds_read_b64_tr_b16 v[78:79], v105 offset:3584
v_mfma_f32_32x32x16_bf16 v[48:63], v[92:95], a[244:247], v[48:63]
s_waitcnt lgkmcnt(6)
v_mfma_f32_32x32x16_bf16 v[0:15], v[64:67], a[136:139], v[0:15]
ds_read_b64_tr_b16 v[80:81], v105 offset:4096
ds_read_b64_tr_b16 v[82:83], v105 offset:5120
v_mfma_f32_32x32x16_bf16 v[16:31], v[64:67], a[152:155], v[16:31]
s_waitcnt lgkmcnt(6)
v_mfma_f32_32x32x16_bf16 v[32:47], v[68:71], a[136:139], v[32:47]
ds_read_b64_tr_b16 v[84:85], v105 offset:4608
ds_read_b64_tr_b16 v[86:87], v105 offset:5632
v_mfma_f32_32x32x16_bf16 v[48:63], v[68:71], a[152:155], v[48:63]
s_waitcnt lgkmcnt(6)
v_mfma_f32_32x32x16_bf16 v[0:15], v[72:75], a[140:143], v[0:15]
ds_read_b64_tr_b16 v[88:89], v105 offset:6144
ds_read_b64_tr_b16 v[90:91], v105 offset:7168
v_mfma_f32_32x32x16_bf16 v[16:31], v[72:75], a[156:159], v[16:31]
s_waitcnt lgkmcnt(6)
v_mfma_f32_32x32x16_bf16 v[32:47], v[76:79], a[140:143], v[32:47]
ds_read_b64_tr_b16 v[92:93], v105 offset:6656
ds_read_b64_tr_b16 v[94:95], v105 offset:7680
v_mfma_f32_32x32x16_bf16 v[48:63], v[76:79], a[156:159], v[48:63]
s_waitcnt lgkmcnt(6)
v_mfma_f32_32x32x16_bf16 v[0:15], v[80:83], a[168:171], v[0:15]
buffer_load_dword v120, off, s[76:79], s94 offset:8 sc1
ds_read_b64_tr_b16 v[64:65], v105 offset:8192
ds_read_b64_tr_b16 v[66:67], v105 offset:9216
v_mfma_f32_32x32x16_bf16 v[16:31], v[80:83], a[184:187], v[16:31]
s_waitcnt lgkmcnt(6)
v_mfma_f32_32x32x16_bf16 v[32:47], v[84:87], a[168:171], v[32:47]
ds_read_b64_tr_b16 v[68:69], v105 offset:8704
ds_read_b64_tr_b16 v[70:71], v105 offset:9728
v_mfma_f32_32x32x16_bf16 v[48:63], v[84:87], a[184:187], v[48:63]
s_waitcnt lgkmcnt(6)
v_mfma_f32_32x32x16_bf16 v[0:15], v[88:91], a[172:175], v[0:15]
ds_read_b64_tr_b16 v[72:73], v105 offset:10240
ds_read_b64_tr_b16 v[74:75], v105 offset:11264
v_mfma_f32_32x32x16_bf16 v[16:31], v[88:91], a[188:191], v[16:31]
s_waitcnt lgkmcnt(6)
v_mfma_f32_32x32x16_bf16 v[32:47], v[92:95], a[172:175], v[32:47]
ds_read_b64_tr_b16 v[76:77], v105 offset:10752
ds_read_b64_tr_b16 v[78:79], v105 offset:11776
v_mfma_f32_32x32x16_bf16 v[48:63], v[92:95], a[188:191], v[48:63]
s_waitcnt lgkmcnt(6)
v_mfma_f32_32x32x16_bf16 v[0:15], v[64:67], a[200:203], v[0:15]
ds_read_b64_tr_b16 v[80:81], v105 offset:12288
ds_read_b64_tr_b16 v[82:83], v105 offset:13312
v_mfma_f32_32x32x16_bf16 v[16:31], v[64:67], a[216:219], v[16:31]
s_waitcnt lgkmcnt(6)
v_mfma_f32_32x32x16_bf16 v[32:47], v[68:71], a[200:203], v[32:47]
ds_read_b64_tr_b16 v[84:85], v105 offset:12800
ds_read_b64_tr_b16 v[86:87], v105 offset:13824
v_mfma_f32_32x32x16_bf16 v[48:63], v[68:71], a[216:219], v[48:63]
s_waitcnt lgkmcnt(6)
v_mfma_f32_32x32x16_bf16 v[0:15], v[72:75], a[204:207], v[0:15]
ds_read_b64_tr_b16 v[88:89], v105 offset:14336
ds_read_b64_tr_b16 v[90:91], v105 offset:15360
v_mfma_f32_32x32x16_bf16 v[16:31], v[72:75], a[220:223], v[16:31]
s_waitcnt lgkmcnt(6)
v_mfma_f32_32x32x16_bf16 v[32:47], v[76:79], a[204:207], v[32:47]
ds_read_b64_tr_b16 v[92:93], v105 offset:14848
ds_read_b64_tr_b16 v[94:95], v105 offset:15872
v_mfma_f32_32x32x16_bf16 v[48:63], v[76:79], a[220:223], v[48:63]
s_waitcnt lgkmcnt(6)
v_mfma_f32_32x32x16_bf16 v[0:15], v[80:83], a[232:235], v[0:15]
v_mfma_f32_32x32x16_bf16 v[16:31], v[80:83], a[248:251], v[16:31]
s_waitcnt lgkmcnt(4)
v_mfma_f32_32x32x16_bf16 v[32:47], v[84:87], a[232:235], v[32:47]
v_mfma_f32_32x32x16_bf16 v[48:63], v[84:87], a[248:251], v[48:63]
s_waitcnt lgkmcnt(2)
v_mfma_f32_32x32x16_bf16 v[0:15], v[88:91], a[236:239], v[0:15]
v_mfma_f32_32x32x16_bf16 v[16:31], v[88:91], a[252:255], v[16:31]
s_waitcnt lgkmcnt(0)
v_mfma_f32_32x32x16_bf16 v[32:47], v[92:95], a[236:239], v[32:47]
v_mfma_f32_32x32x16_bf16 v[48:63], v[92:95], a[252:255], v[48:63]
s_nop 11
s_add_u32 s67, s97, 24576
v_cvt_pk_bf16_f32 v0, v0, v1
v_cvt_pk_bf16_f32 v1, v2, v3
v_cvt_pk_bf16_f32 v2, v4, v5
v_cvt_pk_bf16_f32 v3, v6, v7
buffer_store_dwordx4 v[0:3], v127, s[56:59], s67 offen offset:0 sc1
v_cvt_pk_bf16_f32 v8, v8, v9
v_cvt_pk_bf16_f32 v9, v10, v11
v_cvt_pk_bf16_f32 v10, v12, v13
v_cvt_pk_bf16_f32 v11, v14, v15
buffer_store_dwordx4 v[8:11], v127, s[56:59], s67 offen offset:1024 sc1
v_cvt_pk_bf16_f32 v16, v16, v17
v_cvt_pk_bf16_f32 v17, v18, v19
v_cvt_pk_bf16_f32 v18, v20, v21
v_cvt_pk_bf16_f32 v19, v22, v23
s_mov_b64 exec, s[50:51]
buffer_store_dwordx4 v[16:19], v127, s[56:59], s67 offen offset:2048 sc1
s_mov_b64 exec, -1
v_cvt_pk_bf16_f32 v24, v24, v25
v_cvt_pk_bf16_f32 v25, v26, v27
v_cvt_pk_bf16_f32 v26, v28, v29
v_cvt_pk_bf16_f32 v27, v30, v31
s_mov_b64 exec, s[50:51]
buffer_store_dwordx4 v[24:27], v127, s[56:59], s67 offen offset:3072 sc1
s_mov_b64 exec, -1
s_add_u32 s67, s97, 28672
v_cvt_pk_bf16_f32 v32, v32, v33
v_cvt_pk_bf16_f32 v33, v34, v35
v_cvt_pk_bf16_f32 v34, v36, v37
v_cvt_pk_bf16_f32 v35, v38, v39
buffer_store_dwordx4 v[32:35], v127, s[56:59], s67 offen offset:0 sc1
v_cvt_pk_bf16_f32 v40, v40, v41
v_cvt_pk_bf16_f32 v41, v42, v43
v_cvt_pk_bf16_f32 v42, v44, v45
v_cvt_pk_bf16_f32 v43, v46, v47
buffer_store_dwordx4 v[40:43], v127, s[56:59], s67 offen offset:1024 sc1
v_cvt_pk_bf16_f32 v48, v48, v49
v_cvt_pk_bf16_f32 v49, v50, v51
v_cvt_pk_bf16_f32 v50, v52, v53
v_cvt_pk_bf16_f32 v51, v54, v55
s_mov_b64 exec, s[50:51]
buffer_store_dwordx4 v[48:51], v127, s[56:59], s67 offen offset:2048 sc1
s_mov_b64 exec, -1
v_cvt_pk_bf16_f32 v56, v56, v57
v_cvt_pk_bf16_f32 v57, v58, v59
v_cvt_pk_bf16_f32 v58, v60, v61
v_cvt_pk_bf16_f32 v59, v62, v63
s_mov_b64 exec, s[50:51]
buffer_store_dwordx4 v[56:59], v127, s[56:59], s67 offen offset:3072 sc1
s_mov_b64 exec, -1
s_waitcnt vmcnt(8)
v_readfirstlane_b32 s65, v120
s_xor_b32 s65, s65, s69
s_or_b32 s65, s65, s90
s_cbranch_scc0 L_sp2
L_spd2:
s_waitcnt vmcnt(0)
s_branch L_end
L_cend:
L_tr_0:
s_mov_b32 s70, 0
s_mov_b32 s71, 1
s_mov_b32 s75, 0
s_getpc_b64 s[72:73]
s_branch L_rare_0_0
s_branch L_ret_0
L_tr_1:
s_mov_b32 s70, 8192
s_mov_b32 s71, 0
s_mov_b32 s75, 0
s_getpc_b64 s[72:73]
s_branch L_rare_0_1
s_branch L_ret_1
L_tr_2:
s_mov_b32 s70, 32768
s_mov_b32 s71, 0
s_mov_b32 s75, 0
s_getpc_b64 s[72:73]
s_branch L_rare_0_0
s_branch L_ret_2
L_tr_3:
s_mov_b32 s70, 40960
s_mov_b32 s71, 0
s_mov_b32 s75, 0
s_getpc_b64 s[72:73]
s_branch L_rare_0_1
s_branch L_ret_3
L_tr_4:
s_mov_b32 s70, 65536
s_mov_b32 s71, 0
s_mov_b32 s75, 0
s_getpc_b64 s[72:73]
s_branch L_rare_0_0
s_branch L_ret_4
L_tr_5:
s_mov_b32 s70, 73728
s_mov_b32 s71, 0
s_mov_b32 s75, 0
s_getpc_b64 s[72:73]
s_branch L_rare_0_1
s_branch L_ret_5
L_tr_6:
s_mov_b32 s70, 98304
s_mov_b32 s71, 0
s_mov_b32 s75, 0
s_getpc_b64 s[72:73]
s_branch L_rare_0_0
s_branch L_ret_6
L_tr_7:
s_mov_b32 s70, 106496
s_mov_b32 s71, 0
s_mov_b32 s75, 0
s_getpc_b64 s[72:73]
s_branch L_rare_0_1
s_branch L_ret_7
L_tr_8:
s_mov_b32 s70, 131072
s_mov_b32 s71, 0
s_mov_b32 s75, 0
s_getpc_b64 s[72:73]
s_branch L_rare_0_0
s_branch L_ret_8
L_tr_9:
s_mov_b32 s70, 139264
s_mov_b32 s71, 0
s_mov_b32 s75, 0
s_getpc_b64 s[72:73]
s_branch L_rare_0_1
s_branch L_ret_9
L_tr_10:
s_mov_b32 s70, 0
s_mov_b32 s71, 0
s_mov_b32 s75, 0
s_getpc_b64 s[72:73]
s_branch L_rare_0_0
s_branch L_ret_10
L_tr_11:
s_mov_b32 s70, 8192
s_mov_b32 s71, 0
s_mov_b32 s75, 0
s_getpc_b64 s[72:73]
s_branch L_rare_0_1
s_branch L_ret_11
L_tr_12:
s_mov_b32 s70, 32768
s_mov_b32 s71, 0
s_mov_b32 s75, 0
s_getpc_b64 s[72:73]
s_branch L_rare_0_0
s_branch L_ret_12
L_tr_13:
s_mov_b32 s70, 40960
s_mov_b32 s71, 0
s_mov_b32 s75, 0
s_getpc_b64 s[72:73]
s_branch L_rare_0_1
s_branch L_ret_13
L_tr_14:
s_mov_b32 s70, 65536
s_mov_b32 s71, 0
s_mov_b32 s75, 0
s_getpc_b64 s[72:73]
s_branch L_rare_0_0
s_branch L_ret_14
L_tr_15:
s_mov_b32 s70, 73728
s_mov_b32 s71, 0
s_mov_b32 s75, 1
s_getpc_b64 s[72:73]
s_branch L_rare_0_1
s_branch L_ret_15
L_tr_16:
s_mov_b32 s70, 98304
s_mov_b32 s71, 1
s_mov_b32 s75, 0
s_getpc_b64 s[72:73]
s_branch L_rare_1_0
s_branch L_ret_16
L_tr_17:
s_mov_b32 s70, 106496
s_mov_b32 s71, 0
s_mov_b32 s75, 0
s_getpc_b64 s[72:73]
s_branch L_rare_1_1
s_branch L_ret_17
L_tr_18:
s_mov_b32 s70, 131072
s_mov_b32 s71, 0
s_mov_b32 s75, 0
s_getpc_b64 s[72:73]
s_branch L_rare_1_0
s_branch L_ret_18
L_tr_19:
s_mov_b32 s70, 139264
s_mov_b32 s71, 0
s_mov_b32 s75, 0
s_getpc_b64 s[72:73]
s_branch L_rare_1_1
s_branch L_ret_19
L_tr_20:
s_mov_b32 s70, 0
s_mov_b32 s71, 0
s_mov_b32 s75, 0
s_getpc_b64 s[72:73]
s_branch L_rare_1_0
s_branch L_ret_20
L_tr_21:
s_mov_b32 s70, 8192
s_mov_b32 s71, 0
s_mov_b32 s75, 0
s_getpc_b64 s[72:73]
s_branch L_rare_1_1
s_branch L_ret_21
L_tr_22:
s_mov_b32 s70, 32768
s_mov_b32 s71, 0
s_mov_b32 s75, 0
s_getpc_b64 s[72:73]
s_branch L_rare_1_0
s_branch L_ret_22
L_tr_23:
s_mov_b32 s70, 40960
s_mov_b32 s71, 0
s_mov_b32 s75, 0
s_getpc_b64 s[72:73]
s_branch L_rare_1_1
s_branch L_ret_23
L_tr_24:
s_mov_b32 s70, 65536
s_mov_b32 s71, 0
s_mov_b32 s75, 0
s_getpc_b64 s[72:73]
s_branch L_rare_1_0
s_branch L_ret_24
L_tr_25:
s_mov_b32 s70, 73728
s_mov_b32 s71, 0
s_mov_b32 s75, 0
s_getpc_b64 s[72:73]
s_branch L_rare_1_1
s_branch L_ret_25
L_tr_26:
s_mov_b32 s70, 98304
s_mov_b32 s71, 0
s_mov_b32 s75, 0
s_getpc_b64 s[72:73]
s_branch L_rare_1_0
s_branch L_ret_26
L_tr_27:
s_mov_b32 s70, 106496
s_mov_b32 s71, 0
s_mov_b32 s75, 0
s_getpc_b64 s[72:73]
s_branch L_rare_1_1
s_branch L_ret_27
L_tr_28:
s_mov_b32 s70, 131072
s_mov_b32 s71, 0
s_mov_b32 s75, 0
s_getpc_b64 s[72:73]
s_branch L_rare_1_0
s_branch L_ret_28
L_tr_29:
s_mov_b32 s70, 139264
s_mov_b32 s71, 0
s_mov_b32 s75, 0
s_getpc_b64 s[72:73]
s_branch L_rare_1_1
s_branch L_ret_29
L_tr_30:
s_mov_b32 s70, 0
s_mov_b32 s71, 0
s_mov_b32 s75, 0
s_getpc_b64 s[72:73]
s_branch L_rare_1_0
s_branch L_ret_30
L_tr_31:
s_mov_b32 s70, 8192
s_mov_b32 s71, 0
s_mov_b32 s75, 1
s_getpc_b64 s[72:73]
s_branch L_rare_1_1
s_branch L_ret_31
L_rare_0_0:
s_waitcnt lgkmcnt(0)
s_nop 15
v_mbcnt_lo_u32_b32 v160, -1, 0
v_mbcnt_hi_u32_b32 v160, -1, v160
v_and_b32 v161, 31, v160
v_lshrrev_b32 v162, 5, v160
v_and_b32 v163, 15, v161
v_or_b32 v164, 0, v162
v_xor_b32 v163, v163, v164
v_lshlrev_b32 v163, 4, v163
v_lshl_add_u32 v163, v161, 8, v163
v_add_u32 v163, s70, v163
ds_read_b128 v[176:179], v163 offset:0
s_waitcnt lgkmcnt(0)
v_mfma_f32_32x32x16_bf16 v[0:15], v[176:179], a[128:131], 0
v_mfma_f32_32x32x16_bf16 v[16:31], v[176:179], a[160:163], 0
v_and_b32 v163, 15, v161
v_or_b32 v164, 2, v162
v_xor_b32 v163, v163, v164
v_lshlrev_b32 v163, 4, v163
v_lshl_add_u32 v163, v161, 8, v163
v_add_u32 v163, s70, v163
ds_read_b128 v[180:183], v163 offset:0
s_waitcnt lgkmcnt(0)
v_mfma_f32_32x32x16_bf16 v[0:15], v[180:183], a[132:135], v[0:15]
v_mfma_f32_32x32x16_bf16 v[16:31], v[180:183], a[164:167], v[16:31]
v_and_b32 v163, 15, v161
v_or_b32 v164, 4, v162
v_xor_b32 v163, v163, v164
v_lshlrev_b32 v163, 4, v163
v_lshl_add_u32 v163, v161, 8, v163
v_add_u32 v163, s70, v163
ds_read_b128 v[184:187], v163 offset:0
s_waitcnt lgkmcnt(0)
v_mfma_f32_32x32x16_bf16 v[0:15], v[184:187], a[136:139], v[0:15]
v_mfma_f32_32x32x16_bf16 v[16:31], v[184:187], a[168:171], v[16:31]
v_and_b32 v163, 15, v161
v_or_b32 v164, 6, v162
v_xor_b32 v163, v163, v164
v_lshlrev_b32 v163, 4, v163
v_lshl_add_u32 v163, v161, 8, v163
v_add_u32 v163, s70, v163
ds_read_b128 v[188:191], v163 offset:0
s_waitcnt lgkmcnt(0)
v_mfma_f32_32x32x16_bf16 v[0:15], v[188:191], a[140:143], v[0:15]
v_mfma_f32_32x32x16_bf16 v[16:31], v[188:191], a[172:175], v[16:31]
v_and_b32 v163, 15, v161
v_or_b32 v164, 8, v162
v_xor_b32 v163, v163, v164
v_lshlrev_b32 v163, 4, v163
v_lshl_add_u32 v163, v161, 8, v163
v_add_u32 v163, s70, v163
ds_read_b128 v[176:179], v163 offset:0
s_waitcnt lgkmcnt(0)
v_mfma_f32_32x32x16_bf16 v[0:15], v[176:179], a[144:147], v[0:15]
v_mfma_f32_32x32x16_bf16 v[16:31], v[176:179], a[176:179], v[16:31]
v_and_b32 v163, 15, v161
v_or_b32 v164, 10, v162
v_xor_b32 v163, v163, v164
v_lshlrev_b32 v163, 4, v163
v_lshl_add_u32 v163, v161, 8, v163
v_add_u32 v163, s70, v163
ds_read_b128 v[180:183], v163 offset:0
s_waitcnt lgkmcnt(0)
v_mfma_f32_32x32x16_bf16 v[0:15], v[180:183], a[148:151], v[0:15]
v_mfma_f32_32x32x16_bf16 v[16:31], v[180:183], a[180:183], v[16:31]
v_and_b32 v163, 15, v161
v_or_b32 v164, 12, v162
v_xor_b32 v163, v163, v164
v_lshlrev_b32 v163, 4, v163
v_lshl_add_u32 v163, v161, 8, v163
v_add_u32 v163, s70, v163
ds_read_b128 v[184:187], v163 offset:0
s_waitcnt lgkmcnt(0)
v_mfma_f32_32x32x16_bf16 v[0:15], v[184:187], a[152:155], v[0:15]
v_mfma_f32_32x32x16_bf16 v[16:31], v[184:187], a[184:187], v[16:31]
v_and_b32 v163, 15, v161
v_or_b32 v164, 14, v162
v_xor_b32 v163, v163, v164
v_lshlrev_b32 v163, 4, v163
v_lshl_add_u32 v163, v161, 8, v163
v_add_u32 v163, s70, v163
ds_read_b128 v[188:191], v163 offset:0
s_waitcnt lgkmcnt(0)
v_mfma_f32_32x32x16_bf16 v[0:15], v[188:191], a[156:159], v[0:15]
v_mfma_f32_32x32x16_bf16 v[16:31], v[188:191], a[188:191], v[16:31]
s_nop 15
v_max3_f32 v165, v0, v1, v2
v_max3_f32 v165, v165, v3, v4
v_max3_f32 v165, v165, v5, v6
v_max3_f32 v165, v165, v7, v8
v_max3_f32 v165, v165, v9, v10
v_max3_f32 v165, v165, v11, v12
v_max3_f32 v165, v165, v13, v14
v_max_f32 v165, v165, v15
v_mov_b32 v163, v165
s_nop 1
v_permlane32_swap_b32 v165, v163
v_max_f32 v165, v165, v163
v_max3_f32 v166, v16, v17, v18
v_max3_f32 v166, v166, v19, v20
v_max3_f32 v166, v166, v21, v22
v_max3_f32 v166, v166, v23, v24
v_max3_f32 v166, v166, v25, v26
v_max3_f32 v166, v166, v27, v28
v_max3_f32 v166, v166, v29, v30
v_max_f32 v166, v166, v31
v_mov_b32 v163, v166
s_nop 1
v_permlane32_swap_b32 v166, v163
v_max_f32 v166, v166, v163
s_cmp_eq_u32 s71, 1
s_cbranch_scc1 L_rf_0_0
v_max_f32 v165, v165, v110
v_max_f32 v166, v166, v111
L_rf_0_0:
v_sub_f32 v167, v110, v165
v_min_f32 v163, 0, v167
v_exp_f32 v170, v163
s_nop 0
v_mov_b32 v171, v170
v_mul_f32 v106, v106, v170
v_mov_b32 v110, v165
v_sub_f32 v128, 0, v165
v_sub_f32 v129, 0, v165
v_sub_f32 v130, 0, v165
v_sub_f32 v131, 0, v165
v_sub_f32 v132, 0, v165
v_sub_f32 v133, 0, v165
v_sub_f32 v134, 0, v165
v_sub_f32 v135, 0, v165
v_sub_f32 v136, 0, v165
v_sub_f32 v137, 0, v165
v_sub_f32 v138, 0, v165
v_sub_f32 v139, 0, v165
v_sub_f32 v140, 0, v165
v_sub_f32 v141, 0, v165
v_sub_f32 v142, 0, v165
v_sub_f32 v143, 0, v165
v_sub_f32 v168, v111, v166
v_min_f32 v163, 0, v168
v_exp_f32 v172, v163
s_nop 0
v_mov_b32 v173, v172
v_mul_f32 v107, v107, v172
v_mov_b32 v111, v166
v_sub_f32 v144, 0, v166
v_sub_f32 v145, 0, v166
v_sub_f32 v146, 0, v166
v_sub_f32 v147, 0, v166
v_sub_f32 v148, 0, v166
v_sub_f32 v149, 0, v166
v_sub_f32 v150, 0, v166
v_sub_f32 v151, 0, v166
v_sub_f32 v152, 0, v166
v_sub_f32 v153, 0, v166
v_sub_f32 v154, 0, v166
v_sub_f32 v155, 0, v166
v_sub_f32 v156, 0, v166
v_sub_f32 v157, 0, v166
v_sub_f32 v158, 0, v166
v_sub_f32 v159, 0, v166
s_cmp_eq_u32 s71, 1
s_cbranch_scc1 L_rs_0_0
v_accvgpr_read_b32 v176, a0
v_accvgpr_read_b32 v177, a1
v_pk_mul_f32 v[176:177], v[176:177], v[170:171]
v_accvgpr_write_b32 a0, v176
v_accvgpr_write_b32 a1, v177
v_accvgpr_read_b32 v178, a2
v_accvgpr_read_b32 v179, a3
v_pk_mul_f32 v[178:179], v[178:179], v[170:171]
v_accvgpr_write_b32 a2, v178
v_accvgpr_write_b32 a3, v179
v_accvgpr_read_b32 v180, a4
v_accvgpr_read_b32 v181, a5
v_pk_mul_f32 v[180:181], v[180:181], v[170:171]
v_accvgpr_write_b32 a4, v180
v_accvgpr_write_b32 a5, v181
v_accvgpr_read_b32 v182, a6
v_accvgpr_read_b32 v183, a7
v_pk_mul_f32 v[182:183], v[182:183], v[170:171]
v_accvgpr_write_b32 a6, v182
v_accvgpr_write_b32 a7, v183
v_accvgpr_read_b32 v184, a8
v_accvgpr_read_b32 v185, a9
v_pk_mul_f32 v[184:185], v[184:185], v[170:171]
v_accvgpr_write_b32 a8, v184
v_accvgpr_write_b32 a9, v185
v_accvgpr_read_b32 v186, a10
v_accvgpr_read_b32 v187, a11
v_pk_mul_f32 v[186:187], v[186:187], v[170:171]
v_accvgpr_write_b32 a10, v186
v_accvgpr_write_b32 a11, v187
v_accvgpr_read_b32 v188, a12
v_accvgpr_read_b32 v189, a13
v_pk_mul_f32 v[188:189], v[188:189], v[170:171]
v_accvgpr_write_b32 a12, v188
v_accvgpr_write_b32 a13, v189
v_accvgpr_read_b32 v190, a14
v_accvgpr_read_b32 v191, a15
v_pk_mul_f32 v[190:191], v[190:191], v[170:171]
v_accvgpr_write_b32 a14, v190
v_accvgpr_write_b32 a15, v191
v_accvgpr_read_b32 v176, a16
v_accvgpr_read_b32 v177, a17
v_pk_mul_f32 v[176:177], v[176:177], v[172:173]
v_accvgpr_write_b32 a16, v176
v_accvgpr_write_b32 a17, v177
v_accvgpr_read_b32 v178, a18
v_accvgpr_read_b32 v179, a19
v_pk_mul_f32 v[178:179], v[178:179], v[172:173]
v_accvgpr_write_b32 a18, v178
v_accvgpr_write_b32 a19, v179
v_accvgpr_read_b32 v180, a20
v_accvgpr_read_b32 v181, a21
v_pk_mul_f32 v[180:181], v[180:181], v[172:173]
v_accvgpr_write_b32 a20, v180
v_accvgpr_write_b32 a21, v181
v_accvgpr_read_b32 v182, a22
v_accvgpr_read_b32 v183, a23
v_pk_mul_f32 v[182:183], v[182:183], v[172:173]
v_accvgpr_write_b32 a22, v182
v_accvgpr_write_b32 a23, v183
v_accvgpr_read_b32 v184, a24
v_accvgpr_read_b32 v185, a25
v_pk_mul_f32 v[184:185], v[184:185], v[172:173]
v_accvgpr_write_b32 a24, v184
v_accvgpr_write_b32 a25, v185
v_accvgpr_read_b32 v186, a26
v_accvgpr_read_b32 v187, a27
v_pk_mul_f32 v[186:187], v[186:187], v[172:173]
v_accvgpr_write_b32 a26, v186
v_accvgpr_write_b32 a27, v187
v_accvgpr_read_b32 v188, a28
v_accvgpr_read_b32 v189, a29
v_pk_mul_f32 v[188:189], v[188:189], v[172:173]
v_accvgpr_write_b32 a28, v188
v_accvgpr_write_b32 a29, v189
v_accvgpr_read_b32 v190, a30
v_accvgpr_read_b32 v191, a31
v_pk_mul_f32 v[190:191], v[190:191], v[172:173]
v_accvgpr_write_b32 a30, v190
v_accvgpr_write_b32 a31, v191
v_accvgpr_read_b32 v176, a32
v_accvgpr_read_b32 v177, a33
v_pk_mul_f32 v[176:177], v[176:177], v[170:171]
v_accvgpr_write_b32 a32, v176
v_accvgpr_write_b32 a33, v177
v_accvgpr_read_b32 v178, a34
v_accvgpr_read_b32 v179, a35
v_pk_mul_f32 v[178:179], v[178:179], v[170:171]
v_accvgpr_write_b32 a34, v178
v_accvgpr_write_b32 a35, v179
v_accvgpr_read_b32 v180, a36
v_accvgpr_read_b32 v181, a37
v_pk_mul_f32 v[180:181], v[180:181], v[170:171]
v_accvgpr_write_b32 a36, v180
v_accvgpr_write_b32 a37, v181
v_accvgpr_read_b32 v182, a38
v_accvgpr_read_b32 v183, a39
v_pk_mul_f32 v[182:183], v[182:183], v[170:171]
v_accvgpr_write_b32 a38, v182
v_accvgpr_write_b32 a39, v183
v_accvgpr_read_b32 v184, a40
v_accvgpr_read_b32 v185, a41
v_pk_mul_f32 v[184:185], v[184:185], v[170:171]
v_accvgpr_write_b32 a40, v184
v_accvgpr_write_b32 a41, v185
v_accvgpr_read_b32 v186, a42
v_accvgpr_read_b32 v187, a43
v_pk_mul_f32 v[186:187], v[186:187], v[170:171]
v_accvgpr_write_b32 a42, v186
v_accvgpr_write_b32 a43, v187
v_accvgpr_read_b32 v188, a44
v_accvgpr_read_b32 v189, a45
v_pk_mul_f32 v[188:189], v[188:189], v[170:171]
v_accvgpr_write_b32 a44, v188
v_accvgpr_write_b32 a45, v189
v_accvgpr_read_b32 v190, a46
v_accvgpr_read_b32 v191, a47
v_pk_mul_f32 v[190:191], v[190:191], v[170:171]
v_accvgpr_write_b32 a46, v190
v_accvgpr_write_b32 a47, v191
v_accvgpr_read_b32 v176, a48
v_accvgpr_read_b32 v177, a49
v_pk_mul_f32 v[176:177], v[176:177], v[172:173]
v_accvgpr_write_b32 a48, v176
v_accvgpr_write_b32 a49, v177
v_accvgpr_read_b32 v178, a50
v_accvgpr_read_b32 v179, a51
v_pk_mul_f32 v[178:179], v[178:179], v[172:173]
v_accvgpr_write_b32 a50, v178
v_accvgpr_write_b32 a51, v179
v_accvgpr_read_b32 v180, a52
v_accvgpr_read_b32 v181, a53
v_pk_mul_f32 v[180:181], v[180:181], v[172:173]
v_accvgpr_write_b32 a52, v180
v_accvgpr_write_b32 a53, v181
v_accvgpr_read_b32 v182, a54
v_accvgpr_read_b32 v183, a55
v_pk_mul_f32 v[182:183], v[182:183], v[172:173]
v_accvgpr_write_b32 a54, v182
v_accvgpr_write_b32 a55, v183
v_accvgpr_read_b32 v184, a56
v_accvgpr_read_b32 v185, a57
v_pk_mul_f32 v[184:185], v[184:185], v[172:173]
v_accvgpr_write_b32 a56, v184
v_accvgpr_write_b32 a57, v185
v_accvgpr_read_b32 v186, a58
v_accvgpr_read_b32 v187, a59
v_pk_mul_f32 v[186:187], v[186:187], v[172:173]
v_accvgpr_write_b32 a58, v186
v_accvgpr_write_b32 a59, v187
v_accvgpr_read_b32 v188, a60
v_accvgpr_read_b32 v189, a61
v_pk_mul_f32 v[188:189], v[188:189], v[172:173]
v_accvgpr_write_b32 a60, v188
v_accvgpr_write_b32 a61, v189
v_accvgpr_read_b32 v190, a62
v_accvgpr_read_b32 v191, a63
v_pk_mul_f32 v[190:191], v[190:191], v[172:173]
v_accvgpr_write_b32 a62, v190
v_accvgpr_write_b32 a63, v191
v_accvgpr_read_b32 v176, a64
v_accvgpr_read_b32 v177, a65
v_pk_mul_f32 v[176:177], v[176:177], v[170:171]
v_accvgpr_write_b32 a64, v176
v_accvgpr_write_b32 a65, v177
v_accvgpr_read_b32 v178, a66
v_accvgpr_read_b32 v179, a67
v_pk_mul_f32 v[178:179], v[178:179], v[170:171]
v_accvgpr_write_b32 a66, v178
v_accvgpr_write_b32 a67, v179
v_accvgpr_read_b32 v180, a68
v_accvgpr_read_b32 v181, a69
v_pk_mul_f32 v[180:181], v[180:181], v[170:171]
v_accvgpr_write_b32 a68, v180
v_accvgpr_write_b32 a69, v181
v_accvgpr_read_b32 v182, a70
v_accvgpr_read_b32 v183, a71
v_pk_mul_f32 v[182:183], v[182:183], v[170:171]
v_accvgpr_write_b32 a70, v182
v_accvgpr_write_b32 a71, v183
v_accvgpr_read_b32 v184, a72
v_accvgpr_read_b32 v185, a73
v_pk_mul_f32 v[184:185], v[184:185], v[170:171]
v_accvgpr_write_b32 a72, v184
v_accvgpr_write_b32 a73, v185
v_accvgpr_read_b32 v186, a74
v_accvgpr_read_b32 v187, a75
v_pk_mul_f32 v[186:187], v[186:187], v[170:171]
v_accvgpr_write_b32 a74, v186
v_accvgpr_write_b32 a75, v187
v_accvgpr_read_b32 v188, a76
v_accvgpr_read_b32 v189, a77
v_pk_mul_f32 v[188:189], v[188:189], v[170:171]
v_accvgpr_write_b32 a76, v188
v_accvgpr_write_b32 a77, v189
v_accvgpr_read_b32 v190, a78
v_accvgpr_read_b32 v191, a79
v_pk_mul_f32 v[190:191], v[190:191], v[170:171]
v_accvgpr_write_b32 a78, v190
v_accvgpr_write_b32 a79, v191
v_accvgpr_read_b32 v176, a80
v_accvgpr_read_b32 v177, a81
v_pk_mul_f32 v[176:177], v[176:177], v[172:173]
v_accvgpr_write_b32 a80, v176
v_accvgpr_write_b32 a81, v177
v_accvgpr_read_b32 v178, a82
v_accvgpr_read_b32 v179, a83
v_pk_mul_f32 v[178:179], v[178:179], v[172:173]
v_accvgpr_write_b32 a82, v178
v_accvgpr_write_b32 a83, v179
v_accvgpr_read_b32 v180, a84
v_accvgpr_read_b32 v181, a85
v_pk_mul_f32 v[180:181], v[180:181], v[172:173]
v_accvgpr_write_b32 a84, v180
v_accvgpr_write_b32 a85, v181
v_accvgpr_read_b32 v182, a86
v_accvgpr_read_b32 v183, a87
v_pk_mul_f32 v[182:183], v[182:183], v[172:173]
v_accvgpr_write_b32 a86, v182
v_accvgpr_write_b32 a87, v183
v_accvgpr_read_b32 v184, a88
v_accvgpr_read_b32 v185, a89
v_pk_mul_f32 v[184:185], v[184:185], v[172:173]
v_accvgpr_write_b32 a88, v184
v_accvgpr_write_b32 a89, v185
v_accvgpr_read_b32 v186, a90
v_accvgpr_read_b32 v187, a91
v_pk_mul_f32 v[186:187], v[186:187], v[172:173]
v_accvgpr_write_b32 a90, v186
v_accvgpr_write_b32 a91, v187
v_accvgpr_read_b32 v188, a92
v_accvgpr_read_b32 v189, a93
v_pk_mul_f32 v[188:189], v[188:189], v[172:173]
v_accvgpr_write_b32 a92, v188
v_accvgpr_write_b32 a93, v189
v_accvgpr_read_b32 v190, a94
v_accvgpr_read_b32 v191, a95
v_pk_mul_f32 v[190:191], v[190:191], v[172:173]
v_accvgpr_write_b32 a94, v190
v_accvgpr_write_b32 a95, v191
v_accvgpr_read_b32 v176, a96
v_accvgpr_read_b32 v177, a97
v_pk_mul_f32 v[176:177], v[176:177], v[170:171]
v_accvgpr_write_b32 a96, v176
v_accvgpr_write_b32 a97, v177
v_accvgpr_read_b32 v178, a98
v_accvgpr_read_b32 v179, a99
v_pk_mul_f32 v[178:179], v[178:179], v[170:171]
v_accvgpr_write_b32 a98, v178
v_accvgpr_write_b32 a99, v179
v_accvgpr_read_b32 v180, a100
v_accvgpr_read_b32 v181, a101
v_pk_mul_f32 v[180:181], v[180:181], v[170:171]
v_accvgpr_write_b32 a100, v180
v_accvgpr_write_b32 a101, v181
v_accvgpr_read_b32 v182, a102
v_accvgpr_read_b32 v183, a103
v_pk_mul_f32 v[182:183], v[182:183], v[170:171]
v_accvgpr_write_b32 a102, v182
v_accvgpr_write_b32 a103, v183
v_accvgpr_read_b32 v184, a104
v_accvgpr_read_b32 v185, a105
v_pk_mul_f32 v[184:185], v[184:185], v[170:171]
v_accvgpr_write_b32 a104, v184
v_accvgpr_write_b32 a105, v185
v_accvgpr_read_b32 v186, a106
v_accvgpr_read_b32 v187, a107
v_pk_mul_f32 v[186:187], v[186:187], v[170:171]
v_accvgpr_write_b32 a106, v186
v_accvgpr_write_b32 a107, v187
v_accvgpr_read_b32 v188, a108
v_accvgpr_read_b32 v189, a109
v_pk_mul_f32 v[188:189], v[188:189], v[170:171]
v_accvgpr_write_b32 a108, v188
v_accvgpr_write_b32 a109, v189
v_accvgpr_read_b32 v190, a110
v_accvgpr_read_b32 v191, a111
v_pk_mul_f32 v[190:191], v[190:191], v[170:171]
v_accvgpr_write_b32 a110, v190
v_accvgpr_write_b32 a111, v191
v_accvgpr_read_b32 v176, a112
v_accvgpr_read_b32 v177, a113
v_pk_mul_f32 v[176:177], v[176:177], v[172:173]
v_accvgpr_write_b32 a112, v176
v_accvgpr_write_b32 a113, v177
v_accvgpr_read_b32 v178, a114
v_accvgpr_read_b32 v179, a115
v_pk_mul_f32 v[178:179], v[178:179], v[172:173]
v_accvgpr_write_b32 a114, v178
v_accvgpr_write_b32 a115, v179
v_accvgpr_read_b32 v180, a116
v_accvgpr_read_b32 v181, a117
v_pk_mul_f32 v[180:181], v[180:181], v[172:173]
v_accvgpr_write_b32 a116, v180
v_accvgpr_write_b32 a117, v181
v_accvgpr_read_b32 v182, a118
v_accvgpr_read_b32 v183, a119
v_pk_mul_f32 v[182:183], v[182:183], v[172:173]
v_accvgpr_write_b32 a118, v182
v_accvgpr_write_b32 a119, v183
v_accvgpr_read_b32 v184, a120
v_accvgpr_read_b32 v185, a121
v_pk_mul_f32 v[184:185], v[184:185], v[172:173]
v_accvgpr_write_b32 a120, v184
v_accvgpr_write_b32 a121, v185
v_accvgpr_read_b32 v186, a122
v_accvgpr_read_b32 v187, a123
v_pk_mul_f32 v[186:187], v[186:187], v[172:173]
v_accvgpr_write_b32 a122, v186
v_accvgpr_write_b32 a123, v187
v_accvgpr_read_b32 v188, a124
v_accvgpr_read_b32 v189, a125
v_pk_mul_f32 v[188:189], v[188:189], v[172:173]
v_accvgpr_write_b32 a124, v188
v_accvgpr_write_b32 a125, v189
v_accvgpr_read_b32 v190, a126
v_accvgpr_read_b32 v191, a127
v_pk_mul_f32 v[190:191], v[190:191], v[172:173]
v_accvgpr_write_b32 a126, v190
v_accvgpr_write_b32 a127, v191
L_rs_0_0:
s_cmp_eq_u32 s75, 1
s_cbranch_scc1 L_rl_0_0
v_add_f32 v32, v32, v167
v_add_f32 v33, v33, v167
v_add_f32 v34, v34, v167
v_add_f32 v35, v35, v167
v_add_f32 v36, v36, v167
v_add_f32 v37, v37, v167
v_add_f32 v38, v38, v167
v_add_f32 v39, v39, v167
v_add_f32 v40, v40, v167
v_add_f32 v41, v41, v167
v_add_f32 v42, v42, v167
v_add_f32 v43, v43, v167
v_add_f32 v44, v44, v167
v_add_f32 v45, v45, v167
v_add_f32 v46, v46, v167
v_add_f32 v47, v47, v167
v_add_f32 v48, v48, v168
v_add_f32 v49, v49, v168
v_add_f32 v50, v50, v168
v_add_f32 v51, v51, v168
v_add_f32 v52, v52, v168
v_add_f32 v53, v53, v168
v_add_f32 v54, v54, v168
v_add_f32 v55, v55, v168
v_add_f32 v56, v56, v168
v_add_f32 v57, v57, v168
v_add_f32 v58, v58, v168
v_add_f32 v59, v59, v168
v_add_f32 v60, v60, v168
v_add_f32 v61, v61, v168
v_add_f32 v62, v62, v168
v_add_f32 v63, v63, v168
L_rl_0_0:
v_sub_f32 v0, v0, v165
v_exp_f32 v0, v0
v_sub_f32 v1, v1, v165
v_exp_f32 v1, v1
v_sub_f32 v2, v2, v165
v_exp_f32 v2, v2
v_sub_f32 v3, v3, v165
v_exp_f32 v3, v3
v_sub_f32 v4, v4, v165
v_exp_f32 v4, v4
v_sub_f32 v5, v5, v165
v_exp_f32 v5, v5
v_sub_f32 v6, v6, v165
v_exp_f32 v6, v6
v_sub_f32 v7, v7, v165
v_exp_f32 v7, v7
v_sub_f32 v8, v8, v165
v_exp_f32 v8, v8
v_sub_f32 v9, v9, v165
v_exp_f32 v9, v9
v_sub_f32 v10, v10, v165
v_exp_f32 v10, v10
v_sub_f32 v11, v11, v165
v_exp_f32 v11, v11
v_sub_f32 v12, v12, v165
v_exp_f32 v12, v12
v_sub_f32 v13, v13, v165
v_exp_f32 v13, v13
v_sub_f32 v14, v14, v165
v_exp_f32 v14, v14
v_sub_f32 v15, v15, v165
v_exp_f32 v15, v15
s_nop 0
v_add_f32 v108, v0, v1
v_add_f32 v108, v108, v2
v_add_f32 v108, v108, v3
v_add_f32 v108, v108, v4
v_add_f32 v108, v108, v5
v_add_f32 v108, v108, v6
v_add_f32 v108, v108, v7
v_add_f32 v108, v108, v8
v_add_f32 v108, v108, v9
v_add_f32 v108, v108, v10
v_add_f32 v108, v108, v11
v_add_f32 v108, v108, v12
v_add_f32 v108, v108, v13
v_add_f32 v108, v108, v14
v_add_f32 v108, v108, v15
v_cvt_pk_bf16_f32 v0, v0, v1
v_cvt_pk_bf16_f32 v1, v2, v3
v_cvt_pk_bf16_f32 v2, v4, v5
v_cvt_pk_bf16_f32 v3, v6, v7
v_cvt_pk_bf16_f32 v8, v8, v9
v_cvt_pk_bf16_f32 v9, v10, v11
v_cvt_pk_bf16_f32 v10, v12, v13
v_cvt_pk_bf16_f32 v11, v14, v15
v_sub_f32 v16, v16, v166
v_exp_f32 v16, v16
v_sub_f32 v17, v17, v166
v_exp_f32 v17, v17
v_sub_f32 v18, v18, v166
v_exp_f32 v18, v18
v_sub_f32 v19, v19, v166
v_exp_f32 v19, v19
v_sub_f32 v20, v20, v166
v_exp_f32 v20, v20
v_sub_f32 v21, v21, v166
v_exp_f32 v21, v21
v_sub_f32 v22, v22, v166
v_exp_f32 v22, v22
v_sub_f32 v23, v23, v166
v_exp_f32 v23, v23
v_sub_f32 v24, v24, v166
v_exp_f32 v24, v24
v_sub_f32 v25, v25, v166
v_exp_f32 v25, v25
v_sub_f32 v26, v26, v166
v_exp_f32 v26, v26
v_sub_f32 v27, v27, v166
v_exp_f32 v27, v27
v_sub_f32 v28, v28, v166
v_exp_f32 v28, v28
v_sub_f32 v29, v29, v166
v_exp_f32 v29, v29
v_sub_f32 v30, v30, v166
v_exp_f32 v30, v30
v_sub_f32 v31, v31, v166
v_exp_f32 v31, v31
s_nop 0
v_add_f32 v109, v16, v17
v_add_f32 v109, v109, v18
v_add_f32 v109, v109, v19
v_add_f32 v109, v109, v20
v_add_f32 v109, v109, v21
v_add_f32 v109, v109, v22
v_add_f32 v109, v109, v23
v_add_f32 v109, v109, v24
v_add_f32 v109, v109, v25
v_add_f32 v109, v109, v26
v_add_f32 v109, v109, v27
v_add_f32 v109, v109, v28
v_add_f32 v109, v109, v29
v_add_f32 v109, v109, v30
v_add_f32 v109, v109, v31
v_cvt_pk_bf16_f32 v16, v16, v17
v_cvt_pk_bf16_f32 v17, v18, v19
v_cvt_pk_bf16_f32 v18, v20, v21
v_cvt_pk_bf16_f32 v19, v22, v23
v_cvt_pk_bf16_f32 v24, v24, v25
v_cvt_pk_bf16_f32 v25, v26, v27
v_cvt_pk_bf16_f32 v26, v28, v29
v_cvt_pk_bf16_f32 v27, v30, v31
s_nop 3
s_add_u32 s72, s72, 4
s_addc_u32 s73, s73, 0
s_setpc_b64 s[72:73]
L_rare_0_1:
s_waitcnt lgkmcnt(0)
s_nop 15
v_mbcnt_lo_u32_b32 v160, -1, 0
v_mbcnt_hi_u32_b32 v160, -1, v160
v_and_b32 v161, 31, v160
v_lshrrev_b32 v162, 5, v160
v_and_b32 v163, 15, v161
v_or_b32 v164, 0, v162
v_xor_b32 v163, v163, v164
v_lshlrev_b32 v163, 4, v163
v_lshl_add_u32 v163, v161, 8, v163
v_add_u32 v163, s70, v163
ds_read_b128 v[176:179], v163 offset:0
s_waitcnt lgkmcnt(0)
v_mfma_f32_32x32x16_bf16 v[32:47], v[176:179], a[128:131], 0
v_mfma_f32_32x32x16_bf16 v[48:63], v[176:179], a[160:163], 0
v_and_b32 v163, 15, v161
v_or_b32 v164, 2, v162
v_xor_b32 v163, v163, v164
v_lshlrev_b32 v163, 4, v163
v_lshl_add_u32 v163, v161, 8, v163
v_add_u32 v163, s70, v163
ds_read_b128 v[180:183], v163 offset:0
s_waitcnt lgkmcnt(0)
v_mfma_f32_32x32x16_bf16 v[32:47], v[180:183], a[132:135], v[32:47]
v_mfma_f32_32x32x16_bf16 v[48:63], v[180:183], a[164:167], v[48:63]
v_and_b32 v163, 15, v161
v_or_b32 v164, 4, v162
v_xor_b32 v163, v163, v164
v_lshlrev_b32 v163, 4, v163
v_lshl_add_u32 v163, v161, 8, v163
v_add_u32 v163, s70, v163
ds_read_b128 v[184:187], v163 offset:0
s_waitcnt lgkmcnt(0)
v_mfma_f32_32x32x16_bf16 v[32:47], v[184:187], a[136:139], v[32:47]
v_mfma_f32_32x32x16_bf16 v[48:63], v[184:187], a[168:171], v[48:63]
v_and_b32 v163, 15, v161
v_or_b32 v164, 6, v162
v_xor_b32 v163, v163, v164
v_lshlrev_b32 v163, 4, v163
v_lshl_add_u32 v163, v161, 8, v163
v_add_u32 v163, s70, v163
ds_read_b128 v[188:191], v163 offset:0
s_waitcnt lgkmcnt(0)
v_mfma_f32_32x32x16_bf16 v[32:47], v[188:191], a[140:143], v[32:47]
v_mfma_f32_32x32x16_bf16 v[48:63], v[188:191], a[172:175], v[48:63]
v_and_b32 v163, 15, v161
v_or_b32 v164, 8, v162
v_xor_b32 v163, v163, v164
v_lshlrev_b32 v163, 4, v163
v_lshl_add_u32 v163, v161, 8, v163
v_add_u32 v163, s70, v163
ds_read_b128 v[176:179], v163 offset:0
s_waitcnt lgkmcnt(0)
v_mfma_f32_32x32x16_bf16 v[32:47], v[176:179], a[144:147], v[32:47]
v_mfma_f32_32x32x16_bf16 v[48:63], v[176:179], a[176:179], v[48:63]
v_and_b32 v163, 15, v161
v_or_b32 v164, 10, v162
v_xor_b32 v163, v163, v164
v_lshlrev_b32 v163, 4, v163
v_lshl_add_u32 v163, v161, 8, v163
v_add_u32 v163, s70, v163
ds_read_b128 v[180:183], v163 offset:0
s_waitcnt lgkmcnt(0)
v_mfma_f32_32x32x16_bf16 v[32:47], v[180:183], a[148:151], v[32:47]
v_mfma_f32_32x32x16_bf16 v[48:63], v[180:183], a[180:183], v[48:63]
v_and_b32 v163, 15, v161
v_or_b32 v164, 12, v162
v_xor_b32 v163, v163, v164
v_lshlrev_b32 v163, 4, v163
v_lshl_add_u32 v163, v161, 8, v163
v_add_u32 v163, s70, v163
ds_read_b128 v[184:187], v163 offset:0
s_waitcnt lgkmcnt(0)
v_mfma_f32_32x32x16_bf16 v[32:47], v[184:187], a[152:155], v[32:47]
v_mfma_f32_32x32x16_bf16 v[48:63], v[184:187], a[184:187], v[48:63]
v_and_b32 v163, 15, v161
v_or_b32 v164, 14, v162
v_xor_b32 v163, v163, v164
v_lshlrev_b32 v163, 4, v163
v_lshl_add_u32 v163, v161, 8, v163
v_add_u32 v163, s70, v163
ds_read_b128 v[188:191], v163 offset:0
s_waitcnt lgkmcnt(0)
v_mfma_f32_32x32x16_bf16 v[32:47], v[188:191], a[156:159], v[32:47]
v_mfma_f32_32x32x16_bf16 v[48:63], v[188:191], a[188:191], v[48:63]
s_nop 15
v_max3_f32 v165, v32, v33, v34
v_max3_f32 v165, v165, v35, v36
v_max3_f32 v165, v165, v37, v38
v_max3_f32 v165, v165, v39, v40
v_max3_f32 v165, v165, v41, v42
v_max3_f32 v165, v165, v43, v44
v_max3_f32 v165, v165, v45, v46
v_max_f32 v165, v165, v47
v_mov_b32 v163, v165
s_nop 1
v_permlane32_swap_b32 v165, v163
v_max_f32 v165, v165, v163
v_max3_f32 v166, v48, v49, v50
v_max3_f32 v166, v166, v51, v52
v_max3_f32 v166, v166, v53, v54
v_max3_f32 v166, v166, v55, v56
v_max3_f32 v166, v166, v57, v58
v_max3_f32 v166, v166, v59, v60
v_max3_f32 v166, v166, v61, v62
v_max_f32 v166, v166, v63
v_mov_b32 v163, v166
s_nop 1
v_permlane32_swap_b32 v166, v163
v_max_f32 v166, v166, v163
s_cmp_eq_u32 s71, 1
s_cbranch_scc1 L_rf_0_1
v_max_f32 v165, v165, v110
v_max_f32 v166, v166, v111
L_rf_0_1:
v_sub_f32 v167, v110, v165
v_min_f32 v163, 0, v167
v_exp_f32 v170, v163
s_nop 0
v_mov_b32 v171, v170
v_mul_f32 v106, v106, v170
v_mov_b32 v110, v165
v_sub_f32 v128, 0, v165
v_sub_f32 v129, 0, v165
v_sub_f32 v130, 0, v165
v_sub_f32 v131, 0, v165
v_sub_f32 v132, 0, v165
v_sub_f32 v133, 0, v165
v_sub_f32 v134, 0, v165
v_sub_f32 v135, 0, v165
v_sub_f32 v136, 0, v165
v_sub_f32 v137, 0, v165
v_sub_f32 v138, 0, v165
v_sub_f32 v139, 0, v165
v_sub_f32 v140, 0, v165
v_sub_f32 v141, 0, v165
v_sub_f32 v142, 0, v165
v_sub_f32 v143, 0, v165
v_sub_f32 v168, v111, v166
v_min_f32 v163, 0, v168
v_exp_f32 v172, v163
s_nop 0
v_mov_b32 v173, v172
v_mul_f32 v107, v107, v172
v_mov_b32 v111, v166
v_sub_f32 v144, 0, v166
v_sub_f32 v145, 0, v166
v_sub_f32 v146, 0, v166
v_sub_f32 v147, 0, v166
v_sub_f32 v148, 0, v166
v_sub_f32 v149, 0, v166
v_sub_f32 v150, 0, v166
v_sub_f32 v151, 0, v166
v_sub_f32 v152, 0, v166
v_sub_f32 v153, 0, v166
v_sub_f32 v154, 0, v166
v_sub_f32 v155, 0, v166
v_sub_f32 v156, 0, v166
v_sub_f32 v157, 0, v166
v_sub_f32 v158, 0, v166
v_sub_f32 v159, 0, v166
s_cmp_eq_u32 s71, 1
s_cbranch_scc1 L_rs_0_1
v_accvgpr_read_b32 v176, a0
v_accvgpr_read_b32 v177, a1
v_pk_mul_f32 v[176:177], v[176:177], v[170:171]
v_accvgpr_write_b32 a0, v176
v_accvgpr_write_b32 a1, v177
v_accvgpr_read_b32 v178, a2
v_accvgpr_read_b32 v179, a3
v_pk_mul_f32 v[178:179], v[178:179], v[170:171]
v_accvgpr_write_b32 a2, v178
v_accvgpr_write_b32 a3, v179
v_accvgpr_read_b32 v180, a4
v_accvgpr_read_b32 v181, a5
v_pk_mul_f32 v[180:181], v[180:181], v[170:171]
v_accvgpr_write_b32 a4, v180
v_accvgpr_write_b32 a5, v181
v_accvgpr_read_b32 v182, a6
v_accvgpr_read_b32 v183, a7
v_pk_mul_f32 v[182:183], v[182:183], v[170:171]
v_accvgpr_write_b32 a6, v182
v_accvgpr_write_b32 a7, v183
v_accvgpr_read_b32 v184, a8
v_accvgpr_read_b32 v185, a9
v_pk_mul_f32 v[184:185], v[184:185], v[170:171]
v_accvgpr_write_b32 a8, v184
v_accvgpr_write_b32 a9, v185
v_accvgpr_read_b32 v186, a10
v_accvgpr_read_b32 v187, a11
v_pk_mul_f32 v[186:187], v[186:187], v[170:171]
v_accvgpr_write_b32 a10, v186
v_accvgpr_write_b32 a11, v187
v_accvgpr_read_b32 v188, a12
v_accvgpr_read_b32 v189, a13
v_pk_mul_f32 v[188:189], v[188:189], v[170:171]
v_accvgpr_write_b32 a12, v188
v_accvgpr_write_b32 a13, v189
v_accvgpr_read_b32 v190, a14
v_accvgpr_read_b32 v191, a15
v_pk_mul_f32 v[190:191], v[190:191], v[170:171]
v_accvgpr_write_b32 a14, v190
v_accvgpr_write_b32 a15, v191
v_accvgpr_read_b32 v176, a16
v_accvgpr_read_b32 v177, a17
v_pk_mul_f32 v[176:177], v[176:177], v[172:173]
v_accvgpr_write_b32 a16, v176
v_accvgpr_write_b32 a17, v177
v_accvgpr_read_b32 v178, a18
v_accvgpr_read_b32 v179, a19
v_pk_mul_f32 v[178:179], v[178:179], v[172:173]
v_accvgpr_write_b32 a18, v178
v_accvgpr_write_b32 a19, v179
v_accvgpr_read_b32 v180, a20
v_accvgpr_read_b32 v181, a21
v_pk_mul_f32 v[180:181], v[180:181], v[172:173]
v_accvgpr_write_b32 a20, v180
v_accvgpr_write_b32 a21, v181
v_accvgpr_read_b32 v182, a22
v_accvgpr_read_b32 v183, a23
v_pk_mul_f32 v[182:183], v[182:183], v[172:173]
v_accvgpr_write_b32 a22, v182
v_accvgpr_write_b32 a23, v183
v_accvgpr_read_b32 v184, a24
v_accvgpr_read_b32 v185, a25
v_pk_mul_f32 v[184:185], v[184:185], v[172:173]
v_accvgpr_write_b32 a24, v184
v_accvgpr_write_b32 a25, v185
v_accvgpr_read_b32 v186, a26
v_accvgpr_read_b32 v187, a27
v_pk_mul_f32 v[186:187], v[186:187], v[172:173]
v_accvgpr_write_b32 a26, v186
v_accvgpr_write_b32 a27, v187
v_accvgpr_read_b32 v188, a28
v_accvgpr_read_b32 v189, a29
v_pk_mul_f32 v[188:189], v[188:189], v[172:173]
v_accvgpr_write_b32 a28, v188
v_accvgpr_write_b32 a29, v189
v_accvgpr_read_b32 v190, a30
v_accvgpr_read_b32 v191, a31
v_pk_mul_f32 v[190:191], v[190:191], v[172:173]
v_accvgpr_write_b32 a30, v190
v_accvgpr_write_b32 a31, v191
v_accvgpr_read_b32 v176, a32
v_accvgpr_read_b32 v177, a33
v_pk_mul_f32 v[176:177], v[176:177], v[170:171]
v_accvgpr_write_b32 a32, v176
v_accvgpr_write_b32 a33, v177
v_accvgpr_read_b32 v178, a34
v_accvgpr_read_b32 v179, a35
v_pk_mul_f32 v[178:179], v[178:179], v[170:171]
v_accvgpr_write_b32 a34, v178
v_accvgpr_write_b32 a35, v179
v_accvgpr_read_b32 v180, a36
v_accvgpr_read_b32 v181, a37
v_pk_mul_f32 v[180:181], v[180:181], v[170:171]
v_accvgpr_write_b32 a36, v180
v_accvgpr_write_b32 a37, v181
v_accvgpr_read_b32 v182, a38
v_accvgpr_read_b32 v183, a39
v_pk_mul_f32 v[182:183], v[182:183], v[170:171]
v_accvgpr_write_b32 a38, v182
v_accvgpr_write_b32 a39, v183
v_accvgpr_read_b32 v184, a40
v_accvgpr_read_b32 v185, a41
v_pk_mul_f32 v[184:185], v[184:185], v[170:171]
v_accvgpr_write_b32 a40, v184
v_accvgpr_write_b32 a41, v185
v_accvgpr_read_b32 v186, a42
v_accvgpr_read_b32 v187, a43
v_pk_mul_f32 v[186:187], v[186:187], v[170:171]
v_accvgpr_write_b32 a42, v186
v_accvgpr_write_b32 a43, v187
v_accvgpr_read_b32 v188, a44
v_accvgpr_read_b32 v189, a45
v_pk_mul_f32 v[188:189], v[188:189], v[170:171]
v_accvgpr_write_b32 a44, v188
v_accvgpr_write_b32 a45, v189
v_accvgpr_read_b32 v190, a46
v_accvgpr_read_b32 v191, a47
v_pk_mul_f32 v[190:191], v[190:191], v[170:171]
v_accvgpr_write_b32 a46, v190
v_accvgpr_write_b32 a47, v191
v_accvgpr_read_b32 v176, a48
v_accvgpr_read_b32 v177, a49
v_pk_mul_f32 v[176:177], v[176:177], v[172:173]
v_accvgpr_write_b32 a48, v176
v_accvgpr_write_b32 a49, v177
v_accvgpr_read_b32 v178, a50
v_accvgpr_read_b32 v179, a51
v_pk_mul_f32 v[178:179], v[178:179], v[172:173]
v_accvgpr_write_b32 a50, v178
v_accvgpr_write_b32 a51, v179
v_accvgpr_read_b32 v180, a52
v_accvgpr_read_b32 v181, a53
v_pk_mul_f32 v[180:181], v[180:181], v[172:173]
v_accvgpr_write_b32 a52, v180
v_accvgpr_write_b32 a53, v181
v_accvgpr_read_b32 v182, a54
v_accvgpr_read_b32 v183, a55
v_pk_mul_f32 v[182:183], v[182:183], v[172:173]
v_accvgpr_write_b32 a54, v182
v_accvgpr_write_b32 a55, v183
v_accvgpr_read_b32 v184, a56
v_accvgpr_read_b32 v185, a57
v_pk_mul_f32 v[184:185], v[184:185], v[172:173]
v_accvgpr_write_b32 a56, v184
v_accvgpr_write_b32 a57, v185
v_accvgpr_read_b32 v186, a58
v_accvgpr_read_b32 v187, a59
v_pk_mul_f32 v[186:187], v[186:187], v[172:173]
v_accvgpr_write_b32 a58, v186
v_accvgpr_write_b32 a59, v187
v_accvgpr_read_b32 v188, a60
v_accvgpr_read_b32 v189, a61
v_pk_mul_f32 v[188:189], v[188:189], v[172:173]
v_accvgpr_write_b32 a60, v188
v_accvgpr_write_b32 a61, v189
v_accvgpr_read_b32 v190, a62
v_accvgpr_read_b32 v191, a63
v_pk_mul_f32 v[190:191], v[190:191], v[172:173]
v_accvgpr_write_b32 a62, v190
v_accvgpr_write_b32 a63, v191
v_accvgpr_read_b32 v176, a64
v_accvgpr_read_b32 v177, a65
v_pk_mul_f32 v[176:177], v[176:177], v[170:171]
v_accvgpr_write_b32 a64, v176
v_accvgpr_write_b32 a65, v177
v_accvgpr_read_b32 v178, a66
v_accvgpr_read_b32 v179, a67
v_pk_mul_f32 v[178:179], v[178:179], v[170:171]
v_accvgpr_write_b32 a66, v178
v_accvgpr_write_b32 a67, v179
v_accvgpr_read_b32 v180, a68
v_accvgpr_read_b32 v181, a69
v_pk_mul_f32 v[180:181], v[180:181], v[170:171]
v_accvgpr_write_b32 a68, v180
v_accvgpr_write_b32 a69, v181
v_accvgpr_read_b32 v182, a70
v_accvgpr_read_b32 v183, a71
v_pk_mul_f32 v[182:183], v[182:183], v[170:171]
v_accvgpr_write_b32 a70, v182
v_accvgpr_write_b32 a71, v183
v_accvgpr_read_b32 v184, a72
v_accvgpr_read_b32 v185, a73
v_pk_mul_f32 v[184:185], v[184:185], v[170:171]
v_accvgpr_write_b32 a72, v184
v_accvgpr_write_b32 a73, v185
v_accvgpr_read_b32 v186, a74
v_accvgpr_read_b32 v187, a75
v_pk_mul_f32 v[186:187], v[186:187], v[170:171]
v_accvgpr_write_b32 a74, v186
v_accvgpr_write_b32 a75, v187
v_accvgpr_read_b32 v188, a76
v_accvgpr_read_b32 v189, a77
v_pk_mul_f32 v[188:189], v[188:189], v[170:171]
v_accvgpr_write_b32 a76, v188
v_accvgpr_write_b32 a77, v189
v_accvgpr_read_b32 v190, a78
v_accvgpr_read_b32 v191, a79
v_pk_mul_f32 v[190:191], v[190:191], v[170:171]
v_accvgpr_write_b32 a78, v190
v_accvgpr_write_b32 a79, v191
v_accvgpr_read_b32 v176, a80
v_accvgpr_read_b32 v177, a81
v_pk_mul_f32 v[176:177], v[176:177], v[172:173]
v_accvgpr_write_b32 a80, v176
v_accvgpr_write_b32 a81, v177
v_accvgpr_read_b32 v178, a82
v_accvgpr_read_b32 v179, a83
v_pk_mul_f32 v[178:179], v[178:179], v[172:173]
v_accvgpr_write_b32 a82, v178
v_accvgpr_write_b32 a83, v179
v_accvgpr_read_b32 v180, a84
v_accvgpr_read_b32 v181, a85
v_pk_mul_f32 v[180:181], v[180:181], v[172:173]
v_accvgpr_write_b32 a84, v180
v_accvgpr_write_b32 a85, v181
v_accvgpr_read_b32 v182, a86
v_accvgpr_read_b32 v183, a87
v_pk_mul_f32 v[182:183], v[182:183], v[172:173]
v_accvgpr_write_b32 a86, v182
v_accvgpr_write_b32 a87, v183
v_accvgpr_read_b32 v184, a88
v_accvgpr_read_b32 v185, a89
v_pk_mul_f32 v[184:185], v[184:185], v[172:173]
v_accvgpr_write_b32 a88, v184
v_accvgpr_write_b32 a89, v185
v_accvgpr_read_b32 v186, a90
v_accvgpr_read_b32 v187, a91
v_pk_mul_f32 v[186:187], v[186:187], v[172:173]
v_accvgpr_write_b32 a90, v186
v_accvgpr_write_b32 a91, v187
v_accvgpr_read_b32 v188, a92
v_accvgpr_read_b32 v189, a93
v_pk_mul_f32 v[188:189], v[188:189], v[172:173]
v_accvgpr_write_b32 a92, v188
v_accvgpr_write_b32 a93, v189
v_accvgpr_read_b32 v190, a94
v_accvgpr_read_b32 v191, a95
v_pk_mul_f32 v[190:191], v[190:191], v[172:173]
v_accvgpr_write_b32 a94, v190
v_accvgpr_write_b32 a95, v191
v_accvgpr_read_b32 v176, a96
v_accvgpr_read_b32 v177, a97
v_pk_mul_f32 v[176:177], v[176:177], v[170:171]
v_accvgpr_write_b32 a96, v176
v_accvgpr_write_b32 a97, v177
v_accvgpr_read_b32 v178, a98
v_accvgpr_read_b32 v179, a99
v_pk_mul_f32 v[178:179], v[178:179], v[170:171]
v_accvgpr_write_b32 a98, v178
v_accvgpr_write_b32 a99, v179
v_accvgpr_read_b32 v180, a100
v_accvgpr_read_b32 v181, a101
v_pk_mul_f32 v[180:181], v[180:181], v[170:171]
v_accvgpr_write_b32 a100, v180
v_accvgpr_write_b32 a101, v181
v_accvgpr_read_b32 v182, a102
v_accvgpr_read_b32 v183, a103
v_pk_mul_f32 v[182:183], v[182:183], v[170:171]
v_accvgpr_write_b32 a102, v182
v_accvgpr_write_b32 a103, v183
v_accvgpr_read_b32 v184, a104
v_accvgpr_read_b32 v185, a105
v_pk_mul_f32 v[184:185], v[184:185], v[170:171]
v_accvgpr_write_b32 a104, v184
v_accvgpr_write_b32 a105, v185
v_accvgpr_read_b32 v186, a106
v_accvgpr_read_b32 v187, a107
v_pk_mul_f32 v[186:187], v[186:187], v[170:171]
v_accvgpr_write_b32 a106, v186
v_accvgpr_write_b32 a107, v187
v_accvgpr_read_b32 v188, a108
v_accvgpr_read_b32 v189, a109
v_pk_mul_f32 v[188:189], v[188:189], v[170:171]
v_accvgpr_write_b32 a108, v188
v_accvgpr_write_b32 a109, v189
v_accvgpr_read_b32 v190, a110
v_accvgpr_read_b32 v191, a111
v_pk_mul_f32 v[190:191], v[190:191], v[170:171]
v_accvgpr_write_b32 a110, v190
v_accvgpr_write_b32 a111, v191
v_accvgpr_read_b32 v176, a112
v_accvgpr_read_b32 v177, a113
v_pk_mul_f32 v[176:177], v[176:177], v[172:173]
v_accvgpr_write_b32 a112, v176
v_accvgpr_write_b32 a113, v177
v_accvgpr_read_b32 v178, a114
v_accvgpr_read_b32 v179, a115
v_pk_mul_f32 v[178:179], v[178:179], v[172:173]
v_accvgpr_write_b32 a114, v178
v_accvgpr_write_b32 a115, v179
v_accvgpr_read_b32 v180, a116
v_accvgpr_read_b32 v181, a117
v_pk_mul_f32 v[180:181], v[180:181], v[172:173]
v_accvgpr_write_b32 a116, v180
v_accvgpr_write_b32 a117, v181
v_accvgpr_read_b32 v182, a118
v_accvgpr_read_b32 v183, a119
v_pk_mul_f32 v[182:183], v[182:183], v[172:173]
v_accvgpr_write_b32 a118, v182
v_accvgpr_write_b32 a119, v183
v_accvgpr_read_b32 v184, a120
v_accvgpr_read_b32 v185, a121
v_pk_mul_f32 v[184:185], v[184:185], v[172:173]
v_accvgpr_write_b32 a120, v184
v_accvgpr_write_b32 a121, v185
v_accvgpr_read_b32 v186, a122
v_accvgpr_read_b32 v187, a123
v_pk_mul_f32 v[186:187], v[186:187], v[172:173]
v_accvgpr_write_b32 a122, v186
v_accvgpr_write_b32 a123, v187
v_accvgpr_read_b32 v188, a124
v_accvgpr_read_b32 v189, a125
v_pk_mul_f32 v[188:189], v[188:189], v[172:173]
v_accvgpr_write_b32 a124, v188
v_accvgpr_write_b32 a125, v189
v_accvgpr_read_b32 v190, a126
v_accvgpr_read_b32 v191, a127
v_pk_mul_f32 v[190:191], v[190:191], v[172:173]
v_accvgpr_write_b32 a126, v190
v_accvgpr_write_b32 a127, v191
L_rs_0_1:
s_cmp_eq_u32 s75, 1
s_cbranch_scc1 L_rl_0_1
v_add_f32 v0, v0, v167
v_add_f32 v1, v1, v167
v_add_f32 v2, v2, v167
v_add_f32 v3, v3, v167
v_add_f32 v4, v4, v167
v_add_f32 v5, v5, v167
v_add_f32 v6, v6, v167
v_add_f32 v7, v7, v167
v_add_f32 v8, v8, v167
v_add_f32 v9, v9, v167
v_add_f32 v10, v10, v167
v_add_f32 v11, v11, v167
v_add_f32 v12, v12, v167
v_add_f32 v13, v13, v167
v_add_f32 v14, v14, v167
v_add_f32 v15, v15, v167
v_add_f32 v16, v16, v168
v_add_f32 v17, v17, v168
v_add_f32 v18, v18, v168
v_add_f32 v19, v19, v168
v_add_f32 v20, v20, v168
v_add_f32 v21, v21, v168
v_add_f32 v22, v22, v168
v_add_f32 v23, v23, v168
v_add_f32 v24, v24, v168
v_add_f32 v25, v25, v168
v_add_f32 v26, v26, v168
v_add_f32 v27, v27, v168
v_add_f32 v28, v28, v168
v_add_f32 v29, v29, v168
v_add_f32 v30, v30, v168
v_add_f32 v31, v31, v168
L_rl_0_1:
v_sub_f32 v32, v32, v165
v_exp_f32 v32, v32
v_sub_f32 v33, v33, v165
v_exp_f32 v33, v33
v_sub_f32 v34, v34, v165
v_exp_f32 v34, v34
v_sub_f32 v35, v35, v165
v_exp_f32 v35, v35
v_sub_f32 v36, v36, v165
v_exp_f32 v36, v36
v_sub_f32 v37, v37, v165
v_exp_f32 v37, v37
v_sub_f32 v38, v38, v165
v_exp_f32 v38, v38
v_sub_f32 v39, v39, v165
v_exp_f32 v39, v39
v_sub_f32 v40, v40, v165
v_exp_f32 v40, v40
v_sub_f32 v41, v41, v165
v_exp_f32 v41, v41
v_sub_f32 v42, v42, v165
v_exp_f32 v42, v42
v_sub_f32 v43, v43, v165
v_exp_f32 v43, v43
v_sub_f32 v44, v44, v165
v_exp_f32 v44, v44
v_sub_f32 v45, v45, v165
v_exp_f32 v45, v45
v_sub_f32 v46, v46, v165
v_exp_f32 v46, v46
v_sub_f32 v47, v47, v165
v_exp_f32 v47, v47
s_nop 0
v_add_f32 v108, v32, v33
v_add_f32 v108, v108, v34
v_add_f32 v108, v108, v35
v_add_f32 v108, v108, v36
v_add_f32 v108, v108, v37
v_add_f32 v108, v108, v38
v_add_f32 v108, v108, v39
v_add_f32 v108, v108, v40
v_add_f32 v108, v108, v41
v_add_f32 v108, v108, v42
v_add_f32 v108, v108, v43
v_add_f32 v108, v108, v44
v_add_f32 v108, v108, v45
v_add_f32 v108, v108, v46
v_add_f32 v108, v108, v47
v_cvt_pk_bf16_f32 v32, v32, v33
v_cvt_pk_bf16_f32 v33, v34, v35
v_cvt_pk_bf16_f32 v34, v36, v37
v_cvt_pk_bf16_f32 v35, v38, v39
v_cvt_pk_bf16_f32 v40, v40, v41
v_cvt_pk_bf16_f32 v41, v42, v43
v_cvt_pk_bf16_f32 v42, v44, v45
v_cvt_pk_bf16_f32 v43, v46, v47
v_sub_f32 v48, v48, v166
v_exp_f32 v48, v48
v_sub_f32 v49, v49, v166
v_exp_f32 v49, v49
v_sub_f32 v50, v50, v166
v_exp_f32 v50, v50
v_sub_f32 v51, v51, v166
v_exp_f32 v51, v51
v_sub_f32 v52, v52, v166
v_exp_f32 v52, v52
v_sub_f32 v53, v53, v166
v_exp_f32 v53, v53
v_sub_f32 v54, v54, v166
v_exp_f32 v54, v54
v_sub_f32 v55, v55, v166
v_exp_f32 v55, v55
v_sub_f32 v56, v56, v166
v_exp_f32 v56, v56
v_sub_f32 v57, v57, v166
v_exp_f32 v57, v57
v_sub_f32 v58, v58, v166
v_exp_f32 v58, v58
v_sub_f32 v59, v59, v166
v_exp_f32 v59, v59
v_sub_f32 v60, v60, v166
v_exp_f32 v60, v60
v_sub_f32 v61, v61, v166
v_exp_f32 v61, v61
v_sub_f32 v62, v62, v166
v_exp_f32 v62, v62
v_sub_f32 v63, v63, v166
v_exp_f32 v63, v63
s_nop 0
v_add_f32 v109, v48, v49
v_add_f32 v109, v109, v50
v_add_f32 v109, v109, v51
v_add_f32 v109, v109, v52
v_add_f32 v109, v109, v53
v_add_f32 v109, v109, v54
v_add_f32 v109, v109, v55
v_add_f32 v109, v109, v56
v_add_f32 v109, v109, v57
v_add_f32 v109, v109, v58
v_add_f32 v109, v109, v59
v_add_f32 v109, v109, v60
v_add_f32 v109, v109, v61
v_add_f32 v109, v109, v62
v_add_f32 v109, v109, v63
v_cvt_pk_bf16_f32 v48, v48, v49
v_cvt_pk_bf16_f32 v49, v50, v51
v_cvt_pk_bf16_f32 v50, v52, v53
v_cvt_pk_bf16_f32 v51, v54, v55
v_cvt_pk_bf16_f32 v56, v56, v57
v_cvt_pk_bf16_f32 v57, v58, v59
v_cvt_pk_bf16_f32 v58, v60, v61
v_cvt_pk_bf16_f32 v59, v62, v63
s_nop 3
s_add_u32 s72, s72, 4
s_addc_u32 s73, s73, 0
s_setpc_b64 s[72:73]
L_rare_1_0:
s_waitcnt lgkmcnt(0)
s_nop 15
v_mbcnt_lo_u32_b32 v160, -1, 0
v_mbcnt_hi_u32_b32 v160, -1, v160
v_and_b32 v161, 31, v160
v_lshrrev_b32 v162, 5, v160
v_and_b32 v163, 15, v161
v_or_b32 v164, 0, v162
v_xor_b32 v163, v163, v164
v_lshlrev_b32 v163, 4, v163
v_lshl_add_u32 v163, v161, 8, v163
v_add_u32 v163, s70, v163
ds_read_b128 v[176:179], v163 offset:0
s_waitcnt lgkmcnt(0)
v_mfma_f32_32x32x16_bf16 v[0:15], v[176:179], v[192:195], 0
v_mfma_f32_32x32x16_bf16 v[16:31], v[176:179], v[208:211], 0
v_and_b32 v163, 15, v161
v_or_b32 v164, 2, v162
v_xor_b32 v163, v163, v164
v_lshlrev_b32 v163, 4, v163
v_lshl_add_u32 v163, v161, 8, v163
v_add_u32 v163, s70, v163
ds_read_b128 v[180:183], v163 offset:0
s_waitcnt lgkmcnt(0)
v_mfma_f32_32x32x16_bf16 v[0:15], v[180:183], v[196:199], v[0:15]
v_mfma_f32_32x32x16_bf16 v[16:31], v[180:183], v[212:215], v[16:31]
v_and_b32 v163, 15, v161
v_or_b32 v164, 4, v162
v_xor_b32 v163, v163, v164
v_lshlrev_b32 v163, 4, v163
v_lshl_add_u32 v163, v161, 8, v163
v_add_u32 v163, s70, v163
ds_read_b128 v[184:187], v163 offset:0
s_waitcnt lgkmcnt(0)
v_mfma_f32_32x32x16_bf16 v[0:15], v[184:187], v[224:227], v[0:15]
v_mfma_f32_32x32x16_bf16 v[16:31], v[184:187], v[240:243], v[16:31]
v_and_b32 v163, 15, v161
v_or_b32 v164, 6, v162
v_xor_b32 v163, v163, v164
v_lshlrev_b32 v163, 4, v163
v_lshl_add_u32 v163, v161, 8, v163
v_add_u32 v163, s70, v163
ds_read_b128 v[188:191], v163 offset:0
s_waitcnt lgkmcnt(0)
v_mfma_f32_32x32x16_bf16 v[0:15], v[188:191], v[228:231], v[0:15]
v_mfma_f32_32x32x16_bf16 v[16:31], v[188:191], v[244:247], v[16:31]
v_and_b32 v163, 15, v161
v_or_b32 v164, 8, v162
v_xor_b32 v163, v163, v164
v_lshlrev_b32 v163, 4, v163
v_lshl_add_u32 v163, v161, 8, v163
v_add_u32 v163, s70, v163
ds_read_b128 v[176:179], v163 offset:0
s_waitcnt lgkmcnt(0)
v_mfma_f32_32x32x16_bf16 v[0:15], v[176:179], v[200:203], v[0:15]
v_mfma_f32_32x32x16_bf16 v[16:31], v[176:179], v[216:219], v[16:31]
v_and_b32 v163, 15, v161
v_or_b32 v164, 10, v162
v_xor_b32 v163, v163, v164
v_lshlrev_b32 v163, 4, v163
v_lshl_add_u32 v163, v161, 8, v163
v_add_u32 v163, s70, v163
ds_read_b128 v[180:183], v163 offset:0
s_waitcnt lgkmcnt(0)
v_mfma_f32_32x32x16_bf16 v[0:15], v[180:183], v[204:207], v[0:15]
v_mfma_f32_32x32x16_bf16 v[16:31], v[180:183], v[220:223], v[16:31]
v_and_b32 v163, 15, v161
v_or_b32 v164, 12, v162
v_xor_b32 v163, v163, v164
v_lshlrev_b32 v163, 4, v163
v_lshl_add_u32 v163, v161, 8, v163
v_add_u32 v163, s70, v163
ds_read_b128 v[184:187], v163 offset:0
s_waitcnt lgkmcnt(0)
v_mfma_f32_32x32x16_bf16 v[0:15], v[184:187], v[232:235], v[0:15]
v_mfma_f32_32x32x16_bf16 v[16:31], v[184:187], v[248:251], v[16:31]
v_and_b32 v163, 15, v161
v_or_b32 v164, 14, v162
v_xor_b32 v163, v163, v164
v_lshlrev_b32 v163, 4, v163
v_lshl_add_u32 v163, v161, 8, v163
v_add_u32 v163, s70, v163
ds_read_b128 v[188:191], v163 offset:0
s_waitcnt lgkmcnt(0)
v_mfma_f32_32x32x16_bf16 v[0:15], v[188:191], v[236:239], v[0:15]
v_mfma_f32_32x32x16_bf16 v[16:31], v[188:191], v[252:255], v[16:31]
s_nop 15
v_max3_f32 v165, v0, v1, v2
v_max3_f32 v165, v165, v3, v4
v_max3_f32 v165, v165, v5, v6
v_max3_f32 v165, v165, v7, v8
v_max3_f32 v165, v165, v9, v10
v_max3_f32 v165, v165, v11, v12
v_max3_f32 v165, v165, v13, v14
v_max_f32 v165, v165, v15
v_mov_b32 v163, v165
s_nop 1
v_permlane32_swap_b32 v165, v163
v_max_f32 v165, v165, v163
v_max3_f32 v166, v16, v17, v18
v_max3_f32 v166, v166, v19, v20
v_max3_f32 v166, v166, v21, v22
v_max3_f32 v166, v166, v23, v24
v_max3_f32 v166, v166, v25, v26
v_max3_f32 v166, v166, v27, v28
v_max3_f32 v166, v166, v29, v30
v_max_f32 v166, v166, v31
v_mov_b32 v163, v166
s_nop 1
v_permlane32_swap_b32 v166, v163
v_max_f32 v166, v166, v163
s_cmp_eq_u32 s71, 1
s_cbranch_scc1 L_rf_1_0
v_max_f32 v165, v165, v110
v_max_f32 v166, v166, v111
L_rf_1_0:
v_sub_f32 v167, v110, v165
v_min_f32 v163, 0, v167
v_exp_f32 v170, v163
s_nop 0
v_mov_b32 v171, v170
v_mul_f32 v112, v112, v170
v_mov_b32 v110, v165
v_sub_f32 v128, 0, v165
v_sub_f32 v129, 0, v165
v_sub_f32 v130, 0, v165
v_sub_f32 v131, 0, v165
v_sub_f32 v132, 0, v165
v_sub_f32 v133, 0, v165
v_sub_f32 v134, 0, v165
v_sub_f32 v135, 0, v165
v_sub_f32 v136, 0, v165
v_sub_f32 v137, 0, v165
v_sub_f32 v138, 0, v165
v_sub_f32 v139, 0, v165
v_sub_f32 v140, 0, v165
v_sub_f32 v141, 0, v165
v_sub_f32 v142, 0, v165
v_sub_f32 v143, 0, v165
v_sub_f32 v168, v111, v166
v_min_f32 v163, 0, v168
v_exp_f32 v172, v163
s_nop 0
v_mov_b32 v173, v172
v_mul_f32 v113, v113, v172
v_mov_b32 v111, v166
v_sub_f32 v144, 0, v166
v_sub_f32 v145, 0, v166
v_sub_f32 v146, 0, v166
v_sub_f32 v147, 0, v166
v_sub_f32 v148, 0, v166
v_sub_f32 v149, 0, v166
v_sub_f32 v150, 0, v166
v_sub_f32 v151, 0, v166
v_sub_f32 v152, 0, v166
v_sub_f32 v153, 0, v166
v_sub_f32 v154, 0, v166
v_sub_f32 v155, 0, v166
v_sub_f32 v156, 0, v166
v_sub_f32 v157, 0, v166
v_sub_f32 v158, 0, v166
v_sub_f32 v159, 0, v166
s_cmp_eq_u32 s71, 1
s_cbranch_scc1 L_rs_1_0
v_accvgpr_read_b32 v176, a128
v_accvgpr_read_b32 v177, a129
v_pk_mul_f32 v[176:177], v[176:177], v[170:171]
v_accvgpr_write_b32 a128, v176
v_accvgpr_write_b32 a129, v177
v_accvgpr_read_b32 v178, a130
v_accvgpr_read_b32 v179, a131
v_pk_mul_f32 v[178:179], v[178:179], v[170:171]
v_accvgpr_write_b32 a130, v178
v_accvgpr_write_b32 a131, v179
v_accvgpr_read_b32 v180, a132
v_accvgpr_read_b32 v181, a133
v_pk_mul_f32 v[180:181], v[180:181], v[170:171]
v_accvgpr_write_b32 a132, v180
v_accvgpr_write_b32 a133, v181
v_accvgpr_read_b32 v182, a134
v_accvgpr_read_b32 v183, a135
v_pk_mul_f32 v[182:183], v[182:183], v[170:171]
v_accvgpr_write_b32 a134, v182
v_accvgpr_write_b32 a135, v183
v_accvgpr_read_b32 v184, a136
v_accvgpr_read_b32 v185, a137
v_pk_mul_f32 v[184:185], v[184:185], v[170:171]
v_accvgpr_write_b32 a136, v184
v_accvgpr_write_b32 a137, v185
v_accvgpr_read_b32 v186, a138
v_accvgpr_read_b32 v187, a139
v_pk_mul_f32 v[186:187], v[186:187], v[170:171]
v_accvgpr_write_b32 a138, v186
v_accvgpr_write_b32 a139, v187
v_accvgpr_read_b32 v188, a140
v_accvgpr_read_b32 v189, a141
v_pk_mul_f32 v[188:189], v[188:189], v[170:171]
v_accvgpr_write_b32 a140, v188
v_accvgpr_write_b32 a141, v189
v_accvgpr_read_b32 v190, a142
v_accvgpr_read_b32 v191, a143
v_pk_mul_f32 v[190:191], v[190:191], v[170:171]
v_accvgpr_write_b32 a142, v190
v_accvgpr_write_b32 a143, v191
v_accvgpr_read_b32 v176, a144
v_accvgpr_read_b32 v177, a145
v_pk_mul_f32 v[176:177], v[176:177], v[172:173]
v_accvgpr_write_b32 a144, v176
v_accvgpr_write_b32 a145, v177
v_accvgpr_read_b32 v178, a146
v_accvgpr_read_b32 v179, a147
v_pk_mul_f32 v[178:179], v[178:179], v[172:173]
v_accvgpr_write_b32 a146, v178
v_accvgpr_write_b32 a147, v179
v_accvgpr_read_b32 v180, a148
v_accvgpr_read_b32 v181, a149
v_pk_mul_f32 v[180:181], v[180:181], v[172:173]
v_accvgpr_write_b32 a148, v180
v_accvgpr_write_b32 a149, v181
v_accvgpr_read_b32 v182, a150
v_accvgpr_read_b32 v183, a151
v_pk_mul_f32 v[182:183], v[182:183], v[172:173]
v_accvgpr_write_b32 a150, v182
v_accvgpr_write_b32 a151, v183
v_accvgpr_read_b32 v184, a152
v_accvgpr_read_b32 v185, a153
v_pk_mul_f32 v[184:185], v[184:185], v[172:173]
v_accvgpr_write_b32 a152, v184
v_accvgpr_write_b32 a153, v185
v_accvgpr_read_b32 v186, a154
v_accvgpr_read_b32 v187, a155
v_pk_mul_f32 v[186:187], v[186:187], v[172:173]
v_accvgpr_write_b32 a154, v186
v_accvgpr_write_b32 a155, v187
v_accvgpr_read_b32 v188, a156
v_accvgpr_read_b32 v189, a157
v_pk_mul_f32 v[188:189], v[188:189], v[172:173]
v_accvgpr_write_b32 a156, v188
v_accvgpr_write_b32 a157, v189
v_accvgpr_read_b32 v190, a158
v_accvgpr_read_b32 v191, a159
v_pk_mul_f32 v[190:191], v[190:191], v[172:173]
v_accvgpr_write_b32 a158, v190
v_accvgpr_write_b32 a159, v191
v_accvgpr_read_b32 v176, a160
v_accvgpr_read_b32 v177, a161
v_pk_mul_f32 v[176:177], v[176:177], v[170:171]
v_accvgpr_write_b32 a160, v176
v_accvgpr_write_b32 a161, v177
v_accvgpr_read_b32 v178, a162
v_accvgpr_read_b32 v179, a163
v_pk_mul_f32 v[178:179], v[178:179], v[170:171]
v_accvgpr_write_b32 a162, v178
v_accvgpr_write_b32 a163, v179
v_accvgpr_read_b32 v180, a164
v_accvgpr_read_b32 v181, a165
v_pk_mul_f32 v[180:181], v[180:181], v[170:171]
v_accvgpr_write_b32 a164, v180
v_accvgpr_write_b32 a165, v181
v_accvgpr_read_b32 v182, a166
v_accvgpr_read_b32 v183, a167
v_pk_mul_f32 v[182:183], v[182:183], v[170:171]
v_accvgpr_write_b32 a166, v182
v_accvgpr_write_b32 a167, v183
v_accvgpr_read_b32 v184, a168
v_accvgpr_read_b32 v185, a169
v_pk_mul_f32 v[184:185], v[184:185], v[170:171]
v_accvgpr_write_b32 a168, v184
v_accvgpr_write_b32 a169, v185
v_accvgpr_read_b32 v186, a170
v_accvgpr_read_b32 v187, a171
v_pk_mul_f32 v[186:187], v[186:187], v[170:171]
v_accvgpr_write_b32 a170, v186
v_accvgpr_write_b32 a171, v187
v_accvgpr_read_b32 v188, a172
v_accvgpr_read_b32 v189, a173
v_pk_mul_f32 v[188:189], v[188:189], v[170:171]
v_accvgpr_write_b32 a172, v188
v_accvgpr_write_b32 a173, v189
v_accvgpr_read_b32 v190, a174
v_accvgpr_read_b32 v191, a175
v_pk_mul_f32 v[190:191], v[190:191], v[170:171]
v_accvgpr_write_b32 a174, v190
v_accvgpr_write_b32 a175, v191
v_accvgpr_read_b32 v176, a176
v_accvgpr_read_b32 v177, a177
v_pk_mul_f32 v[176:177], v[176:177], v[172:173]
v_accvgpr_write_b32 a176, v176
v_accvgpr_write_b32 a177, v177
v_accvgpr_read_b32 v178, a178
v_accvgpr_read_b32 v179, a179
v_pk_mul_f32 v[178:179], v[178:179], v[172:173]
v_accvgpr_write_b32 a178, v178
v_accvgpr_write_b32 a179, v179
v_accvgpr_read_b32 v180, a180
v_accvgpr_read_b32 v181, a181
v_pk_mul_f32 v[180:181], v[180:181], v[172:173]
v_accvgpr_write_b32 a180, v180
v_accvgpr_write_b32 a181, v181
v_accvgpr_read_b32 v182, a182
v_accvgpr_read_b32 v183, a183
v_pk_mul_f32 v[182:183], v[182:183], v[172:173]
v_accvgpr_write_b32 a182, v182
v_accvgpr_write_b32 a183, v183
v_accvgpr_read_b32 v184, a184
v_accvgpr_read_b32 v185, a185
v_pk_mul_f32 v[184:185], v[184:185], v[172:173]
v_accvgpr_write_b32 a184, v184
v_accvgpr_write_b32 a185, v185
v_accvgpr_read_b32 v186, a186
v_accvgpr_read_b32 v187, a187
v_pk_mul_f32 v[186:187], v[186:187], v[172:173]
v_accvgpr_write_b32 a186, v186
v_accvgpr_write_b32 a187, v187
v_accvgpr_read_b32 v188, a188
v_accvgpr_read_b32 v189, a189
v_pk_mul_f32 v[188:189], v[188:189], v[172:173]
v_accvgpr_write_b32 a188, v188
v_accvgpr_write_b32 a189, v189
v_accvgpr_read_b32 v190, a190
v_accvgpr_read_b32 v191, a191
v_pk_mul_f32 v[190:191], v[190:191], v[172:173]
v_accvgpr_write_b32 a190, v190
v_accvgpr_write_b32 a191, v191
v_accvgpr_read_b32 v176, a192
v_accvgpr_read_b32 v177, a193
v_pk_mul_f32 v[176:177], v[176:177], v[170:171]
v_accvgpr_write_b32 a192, v176
v_accvgpr_write_b32 a193, v177
v_accvgpr_read_b32 v178, a194
v_accvgpr_read_b32 v179, a195
v_pk_mul_f32 v[178:179], v[178:179], v[170:171]
v_accvgpr_write_b32 a194, v178
v_accvgpr_write_b32 a195, v179
v_accvgpr_read_b32 v180, a196
v_accvgpr_read_b32 v181, a197
v_pk_mul_f32 v[180:181], v[180:181], v[170:171]
v_accvgpr_write_b32 a196, v180
v_accvgpr_write_b32 a197, v181
v_accvgpr_read_b32 v182, a198
v_accvgpr_read_b32 v183, a199
v_pk_mul_f32 v[182:183], v[182:183], v[170:171]
v_accvgpr_write_b32 a198, v182
v_accvgpr_write_b32 a199, v183
v_accvgpr_read_b32 v184, a200
v_accvgpr_read_b32 v185, a201
v_pk_mul_f32 v[184:185], v[184:185], v[170:171]
v_accvgpr_write_b32 a200, v184
v_accvgpr_write_b32 a201, v185
v_accvgpr_read_b32 v186, a202
v_accvgpr_read_b32 v187, a203
v_pk_mul_f32 v[186:187], v[186:187], v[170:171]
v_accvgpr_write_b32 a202, v186
v_accvgpr_write_b32 a203, v187
v_accvgpr_read_b32 v188, a204
v_accvgpr_read_b32 v189, a205
v_pk_mul_f32 v[188:189], v[188:189], v[170:171]
v_accvgpr_write_b32 a204, v188
v_accvgpr_write_b32 a205, v189
v_accvgpr_read_b32 v190, a206
v_accvgpr_read_b32 v191, a207
v_pk_mul_f32 v[190:191], v[190:191], v[170:171]
v_accvgpr_write_b32 a206, v190
v_accvgpr_write_b32 a207, v191
v_accvgpr_read_b32 v176, a208
v_accvgpr_read_b32 v177, a209
v_pk_mul_f32 v[176:177], v[176:177], v[172:173]
v_accvgpr_write_b32 a208, v176
v_accvgpr_write_b32 a209, v177
v_accvgpr_read_b32 v178, a210
v_accvgpr_read_b32 v179, a211
v_pk_mul_f32 v[178:179], v[178:179], v[172:173]
v_accvgpr_write_b32 a210, v178
v_accvgpr_write_b32 a211, v179
v_accvgpr_read_b32 v180, a212
v_accvgpr_read_b32 v181, a213
v_pk_mul_f32 v[180:181], v[180:181], v[172:173]
v_accvgpr_write_b32 a212, v180
v_accvgpr_write_b32 a213, v181
v_accvgpr_read_b32 v182, a214
v_accvgpr_read_b32 v183, a215
v_pk_mul_f32 v[182:183], v[182:183], v[172:173]
v_accvgpr_write_b32 a214, v182
v_accvgpr_write_b32 a215, v183
v_accvgpr_read_b32 v184, a216
v_accvgpr_read_b32 v185, a217
v_pk_mul_f32 v[184:185], v[184:185], v[172:173]
v_accvgpr_write_b32 a216, v184
v_accvgpr_write_b32 a217, v185
v_accvgpr_read_b32 v186, a218
v_accvgpr_read_b32 v187, a219
v_pk_mul_f32 v[186:187], v[186:187], v[172:173]
v_accvgpr_write_b32 a218, v186
v_accvgpr_write_b32 a219, v187
v_accvgpr_read_b32 v188, a220
v_accvgpr_read_b32 v189, a221
v_pk_mul_f32 v[188:189], v[188:189], v[172:173]
v_accvgpr_write_b32 a220, v188
v_accvgpr_write_b32 a221, v189
v_accvgpr_read_b32 v190, a222
v_accvgpr_read_b32 v191, a223
v_pk_mul_f32 v[190:191], v[190:191], v[172:173]
v_accvgpr_write_b32 a222, v190
v_accvgpr_write_b32 a223, v191
v_accvgpr_read_b32 v176, a224
v_accvgpr_read_b32 v177, a225
v_pk_mul_f32 v[176:177], v[176:177], v[170:171]
v_accvgpr_write_b32 a224, v176
v_accvgpr_write_b32 a225, v177
v_accvgpr_read_b32 v178, a226
v_accvgpr_read_b32 v179, a227
v_pk_mul_f32 v[178:179], v[178:179], v[170:171]
v_accvgpr_write_b32 a226, v178
v_accvgpr_write_b32 a227, v179
v_accvgpr_read_b32 v180, a228
v_accvgpr_read_b32 v181, a229
v_pk_mul_f32 v[180:181], v[180:181], v[170:171]
v_accvgpr_write_b32 a228, v180
v_accvgpr_write_b32 a229, v181
v_accvgpr_read_b32 v182, a230
v_accvgpr_read_b32 v183, a231
v_pk_mul_f32 v[182:183], v[182:183], v[170:171]
v_accvgpr_write_b32 a230, v182
v_accvgpr_write_b32 a231, v183
v_accvgpr_read_b32 v184, a232
v_accvgpr_read_b32 v185, a233
v_pk_mul_f32 v[184:185], v[184:185], v[170:171]
v_accvgpr_write_b32 a232, v184
v_accvgpr_write_b32 a233, v185
v_accvgpr_read_b32 v186, a234
v_accvgpr_read_b32 v187, a235
v_pk_mul_f32 v[186:187], v[186:187], v[170:171]
v_accvgpr_write_b32 a234, v186
v_accvgpr_write_b32 a235, v187
v_accvgpr_read_b32 v188, a236
v_accvgpr_read_b32 v189, a237
v_pk_mul_f32 v[188:189], v[188:189], v[170:171]
v_accvgpr_write_b32 a236, v188
v_accvgpr_write_b32 a237, v189
v_accvgpr_read_b32 v190, a238
v_accvgpr_read_b32 v191, a239
v_pk_mul_f32 v[190:191], v[190:191], v[170:171]
v_accvgpr_write_b32 a238, v190
v_accvgpr_write_b32 a239, v191
v_accvgpr_read_b32 v176, a240
v_accvgpr_read_b32 v177, a241
v_pk_mul_f32 v[176:177], v[176:177], v[172:173]
v_accvgpr_write_b32 a240, v176
v_accvgpr_write_b32 a241, v177
v_accvgpr_read_b32 v178, a242
v_accvgpr_read_b32 v179, a243
v_pk_mul_f32 v[178:179], v[178:179], v[172:173]
v_accvgpr_write_b32 a242, v178
v_accvgpr_write_b32 a243, v179
v_accvgpr_read_b32 v180, a244
v_accvgpr_read_b32 v181, a245
v_pk_mul_f32 v[180:181], v[180:181], v[172:173]
v_accvgpr_write_b32 a244, v180
v_accvgpr_write_b32 a245, v181
v_accvgpr_read_b32 v182, a246
v_accvgpr_read_b32 v183, a247
v_pk_mul_f32 v[182:183], v[182:183], v[172:173]
v_accvgpr_write_b32 a246, v182
v_accvgpr_write_b32 a247, v183
v_accvgpr_read_b32 v184, a248
v_accvgpr_read_b32 v185, a249
v_pk_mul_f32 v[184:185], v[184:185], v[172:173]
v_accvgpr_write_b32 a248, v184
v_accvgpr_write_b32 a249, v185
v_accvgpr_read_b32 v186, a250
v_accvgpr_read_b32 v187, a251
v_pk_mul_f32 v[186:187], v[186:187], v[172:173]
v_accvgpr_write_b32 a250, v186
v_accvgpr_write_b32 a251, v187
v_accvgpr_read_b32 v188, a252
v_accvgpr_read_b32 v189, a253
v_pk_mul_f32 v[188:189], v[188:189], v[172:173]
v_accvgpr_write_b32 a252, v188
v_accvgpr_write_b32 a253, v189
v_accvgpr_read_b32 v190, a254
v_accvgpr_read_b32 v191, a255
v_pk_mul_f32 v[190:191], v[190:191], v[172:173]
v_accvgpr_write_b32 a254, v190
v_accvgpr_write_b32 a255, v191
L_rs_1_0:
s_cmp_eq_u32 s75, 1
s_cbranch_scc1 L_rl_1_0
v_add_f32 v32, v32, v167
v_add_f32 v33, v33, v167
v_add_f32 v34, v34, v167
v_add_f32 v35, v35, v167
v_add_f32 v36, v36, v167
v_add_f32 v37, v37, v167
v_add_f32 v38, v38, v167
v_add_f32 v39, v39, v167
v_add_f32 v40, v40, v167
v_add_f32 v41, v41, v167
v_add_f32 v42, v42, v167
v_add_f32 v43, v43, v167
v_add_f32 v44, v44, v167
v_add_f32 v45, v45, v167
v_add_f32 v46, v46, v167
v_add_f32 v47, v47, v167
v_add_f32 v48, v48, v168
v_add_f32 v49, v49, v168
v_add_f32 v50, v50, v168
v_add_f32 v51, v51, v168
v_add_f32 v52, v52, v168
v_add_f32 v53, v53, v168
v_add_f32 v54, v54, v168
v_add_f32 v55, v55, v168
v_add_f32 v56, v56, v168
v_add_f32 v57, v57, v168
v_add_f32 v58, v58, v168
v_add_f32 v59, v59, v168
v_add_f32 v60, v60, v168
v_add_f32 v61, v61, v168
v_add_f32 v62, v62, v168
v_add_f32 v63, v63, v168
L_rl_1_0:
v_sub_f32 v0, v0, v165
v_exp_f32 v0, v0
v_sub_f32 v1, v1, v165
v_exp_f32 v1, v1
v_sub_f32 v2, v2, v165
v_exp_f32 v2, v2
v_sub_f32 v3, v3, v165
v_exp_f32 v3, v3
v_sub_f32 v4, v4, v165
v_exp_f32 v4, v4
v_sub_f32 v5, v5, v165
v_exp_f32 v5, v5
v_sub_f32 v6, v6, v165
v_exp_f32 v6, v6
v_sub_f32 v7, v7, v165
v_exp_f32 v7, v7
v_sub_f32 v8, v8, v165
v_exp_f32 v8, v8
v_sub_f32 v9, v9, v165
v_exp_f32 v9, v9
v_sub_f32 v10, v10, v165
v_exp_f32 v10, v10
v_sub_f32 v11, v11, v165
v_exp_f32 v11, v11
v_sub_f32 v12, v12, v165
v_exp_f32 v12, v12
v_sub_f32 v13, v13, v165
v_exp_f32 v13, v13
v_sub_f32 v14, v14, v165
v_exp_f32 v14, v14
v_sub_f32 v15, v15, v165
v_exp_f32 v15, v15
s_nop 0
v_add_f32 v108, v0, v1
v_add_f32 v108, v108, v2
v_add_f32 v108, v108, v3
v_add_f32 v108, v108, v4
v_add_f32 v108, v108, v5
v_add_f32 v108, v108, v6
v_add_f32 v108, v108, v7
v_add_f32 v108, v108, v8
v_add_f32 v108, v108, v9
v_add_f32 v108, v108, v10
v_add_f32 v108, v108, v11
v_add_f32 v108, v108, v12
v_add_f32 v108, v108, v13
v_add_f32 v108, v108, v14
v_add_f32 v108, v108, v15
v_cvt_pk_bf16_f32 v0, v0, v1
v_cvt_pk_bf16_f32 v1, v2, v3
v_cvt_pk_bf16_f32 v2, v4, v5
v_cvt_pk_bf16_f32 v3, v6, v7
v_cvt_pk_bf16_f32 v8, v8, v9
v_cvt_pk_bf16_f32 v9, v10, v11
v_cvt_pk_bf16_f32 v10, v12, v13
v_cvt_pk_bf16_f32 v11, v14, v15
v_sub_f32 v16, v16, v166
v_exp_f32 v16, v16
v_sub_f32 v17, v17, v166
v_exp_f32 v17, v17
v_sub_f32 v18, v18, v166
v_exp_f32 v18, v18
v_sub_f32 v19, v19, v166
v_exp_f32 v19, v19
v_sub_f32 v20, v20, v166
v_exp_f32 v20, v20
v_sub_f32 v21, v21, v166
v_exp_f32 v21, v21
v_sub_f32 v22, v22, v166
v_exp_f32 v22, v22
v_sub_f32 v23, v23, v166
v_exp_f32 v23, v23
v_sub_f32 v24, v24, v166
v_exp_f32 v24, v24
v_sub_f32 v25, v25, v166
v_exp_f32 v25, v25
v_sub_f32 v26, v26, v166
v_exp_f32 v26, v26
v_sub_f32 v27, v27, v166
v_exp_f32 v27, v27
v_sub_f32 v28, v28, v166
v_exp_f32 v28, v28
v_sub_f32 v29, v29, v166
v_exp_f32 v29, v29
v_sub_f32 v30, v30, v166
v_exp_f32 v30, v30
v_sub_f32 v31, v31, v166
v_exp_f32 v31, v31
s_nop 0
v_add_f32 v109, v16, v17
v_add_f32 v109, v109, v18
v_add_f32 v109, v109, v19
v_add_f32 v109, v109, v20
v_add_f32 v109, v109, v21
v_add_f32 v109, v109, v22
v_add_f32 v109, v109, v23
v_add_f32 v109, v109, v24
v_add_f32 v109, v109, v25
v_add_f32 v109, v109, v26
v_add_f32 v109, v109, v27
v_add_f32 v109, v109, v28
v_add_f32 v109, v109, v29
v_add_f32 v109, v109, v30
v_add_f32 v109, v109, v31
v_cvt_pk_bf16_f32 v16, v16, v17
v_cvt_pk_bf16_f32 v17, v18, v19
v_cvt_pk_bf16_f32 v18, v20, v21
v_cvt_pk_bf16_f32 v19, v22, v23
v_cvt_pk_bf16_f32 v24, v24, v25
v_cvt_pk_bf16_f32 v25, v26, v27
v_cvt_pk_bf16_f32 v26, v28, v29
v_cvt_pk_bf16_f32 v27, v30, v31
s_nop 3
s_add_u32 s72, s72, 4
s_addc_u32 s73, s73, 0
s_setpc_b64 s[72:73]
L_rare_1_1:
s_waitcnt lgkmcnt(0)
s_nop 15
v_mbcnt_lo_u32_b32 v160, -1, 0
v_mbcnt_hi_u32_b32 v160, -1, v160
v_and_b32 v161, 31, v160
v_lshrrev_b32 v162, 5, v160
v_and_b32 v163, 15, v161
v_or_b32 v164, 0, v162
v_xor_b32 v163, v163, v164
v_lshlrev_b32 v163, 4, v163
v_lshl_add_u32 v163, v161, 8, v163
v_add_u32 v163, s70, v163
ds_read_b128 v[176:179], v163 offset:0
s_waitcnt lgkmcnt(0)
v_mfma_f32_32x32x16_bf16 v[32:47], v[176:179], v[192:195], 0
v_mfma_f32_32x32x16_bf16 v[48:63], v[176:179], v[208:211], 0
v_and_b32 v163, 15, v161
v_or_b32 v164, 2, v162
v_xor_b32 v163, v163, v164
v_lshlrev_b32 v163, 4, v163
v_lshl_add_u32 v163, v161, 8, v163
v_add_u32 v163, s70, v163
ds_read_b128 v[180:183], v163 offset:0
s_waitcnt lgkmcnt(0)
v_mfma_f32_32x32x16_bf16 v[32:47], v[180:183], v[196:199], v[32:47]
v_mfma_f32_32x32x16_bf16 v[48:63], v[180:183], v[212:215], v[48:63]
v_and_b32 v163, 15, v161
v_or_b32 v164, 4, v162
v_xor_b32 v163, v163, v164
v_lshlrev_b32 v163, 4, v163
v_lshl_add_u32 v163, v161, 8, v163
v_add_u32 v163, s70, v163
ds_read_b128 v[184:187], v163 offset:0
s_waitcnt lgkmcnt(0)
v_mfma_f32_32x32x16_bf16 v[32:47], v[184:187], v[224:227], v[32:47]
v_mfma_f32_32x32x16_bf16 v[48:63], v[184:187], v[240:243], v[48:63]
v_and_b32 v163, 15, v161
v_or_b32 v164, 6, v162
v_xor_b32 v163, v163, v164
v_lshlrev_b32 v163, 4, v163
v_lshl_add_u32 v163, v161, 8, v163
v_add_u32 v163, s70, v163
ds_read_b128 v[188:191], v163 offset:0
s_waitcnt lgkmcnt(0)
v_mfma_f32_32x32x16_bf16 v[32:47], v[188:191], v[228:231], v[32:47]
v_mfma_f32_32x32x16_bf16 v[48:63], v[188:191], v[244:247], v[48:63]
v_and_b32 v163, 15, v161
v_or_b32 v164, 8, v162
v_xor_b32 v163, v163, v164
v_lshlrev_b32 v163, 4, v163
v_lshl_add_u32 v163, v161, 8, v163
v_add_u32 v163, s70, v163
ds_read_b128 v[176:179], v163 offset:0
s_waitcnt lgkmcnt(0)
v_mfma_f32_32x32x16_bf16 v[32:47], v[176:179], v[200:203], v[32:47]
v_mfma_f32_32x32x16_bf16 v[48:63], v[176:179], v[216:219], v[48:63]
v_and_b32 v163, 15, v161
v_or_b32 v164, 10, v162
v_xor_b32 v163, v163, v164
v_lshlrev_b32 v163, 4, v163
v_lshl_add_u32 v163, v161, 8, v163
v_add_u32 v163, s70, v163
ds_read_b128 v[180:183], v163 offset:0
s_waitcnt lgkmcnt(0)
v_mfma_f32_32x32x16_bf16 v[32:47], v[180:183], v[204:207], v[32:47]
v_mfma_f32_32x32x16_bf16 v[48:63], v[180:183], v[220:223], v[48:63]
v_and_b32 v163, 15, v161
v_or_b32 v164, 12, v162
v_xor_b32 v163, v163, v164
v_lshlrev_b32 v163, 4, v163
v_lshl_add_u32 v163, v161, 8, v163
v_add_u32 v163, s70, v163
ds_read_b128 v[184:187], v163 offset:0
s_waitcnt lgkmcnt(0)
v_mfma_f32_32x32x16_bf16 v[32:47], v[184:187], v[232:235], v[32:47]
v_mfma_f32_32x32x16_bf16 v[48:63], v[184:187], v[248:251], v[48:63]
v_and_b32 v163, 15, v161
v_or_b32 v164, 14, v162
v_xor_b32 v163, v163, v164
v_lshlrev_b32 v163, 4, v163
v_lshl_add_u32 v163, v161, 8, v163
v_add_u32 v163, s70, v163
ds_read_b128 v[188:191], v163 offset:0
s_waitcnt lgkmcnt(0)
v_mfma_f32_32x32x16_bf16 v[32:47], v[188:191], v[236:239], v[32:47]
v_mfma_f32_32x32x16_bf16 v[48:63], v[188:191], v[252:255], v[48:63]
s_nop 15
v_max3_f32 v165, v32, v33, v34
v_max3_f32 v165, v165, v35, v36
v_max3_f32 v165, v165, v37, v38
v_max3_f32 v165, v165, v39, v40
v_max3_f32 v165, v165, v41, v42
v_max3_f32 v165, v165, v43, v44
v_max3_f32 v165, v165, v45, v46
v_max_f32 v165, v165, v47
v_mov_b32 v163, v165
s_nop 1
v_permlane32_swap_b32 v165, v163
v_max_f32 v165, v165, v163
v_max3_f32 v166, v48, v49, v50
v_max3_f32 v166, v166, v51, v52
v_max3_f32 v166, v166, v53, v54
v_max3_f32 v166, v166, v55, v56
v_max3_f32 v166, v166, v57, v58
v_max3_f32 v166, v166, v59, v60
v_max3_f32 v166, v166, v61, v62
v_max_f32 v166, v166, v63
v_mov_b32 v163, v166
s_nop 1
v_permlane32_swap_b32 v166, v163
v_max_f32 v166, v166, v163
s_cmp_eq_u32 s71, 1
s_cbranch_scc1 L_rf_1_1
v_max_f32 v165, v165, v110
v_max_f32 v166, v166, v111
L_rf_1_1:
v_sub_f32 v167, v110, v165
v_min_f32 v163, 0, v167
v_exp_f32 v170, v163
s_nop 0
v_mov_b32 v171, v170
v_mul_f32 v112, v112, v170
v_mov_b32 v110, v165
v_sub_f32 v128, 0, v165
v_sub_f32 v129, 0, v165
v_sub_f32 v130, 0, v165
v_sub_f32 v131, 0, v165
v_sub_f32 v132, 0, v165
v_sub_f32 v133, 0, v165
v_sub_f32 v134, 0, v165
v_sub_f32 v135, 0, v165
v_sub_f32 v136, 0, v165
v_sub_f32 v137, 0, v165
v_sub_f32 v138, 0, v165
v_sub_f32 v139, 0, v165
v_sub_f32 v140, 0, v165
v_sub_f32 v141, 0, v165
v_sub_f32 v142, 0, v165
v_sub_f32 v143, 0, v165
v_sub_f32 v168, v111, v166
v_min_f32 v163, 0, v168
v_exp_f32 v172, v163
s_nop 0
v_mov_b32 v173, v172
v_mul_f32 v113, v113, v172
v_mov_b32 v111, v166
v_sub_f32 v144, 0, v166
v_sub_f32 v145, 0, v166
v_sub_f32 v146, 0, v166
v_sub_f32 v147, 0, v166
v_sub_f32 v148, 0, v166
v_sub_f32 v149, 0, v166
v_sub_f32 v150, 0, v166
v_sub_f32 v151, 0, v166
v_sub_f32 v152, 0, v166
v_sub_f32 v153, 0, v166
v_sub_f32 v154, 0, v166
v_sub_f32 v155, 0, v166
v_sub_f32 v156, 0, v166
v_sub_f32 v157, 0, v166
v_sub_f32 v158, 0, v166
v_sub_f32 v159, 0, v166
s_cmp_eq_u32 s71, 1
s_cbranch_scc1 L_rs_1_1
v_accvgpr_read_b32 v176, a128
v_accvgpr_read_b32 v177, a129
v_pk_mul_f32 v[176:177], v[176:177], v[170:171]
v_accvgpr_write_b32 a128, v176
v_accvgpr_write_b32 a129, v177
v_accvgpr_read_b32 v178, a130
v_accvgpr_read_b32 v179, a131
v_pk_mul_f32 v[178:179], v[178:179], v[170:171]
v_accvgpr_write_b32 a130, v178
v_accvgpr_write_b32 a131, v179
v_accvgpr_read_b32 v180, a132
v_accvgpr_read_b32 v181, a133
v_pk_mul_f32 v[180:181], v[180:181], v[170:171]
v_accvgpr_write_b32 a132, v180
v_accvgpr_write_b32 a133, v181
v_accvgpr_read_b32 v182, a134
v_accvgpr_read_b32 v183, a135
v_pk_mul_f32 v[182:183], v[182:183], v[170:171]
v_accvgpr_write_b32 a134, v182
v_accvgpr_write_b32 a135, v183
v_accvgpr_read_b32 v184, a136
v_accvgpr_read_b32 v185, a137
v_pk_mul_f32 v[184:185], v[184:185], v[170:171]
v_accvgpr_write_b32 a136, v184
v_accvgpr_write_b32 a137, v185
v_accvgpr_read_b32 v186, a138
v_accvgpr_read_b32 v187, a139
v_pk_mul_f32 v[186:187], v[186:187], v[170:171]
v_accvgpr_write_b32 a138, v186
v_accvgpr_write_b32 a139, v187
v_accvgpr_read_b32 v188, a140
v_accvgpr_read_b32 v189, a141
v_pk_mul_f32 v[188:189], v[188:189], v[170:171]
v_accvgpr_write_b32 a140, v188
v_accvgpr_write_b32 a141, v189
v_accvgpr_read_b32 v190, a142
v_accvgpr_read_b32 v191, a143
v_pk_mul_f32 v[190:191], v[190:191], v[170:171]
v_accvgpr_write_b32 a142, v190
v_accvgpr_write_b32 a143, v191
v_accvgpr_read_b32 v176, a144
v_accvgpr_read_b32 v177, a145
v_pk_mul_f32 v[176:177], v[176:177], v[172:173]
v_accvgpr_write_b32 a144, v176
v_accvgpr_write_b32 a145, v177
v_accvgpr_read_b32 v178, a146
v_accvgpr_read_b32 v179, a147
v_pk_mul_f32 v[178:179], v[178:179], v[172:173]
v_accvgpr_write_b32 a146, v178
v_accvgpr_write_b32 a147, v179
v_accvgpr_read_b32 v180, a148
v_accvgpr_read_b32 v181, a149
v_pk_mul_f32 v[180:181], v[180:181], v[172:173]
v_accvgpr_write_b32 a148, v180
v_accvgpr_write_b32 a149, v181
v_accvgpr_read_b32 v182, a150
v_accvgpr_read_b32 v183, a151
v_pk_mul_f32 v[182:183], v[182:183], v[172:173]
v_accvgpr_write_b32 a150, v182
v_accvgpr_write_b32 a151, v183
v_accvgpr_read_b32 v184, a152
v_accvgpr_read_b32 v185, a153
v_pk_mul_f32 v[184:185], v[184:185], v[172:173]
v_accvgpr_write_b32 a152, v184
v_accvgpr_write_b32 a153, v185
v_accvgpr_read_b32 v186, a154
v_accvgpr_read_b32 v187, a155
v_pk_mul_f32 v[186:187], v[186:187], v[172:173]
v_accvgpr_write_b32 a154, v186
v_accvgpr_write_b32 a155, v187
v_accvgpr_read_b32 v188, a156
v_accvgpr_read_b32 v189, a157
v_pk_mul_f32 v[188:189], v[188:189], v[172:173]
v_accvgpr_write_b32 a156, v188
v_accvgpr_write_b32 a157, v189
v_accvgpr_read_b32 v190, a158
v_accvgpr_read_b32 v191, a159
v_pk_mul_f32 v[190:191], v[190:191], v[172:173]
v_accvgpr_write_b32 a158, v190
v_accvgpr_write_b32 a159, v191
v_accvgpr_read_b32 v176, a160
v_accvgpr_read_b32 v177, a161
v_pk_mul_f32 v[176:177], v[176:177], v[170:171]
v_accvgpr_write_b32 a160, v176
v_accvgpr_write_b32 a161, v177
v_accvgpr_read_b32 v178, a162
v_accvgpr_read_b32 v179, a163
v_pk_mul_f32 v[178:179], v[178:179], v[170:171]
v_accvgpr_write_b32 a162, v178
v_accvgpr_write_b32 a163, v179
v_accvgpr_read_b32 v180, a164
v_accvgpr_read_b32 v181, a165
v_pk_mul_f32 v[180:181], v[180:181], v[170:171]
v_accvgpr_write_b32 a164, v180
v_accvgpr_write_b32 a165, v181
v_accvgpr_read_b32 v182, a166
v_accvgpr_read_b32 v183, a167
v_pk_mul_f32 v[182:183], v[182:183], v[170:171]
v_accvgpr_write_b32 a166, v182
v_accvgpr_write_b32 a167, v183
v_accvgpr_read_b32 v184, a168
v_accvgpr_read_b32 v185, a169
v_pk_mul_f32 v[184:185], v[184:185], v[170:171]
v_accvgpr_write_b32 a168, v184
v_accvgpr_write_b32 a169, v185
v_accvgpr_read_b32 v186, a170
v_accvgpr_read_b32 v187, a171
v_pk_mul_f32 v[186:187], v[186:187], v[170:171]
v_accvgpr_write_b32 a170, v186
v_accvgpr_write_b32 a171, v187
v_accvgpr_read_b32 v188, a172
v_accvgpr_read_b32 v189, a173
v_pk_mul_f32 v[188:189], v[188:189], v[170:171]
v_accvgpr_write_b32 a172, v188
v_accvgpr_write_b32 a173, v189
v_accvgpr_read_b32 v190, a174
v_accvgpr_read_b32 v191, a175
v_pk_mul_f32 v[190:191], v[190:191], v[170:171]
v_accvgpr_write_b32 a174, v190
v_accvgpr_write_b32 a175, v191
v_accvgpr_read_b32 v176, a176
v_accvgpr_read_b32 v177, a177
v_pk_mul_f32 v[176:177], v[176:177], v[172:173]
v_accvgpr_write_b32 a176, v176
v_accvgpr_write_b32 a177, v177
v_accvgpr_read_b32 v178, a178
v_accvgpr_read_b32 v179, a179
v_pk_mul_f32 v[178:179], v[178:179], v[172:173]
v_accvgpr_write_b32 a178, v178
v_accvgpr_write_b32 a179, v179
v_accvgpr_read_b32 v180, a180
v_accvgpr_read_b32 v181, a181
v_pk_mul_f32 v[180:181], v[180:181], v[172:173]
v_accvgpr_write_b32 a180, v180
v_accvgpr_write_b32 a181, v181
v_accvgpr_read_b32 v182, a182
v_accvgpr_read_b32 v183, a183
v_pk_mul_f32 v[182:183], v[182:183], v[172:173]
v_accvgpr_write_b32 a182, v182
v_accvgpr_write_b32 a183, v183
v_accvgpr_read_b32 v184, a184
v_accvgpr_read_b32 v185, a185
v_pk_mul_f32 v[184:185], v[184:185], v[172:173]
v_accvgpr_write_b32 a184, v184
v_accvgpr_write_b32 a185, v185
v_accvgpr_read_b32 v186, a186
v_accvgpr_read_b32 v187, a187
v_pk_mul_f32 v[186:187], v[186:187], v[172:173]
v_accvgpr_write_b32 a186, v186
v_accvgpr_write_b32 a187, v187
v_accvgpr_read_b32 v188, a188
v_accvgpr_read_b32 v189, a189
v_pk_mul_f32 v[188:189], v[188:189], v[172:173]
v_accvgpr_write_b32 a188, v188
v_accvgpr_write_b32 a189, v189
v_accvgpr_read_b32 v190, a190
v_accvgpr_read_b32 v191, a191
v_pk_mul_f32 v[190:191], v[190:191], v[172:173]
v_accvgpr_write_b32 a190, v190
v_accvgpr_write_b32 a191, v191
v_accvgpr_read_b32 v176, a192
v_accvgpr_read_b32 v177, a193
v_pk_mul_f32 v[176:177], v[176:177], v[170:171]
v_accvgpr_write_b32 a192, v176
v_accvgpr_write_b32 a193, v177
v_accvgpr_read_b32 v178, a194
v_accvgpr_read_b32 v179, a195
v_pk_mul_f32 v[178:179], v[178:179], v[170:171]
v_accvgpr_write_b32 a194, v178
v_accvgpr_write_b32 a195, v179
v_accvgpr_read_b32 v180, a196
v_accvgpr_read_b32 v181, a197
v_pk_mul_f32 v[180:181], v[180:181], v[170:171]
v_accvgpr_write_b32 a196, v180
v_accvgpr_write_b32 a197, v181
v_accvgpr_read_b32 v182, a198
v_accvgpr_read_b32 v183, a199
v_pk_mul_f32 v[182:183], v[182:183], v[170:171]
v_accvgpr_write_b32 a198, v182
v_accvgpr_write_b32 a199, v183
v_accvgpr_read_b32 v184, a200
v_accvgpr_read_b32 v185, a201
v_pk_mul_f32 v[184:185], v[184:185], v[170:171]
v_accvgpr_write_b32 a200, v184
v_accvgpr_write_b32 a201, v185
v_accvgpr_read_b32 v186, a202
v_accvgpr_read_b32 v187, a203
v_pk_mul_f32 v[186:187], v[186:187], v[170:171]
v_accvgpr_write_b32 a202, v186
v_accvgpr_write_b32 a203, v187
v_accvgpr_read_b32 v188, a204
v_accvgpr_read_b32 v189, a205
v_pk_mul_f32 v[188:189], v[188:189], v[170:171]
v_accvgpr_write_b32 a204, v188
v_accvgpr_write_b32 a205, v189
v_accvgpr_read_b32 v190, a206
v_accvgpr_read_b32 v191, a207
v_pk_mul_f32 v[190:191], v[190:191], v[170:171]
v_accvgpr_write_b32 a206, v190
v_accvgpr_write_b32 a207, v191
v_accvgpr_read_b32 v176, a208
v_accvgpr_read_b32 v177, a209
v_pk_mul_f32 v[176:177], v[176:177], v[172:173]
v_accvgpr_write_b32 a208, v176
v_accvgpr_write_b32 a209, v177
v_accvgpr_read_b32 v178, a210
v_accvgpr_read_b32 v179, a211
v_pk_mul_f32 v[178:179], v[178:179], v[172:173]
v_accvgpr_write_b32 a210, v178
v_accvgpr_write_b32 a211, v179
v_accvgpr_read_b32 v180, a212
v_accvgpr_read_b32 v181, a213
v_pk_mul_f32 v[180:181], v[180:181], v[172:173]
v_accvgpr_write_b32 a212, v180
v_accvgpr_write_b32 a213, v181
v_accvgpr_read_b32 v182, a214
v_accvgpr_read_b32 v183, a215
v_pk_mul_f32 v[182:183], v[182:183], v[172:173]
v_accvgpr_write_b32 a214, v182
v_accvgpr_write_b32 a215, v183
v_accvgpr_read_b32 v184, a216
v_accvgpr_read_b32 v185, a217
v_pk_mul_f32 v[184:185], v[184:185], v[172:173]
v_accvgpr_write_b32 a216, v184
v_accvgpr_write_b32 a217, v185
v_accvgpr_read_b32 v186, a218
v_accvgpr_read_b32 v187, a219
v_pk_mul_f32 v[186:187], v[186:187], v[172:173]
v_accvgpr_write_b32 a218, v186
v_accvgpr_write_b32 a219, v187
v_accvgpr_read_b32 v188, a220
v_accvgpr_read_b32 v189, a221
v_pk_mul_f32 v[188:189], v[188:189], v[172:173]
v_accvgpr_write_b32 a220, v188
v_accvgpr_write_b32 a221, v189
v_accvgpr_read_b32 v190, a222
v_accvgpr_read_b32 v191, a223
v_pk_mul_f32 v[190:191], v[190:191], v[172:173]
v_accvgpr_write_b32 a222, v190
v_accvgpr_write_b32 a223, v191
v_accvgpr_read_b32 v176, a224
v_accvgpr_read_b32 v177, a225
v_pk_mul_f32 v[176:177], v[176:177], v[170:171]
v_accvgpr_write_b32 a224, v176
v_accvgpr_write_b32 a225, v177
v_accvgpr_read_b32 v178, a226
v_accvgpr_read_b32 v179, a227
v_pk_mul_f32 v[178:179], v[178:179], v[170:171]
v_accvgpr_write_b32 a226, v178
v_accvgpr_write_b32 a227, v179
v_accvgpr_read_b32 v180, a228
v_accvgpr_read_b32 v181, a229
v_pk_mul_f32 v[180:181], v[180:181], v[170:171]
v_accvgpr_write_b32 a228, v180
v_accvgpr_write_b32 a229, v181
v_accvgpr_read_b32 v182, a230
v_accvgpr_read_b32 v183, a231
v_pk_mul_f32 v[182:183], v[182:183], v[170:171]
v_accvgpr_write_b32 a230, v182
v_accvgpr_write_b32 a231, v183
v_accvgpr_read_b32 v184, a232
v_accvgpr_read_b32 v185, a233
v_pk_mul_f32 v[184:185], v[184:185], v[170:171]
v_accvgpr_write_b32 a232, v184
v_accvgpr_write_b32 a233, v185
v_accvgpr_read_b32 v186, a234
v_accvgpr_read_b32 v187, a235
v_pk_mul_f32 v[186:187], v[186:187], v[170:171]
v_accvgpr_write_b32 a234, v186
v_accvgpr_write_b32 a235, v187
v_accvgpr_read_b32 v188, a236
v_accvgpr_read_b32 v189, a237
v_pk_mul_f32 v[188:189], v[188:189], v[170:171]
v_accvgpr_write_b32 a236, v188
v_accvgpr_write_b32 a237, v189
v_accvgpr_read_b32 v190, a238
v_accvgpr_read_b32 v191, a239
v_pk_mul_f32 v[190:191], v[190:191], v[170:171]
v_accvgpr_write_b32 a238, v190
v_accvgpr_write_b32 a239, v191
v_accvgpr_read_b32 v176, a240
v_accvgpr_read_b32 v177, a241
v_pk_mul_f32 v[176:177], v[176:177], v[172:173]
v_accvgpr_write_b32 a240, v176
v_accvgpr_write_b32 a241, v177
v_accvgpr_read_b32 v178, a242
v_accvgpr_read_b32 v179, a243
v_pk_mul_f32 v[178:179], v[178:179], v[172:173]
v_accvgpr_write_b32 a242, v178
v_accvgpr_write_b32 a243, v179
v_accvgpr_read_b32 v180, a244
v_accvgpr_read_b32 v181, a245
v_pk_mul_f32 v[180:181], v[180:181], v[172:173]
v_accvgpr_write_b32 a244, v180
v_accvgpr_write_b32 a245, v181
v_accvgpr_read_b32 v182, a246
v_accvgpr_read_b32 v183, a247
v_pk_mul_f32 v[182:183], v[182:183], v[172:173]
v_accvgpr_write_b32 a246, v182
v_accvgpr_write_b32 a247, v183
v_accvgpr_read_b32 v184, a248
v_accvgpr_read_b32 v185, a249
v_pk_mul_f32 v[184:185], v[184:185], v[172:173]
v_accvgpr_write_b32 a248, v184
v_accvgpr_write_b32 a249, v185
v_accvgpr_read_b32 v186, a250
v_accvgpr_read_b32 v187, a251
v_pk_mul_f32 v[186:187], v[186:187], v[172:173]
v_accvgpr_write_b32 a250, v186
v_accvgpr_write_b32 a251, v187
v_accvgpr_read_b32 v188, a252
v_accvgpr_read_b32 v189, a253
v_pk_mul_f32 v[188:189], v[188:189], v[172:173]
v_accvgpr_write_b32 a252, v188
v_accvgpr_write_b32 a253, v189
v_accvgpr_read_b32 v190, a254
v_accvgpr_read_b32 v191, a255
v_pk_mul_f32 v[190:191], v[190:191], v[172:173]
v_accvgpr_write_b32 a254, v190
v_accvgpr_write_b32 a255, v191
L_rs_1_1:
s_cmp_eq_u32 s75, 1
s_cbranch_scc1 L_rl_1_1
v_add_f32 v0, v0, v167
v_add_f32 v1, v1, v167
v_add_f32 v2, v2, v167
v_add_f32 v3, v3, v167
v_add_f32 v4, v4, v167
v_add_f32 v5, v5, v167
v_add_f32 v6, v6, v167
v_add_f32 v7, v7, v167
v_add_f32 v8, v8, v167
v_add_f32 v9, v9, v167
v_add_f32 v10, v10, v167
v_add_f32 v11, v11, v167
v_add_f32 v12, v12, v167
v_add_f32 v13, v13, v167
v_add_f32 v14, v14, v167
v_add_f32 v15, v15, v167
v_add_f32 v16, v16, v168
v_add_f32 v17, v17, v168
v_add_f32 v18, v18, v168
v_add_f32 v19, v19, v168
v_add_f32 v20, v20, v168
v_add_f32 v21, v21, v168
v_add_f32 v22, v22, v168
v_add_f32 v23, v23, v168
v_add_f32 v24, v24, v168
v_add_f32 v25, v25, v168
v_add_f32 v26, v26, v168
v_add_f32 v27, v27, v168
v_add_f32 v28, v28, v168
v_add_f32 v29, v29, v168
v_add_f32 v30, v30, v168
v_add_f32 v31, v31, v168
L_rl_1_1:
v_sub_f32 v32, v32, v165
v_exp_f32 v32, v32
v_sub_f32 v33, v33, v165
v_exp_f32 v33, v33
v_sub_f32 v34, v34, v165
v_exp_f32 v34, v34
v_sub_f32 v35, v35, v165
v_exp_f32 v35, v35
v_sub_f32 v36, v36, v165
v_exp_f32 v36, v36
v_sub_f32 v37, v37, v165
v_exp_f32 v37, v37
v_sub_f32 v38, v38, v165
v_exp_f32 v38, v38
v_sub_f32 v39, v39, v165
v_exp_f32 v39, v39
v_sub_f32 v40, v40, v165
v_exp_f32 v40, v40
v_sub_f32 v41, v41, v165
v_exp_f32 v41, v41
v_sub_f32 v42, v42, v165
v_exp_f32 v42, v42
v_sub_f32 v43, v43, v165
v_exp_f32 v43, v43
v_sub_f32 v44, v44, v165
v_exp_f32 v44, v44
v_sub_f32 v45, v45, v165
v_exp_f32 v45, v45
v_sub_f32 v46, v46, v165
v_exp_f32 v46, v46
v_sub_f32 v47, v47, v165
v_exp_f32 v47, v47
s_nop 0
v_add_f32 v108, v32, v33
v_add_f32 v108, v108, v34
v_add_f32 v108, v108, v35
v_add_f32 v108, v108, v36
v_add_f32 v108, v108, v37
v_add_f32 v108, v108, v38
v_add_f32 v108, v108, v39
v_add_f32 v108, v108, v40
v_add_f32 v108, v108, v41
v_add_f32 v108, v108, v42
v_add_f32 v108, v108, v43
v_add_f32 v108, v108, v44
v_add_f32 v108, v108, v45
v_add_f32 v108, v108, v46
v_add_f32 v108, v108, v47
v_cvt_pk_bf16_f32 v32, v32, v33
v_cvt_pk_bf16_f32 v33, v34, v35
v_cvt_pk_bf16_f32 v34, v36, v37
v_cvt_pk_bf16_f32 v35, v38, v39
v_cvt_pk_bf16_f32 v40, v40, v41
v_cvt_pk_bf16_f32 v41, v42, v43
v_cvt_pk_bf16_f32 v42, v44, v45
v_cvt_pk_bf16_f32 v43, v46, v47
v_sub_f32 v48, v48, v166
v_exp_f32 v48, v48
v_sub_f32 v49, v49, v166
v_exp_f32 v49, v49
v_sub_f32 v50, v50, v166
v_exp_f32 v50, v50
v_sub_f32 v51, v51, v166
v_exp_f32 v51, v51
v_sub_f32 v52, v52, v166
v_exp_f32 v52, v52
v_sub_f32 v53, v53, v166
v_exp_f32 v53, v53
v_sub_f32 v54, v54, v166
v_exp_f32 v54, v54
v_sub_f32 v55, v55, v166
v_exp_f32 v55, v55
v_sub_f32 v56, v56, v166
v_exp_f32 v56, v56
v_sub_f32 v57, v57, v166
v_exp_f32 v57, v57
v_sub_f32 v58, v58, v166
v_exp_f32 v58, v58
v_sub_f32 v59, v59, v166
v_exp_f32 v59, v59
v_sub_f32 v60, v60, v166
v_exp_f32 v60, v60
v_sub_f32 v61, v61, v166
v_exp_f32 v61, v61
v_sub_f32 v62, v62, v166
v_exp_f32 v62, v62
v_sub_f32 v63, v63, v166
v_exp_f32 v63, v63
s_nop 0
v_add_f32 v109, v48, v49
v_add_f32 v109, v109, v50
v_add_f32 v109, v109, v51
v_add_f32 v109, v109, v52
v_add_f32 v109, v109, v53
v_add_f32 v109, v109, v54
v_add_f32 v109, v109, v55
v_add_f32 v109, v109, v56
v_add_f32 v109, v109, v57
v_add_f32 v109, v109, v58
v_add_f32 v109, v109, v59
v_add_f32 v109, v109, v60
v_add_f32 v109, v109, v61
v_add_f32 v109, v109, v62
v_add_f32 v109, v109, v63
v_cvt_pk_bf16_f32 v48, v48, v49
v_cvt_pk_bf16_f32 v49, v50, v51
v_cvt_pk_bf16_f32 v50, v52, v53
v_cvt_pk_bf16_f32 v51, v54, v55
v_cvt_pk_bf16_f32 v56, v56, v57
v_cvt_pk_bf16_f32 v57, v58, v59
v_cvt_pk_bf16_f32 v58, v60, v61
v_cvt_pk_bf16_f32 v59, v62, v63
s_nop 3
s_add_u32 s72, s72, 4
s_addc_u32 s73, s73, 0
s_setpc_b64 s[72:73]
L_sp0:
s_mov_b32 s64, 0
L_spl0:
s_sleep 2
buffer_load_dword v120, off, s[76:79], s94 offset:0 sc1
buffer_load_dwordx2 v[124:125], off, s[76:79], s94 offset:16 sc1
s_waitcnt vmcnt(0)
buffer_store_dword v124, off, s[76:79], s93 sc1
s_waitcnt vmcnt(0)
v_readfirstlane_b32 s65, v120
s_cmp_lg_u32 s65, s95
s_cbranch_scc1 L_spd0
s_add_u32 s64, s64, 1
s_cmp_lt_u32 s64, 0x80000
s_cbranch_scc1 L_spl0
s_branch L_spd0
L_sp1:
s_mov_b32 s64, 0
L_spl1:
s_sleep 2
buffer_load_dword v120, off, s[76:79], s94 offset:4 sc1
buffer_load_dwordx2 v[124:125], off, s[76:79], s94 offset:16 sc1
s_waitcnt vmcnt(0)
buffer_store_dwordx2 v[124:125], off, s[76:79], s93 sc1
s_waitcnt vmcnt(0)
v_readfirstlane_b32 s65, v120
s_cmp_lg_u32 s65, s96
s_cbranch_scc1 L_spd1
s_add_u32 s64, s64, 1
s_cmp_lt_u32 s64, 0x80000
s_cbranch_scc1 L_spl1
s_branch L_spd1
L_sp2:
s_mov_b32 s64, 0
L_spl2:
s_sleep 2
buffer_load_dword v120, off, s[76:79], s94 offset:8 sc1
buffer_load_dwordx2 v[124:125], off, s[76:79], s94 offset:16 sc1
s_waitcnt vmcnt(0)
buffer_store_dwordx2 v[124:125], off, s[76:79], s93 sc1
s_waitcnt vmcnt(0)
v_readfirstlane_b32 s65, v120
s_cmp_lg_u32 s65, s69
s_cbranch_scc1 L_spd2
s_add_u32 s64, s64, 1
s_cmp_lt_u32 s64, 0x80000
s_cbranch_scc1 L_spl2
s_branch L_spd2
L_end:
)ASM" :: "s"(X), "s"(WQ), "s"(K), "s"(V), "s"(WO), "s"(PW), "s"(w), "s"(ST), "s"(FL), "s"(XB), "s"(wg) : "v0", "v1", "v2", "v3", "v4", "v5", "v6", "v7", "v8", "v9", "v10", "v11", "v12", "v13", "v14", "v15", "v16", "v17", "v18", "v19", "v20", "v21", "v22", "v23", "v24", "v25", "v26", "v27", "v28", "v29", "v30", "v31", "v32", "v33", "v34", "v35", "v36", "v37", "v38", "v39", "v40", "v41", "v42", "v43", "v44", "v45", "v46", "v47", "v48", "v49", "v50", "v51", "v52", "v53", "v54", "v55", "v56", "v57", "v58", "v59", "v60", "v61", "v62", "v63", "v64", "v65", "v66", "v67", "v68", "v69", "v70", "v71", "v72", "v73", "v74", "v75", "v76", "v77", "v78", "v79", "v80", "v81", "v82", "v83", "v84", "v85", "v86", "v87", "v88", "v89", "v90", "v91", "v92", "v93", "v94", "v95", "v96", "v97", "v98", "v99", "v100", "v101", "v102", "v103", "v104", "v105", "v106", "v107", "v108", "v109", "v110", "v111", "v112", "v113", "v114", "v115", "v116", "v117", "v118", "v119", "v120", "v121", "v122", "v123", "v124", "v125", "v126", "v127", "v128", "v129", "v130", "v131", "v132", "v133", "v134", "v135", "v136", "v137", "v138", "v139", "v140", "v141", "v142", "v143", "v144", "v145", "v146", "v147", "v148", "v149", "v150", "v151", "v152", "v153", "v154", "v155", "v156", "v157", "v158", "v159", "v160", "v161", "v162", "v163", "v164", "v165", "v166", "v167", "v168", "v169", "v170", "v171", "v172", "v173", "v174", "v175", "v176", "v177", "v178", "v179", "v180", "v181", "v182", "v183", "v184", "v185", "v186", "v187", "v188", "v189", "v190", "v191", "v192", "v193", "v194", "v195", "v196", "v197", "v198", "v199", "v200", "v201", "v202", "v203", "v204", "v205", "v206", "v207", "v208", "v209", "v210", "v211", "v212", "v213", "v214", "v215", "v216", "v217", "v218", "v219", "v220", "v221", "v222", "v223", "v224", "v225", "v226", "v227", "v228", "v229", "v230", "v231", "v232", "v233", "v234", "v235", "v236", "v237", "v238", "v239", "v240", "v241", "v242", "v243", "v244", "v245", "v246", "v247", "v248", "v249", "v250", "v251", "v252", "v253", "v254", "v255", "a0", "a1", "a2", "a3", "a4", "a5", "a6", "a7", "a8", "a9", "a10", "a11", "a12", "a13", "a14", "a15", "a16", "a17", "a18", "a19", "a20", "a21", "a22", "a23", "a24", "a25", "a26", "a27", "a28", "a29", "a30", "a31", "a32", "a33", "a34", "a35", "a36", "a37", "a38", "a39", "a40", "a41", "a42", "a43", "a44", "a45", "a46", "a47", "a48", "a49", "a50", "a51", "a52", "a53", "a54", "a55", "a56", "a57", "a58", "a59", "a60", "a61", "a62", "a63", "a64", "a65", "a66", "a67", "a68", "a69", "a70", "a71", "a72", "a73", "a74", "a75", "a76", "a77", "a78", "a79", "a80", "a81", "a82", "a83", "a84", "a85", "a86", "a87", "a88", "a89", "a90", "a91", "a92", "a93", "a94", "a95", "a96", "a97", "a98", "a99", "a100", "a101", "a102", "a103", "a104", "a105", "a106", "a107", "a108", "a109", "a110", "a111", "a112", "a113", "a114", "a115", "a116", "a117", "a118", "a119", "a120", "a121", "a122", "a123", "a124", "a125", "a126", "a127", "a128", "a129", "a130", "a131", "a132", "a133", "a134", "a135", "a136", "a137", "a138", "a139", "a140", "a141", "a142", "a143", "a144", "a145", "a146", "a147", "a148", "a149", "a150", "a151", "a152", "a153", "a154", "a155", "a156", "a157", "a158", "a159", "a160", "a161", "a162", "a163", "a164", "a165", "a166", "a167", "a168", "a169", "a170", "a171", "a172", "a173", "a174", "a175", "a176", "a177", "a178", "a179", "a180", "a181", "a182", "a183", "a184", "a185", "a186", "a187", "a188", "a189", "a190", "a191", "a192", "a193", "a194", "a195", "a196", "a197", "a198", "a199", "a200", "a201", "a202", "a203", "a204", "a205", "a206", "a207", "a208", "a209", "a210", "a211", "a212", "a213", "a214", "a215", "a216", "a217", "a218", "a219", "a220", "a221", "a222", "a223", "a224", "a225", "a226", "a227", "a228", "a229", "a230", "a231", "a232", "a233", "a234", "a235", "a236", "a237", "a238", "a239", "a240", "a241", "a242", "a243", "a244", "a245", "a246", "a247", "a248", "a249", "a250", "a251", "a252", "a253", "a254", "a255", "s36", "s37", "s38", "s39", "s40", "s41", "s42", "s43", "s44", "s45", "s46", "s47", "s48", "s49", "s50", "s51", "s52", "s53", "s54", "s55", "s56", "s57", "s58", "s59", "s60", "s61", "s62", "s63", "s64", "s65", "s66", "s67", "s68", "s69", "s70", "s71", "s72", "s73", "s74", "s75", "s76", "s77", "s78", "s79", "s80", "s81", "s82", "s83", "s84", "s85", "s86", "s87", "s88", "s89", "s90", "s91", "s92", "s93", "s94", "s95", "s96", "s97", "s98", "s99", "s100", "s101", "vcc", "scc", "m0", "memory");
}

__global__ __launch_bounds__(256) void reduce_parts(const char* part, char* out) {
  const int t = blockIdx.x * 256 + threadIdx.x;
  const int grp = t >> 13, rem = t & 8191;
  const int lane = rem & 63, hf = (rem >> 6) & 1, qb = (rem >> 7) & 1, nb = (rem >> 8) & 1, C = (rem >> 9) & 3, w = rem >> 11;
  const int q = 128 * (grp & 1) + 64 * (w & 1) + 32 * qb + (lane & 31);
  if (q >= 240) return;
  const char* src = part + (size_t)grp * 8 * 131072 + (size_t)rem * 16;
  u32x4 v[8];
#pragma unroll
  for (int i = 0; i < 8; ++i) v[i] = *(const u32x4*)(src + (size_t)i * 131072);
  float acc[8];
  for (int e = 0; e < 8; ++e) acc[e] = 0.f;
#pragma unroll
  for (int i = 0; i < 8; ++i)
    for (int e = 0; e < 4; ++e) { acc[2 * e] += __uint_as_float(v[i][e] << 16); acc[2 * e + 1] += __uint_as_float(v[i][e] & 0xffff0000u); }
  u32x4 o; for (int e = 0; e < 4; ++e) o[e] = cvtpk(acc[2 * e], acc[2 * e + 1]);
  const int br = grp >> 4, b = (grp >> 1) & 7;
  const int n = 256 * (w >> 1) + 64 * C + 32 * nb + 16 * hf + 8 * (lane >> 5);
  *(u32x4*)(out + ((size_t)(b * 240 + q) * 1024 + br * 512 + n) * 2) = o;
}

extern "C" void kernel_launch(void* const* d_in, const int* in_sizes, int n_in, void* d_out, int out_size, void* d_ws, size_t ws_size, hipStream_t stream) {
  Ptrs P;
  for (int i = 0; i < 2; ++i) {
    P.x[i] = (const char*)d_in[5 * i + 0]; P.k[i] = (const char*)d_in[5 * i + 1]; P.v[i] = (const char*)d_in[5 * i + 2];
    P.wq[i] = (const char*)d_in[5 * i + 3]; P.wo[i] = (const char*)d_in[5 * i + 4];
  }
  hipFuncSetAttribute((const void*)fused_attn, hipFuncAttributeMaxDynamicSharedMemorySize, 163840);
  hipLaunchKernelGGL(fused_attn, dim3(256), dim3(256), 163840, stream, P, (char*)d_ws);
  hipLaunchKernelGGL(reduce_parts, dim3(1024), dim3(256), 0, stream, (const char*)d_ws, (char*)d_out);
}
```
